```python
import torch

def _softmax(x, axis=-1):
    return torch.softmax(x, dim=axis)

def _gelu(x):
    return torch.nn.functional.gelu(x, approximate='tanh')

def _silu(x):
    return torch.nn.functional.silu(x)

def _sigmoid(x):
    return torch.sigmoid(x)

def _layernorm(x, axis=-1, eps=1e-05):
    m = x.mean(dim=axis, keepdim=True)
    v = x.var(dim=axis, keepdim=True, unbiased=False)
    return (x - m) / torch.sqrt(v + eps)

def _scatter_add(tgt, idx, src, axis=0):
    return tgt.clone().index_add_(axis, idx, src)

def _segment_sum(src, seg, n):
    out = torch.zeros((n,) + src.shape[1:], dtype=src.dtype)
    return out.index_add_(0, seg, src)

def _pad(x, pw):
    flat = tuple((v for pair in reversed(pw) for v in pair))
    return torch.nn.functional.pad(x, flat)
kernel_name = 'gated_mix__sliding_block__memory_aug_recurrent_mem_tr__prompt'

def setup_inputs():

    _w = lambda a: (lambda t: t.to(torch.bfloat16) if t.is_floating_point() else t)(torch.as_tensor(a))

    def _A_setup():
        B, S, D = (1, 2048, 448)
        return {'x': _w((lambda *s: torch.randn(*s, dtype=torch.bfloat16) * 0.05)(B, S, D)), 'wqkv': _w((lambda *s: torch.randn(*s, dtype=torch.bfloat16) * 1.6)(D, 3 * D)), 'wo': _w((lambda *s: torch.randn(*s, dtype=torch.bfloat16) * 0.05)(D, D))}
    _A_in = _A_setup()

    def _B_setup():
        B, S, Dm = (1, 2048, 448)
        H, Dh = (16, 256)
        M, seg = (8, 32)
        seg = min(seg, S)
        S = S // seg * seg
        return {'x': _w((lambda *s: torch.randn(*s, dtype=torch.bfloat16) * 0.05)(B, S, Dm)), 'mem0': _w((lambda *s: torch.randn(*s, dtype=torch.bfloat16) * 1.6)(B, M, Dm)), 'Wq': _w((lambda *s: torch.randn(*s, dtype=torch.bfloat16) * 0.8)(Dm, H, Dh) * Dm ** (-0.5)), 'Wk': _w((lambda *s: torch.randn(*s, dtype=torch.bfloat16) * 0.8)(Dm, H, Dh) * Dm ** (-0.5)), 'Wv': _w((lambda *s: torch.randn(*s, dtype=torch.bfloat16) * 1.6)(Dm, H, Dh) * Dm ** (-0.5)), 'Wo': _w((lambda *s: torch.randn(*s, dtype=torch.bfloat16) * 1.6)(H, Dh, Dm) * (H * Dh) ** (-0.5)), 'seg_len': seg}
    _B_in = _B_setup()
    return {'A_x': _A_in['x'], 'A_wqkv': _A_in['wqkv'], 'A_wo': _A_in['wo'], 'B_x': _B_in['x'], 'B_mem0': _B_in['mem0'], 'B_Wq': _B_in['Wq'], 'B_Wk': _B_in['Wk'], 'B_Wv': _B_in['Wv'], 'B_Wo': _B_in['Wo'], 'B_seg_len': _B_in['seg_len'], 'gate_w': _w((lambda *s: torch.randn(*s, dtype=torch.bfloat16) * 0.05)(448) * 16.0)}

def reference(A_x, A_wqkv, A_wo, B_x, B_mem0, B_Wq, B_Wk, B_Wv, B_Wo, B_seg_len, gate_w):

    def _A_ref(x, wqkv, wo):
        B, S, D = x.shape
        H = 16
        Dh = D // H
        W = 256
        qkv = (lambda x, *d: x.permute(*d))((x @ wqkv).reshape(B, S, 3, H, Dh), 2, 0, 3, 1, 4)
        q, k, v = (qkv[0], qkv[1], qkv[2])
        scores = torch.einsum('bhqd,bhkd->bhqk', q, k) * Dh ** (-0.5)
        idx = torch.arange(S)
        mask = (idx[:, None] - idx[None, :] >= 0) & (idx[:, None] - idx[None, :] < W)
        scores = torch.where(mask[None, None], scores, -1000000000.0)
        p = _softmax(scores, axis=-1)
        o = torch.einsum('bhqk,bhkd->bhqd', p, v)
        return (lambda x, *d: x.permute(*d))(o, 0, 2, 1, 3).reshape(B, S, D) @ wo

    def _B_ref(x, mem0, Wq, Wk, Wv, Wo, seg_len):
        B, S, Dm = x.shape
        H, Dh = (Wq.shape[1], Wq.shape[2])
        M = mem0.shape[1]
        scale = Dh ** (-0.5)
        n_seg = S // seg_len
        L = M + seg_len
        idx = torch.arange(L)
        mask = (idx[:, None] < M) | (idx[:, None] >= idx[None, :])
        mem = mem0
        outs = []
        for s in range(n_seg):
            xs = x[:, s * seg_len:(s + 1) * seg_len, :].clone()
            h = torch.concatenate([mem, xs], axis=1)
            q = torch.einsum('btd,dhe->bhte', h, Wq)
            k = torch.einsum('btd,dhe->bhte', h, Wk)
            v = torch.einsum('btd,dhe->bhte', h, Wv)
            scores = torch.einsum('bhqe,bhke->bhqk', q, k) * scale
            scores = torch.where(mask[None, None], scores, -1000000000.0)
            p = _softmax(scores, axis=-1)
            o = torch.einsum('bhqk,bhke->bhqe', p, v)
            o = torch.einsum('bhte,hed->btd', o, Wo)
            mem = o[:, :M, :].clone()
            outs.append(o[:, M:, :])
        return torch.concatenate(outs, axis=1)
    x = A_x
    g = _sigmoid((x * gate_w).sum(-1, keepdims=True))
    a = _A_ref(x, A_wqkv, A_wo)
    b = _B_ref(x, B_mem0, B_Wq, B_Wk, B_Wv, B_Wo, B_seg_len)
    return g * a + (1 - g) * b
```

```cpp
#include <hip/hip_runtime.h>
#include <hip/hip_bf16.h>
#include <stdint.h>

#define GAS __attribute__((address_space(1)))
#define LAS __attribute__((address_space(3)))
typedef __bf16 bf16x2 __attribute__((ext_vector_type(2)));
typedef __bf16 bf16x4 __attribute__((ext_vector_type(4)));
typedef __bf16 bf16x8 __attribute__((ext_vector_type(8)));
typedef __bf16 bf16x16 __attribute__((ext_vector_type(16)));
typedef short s16x4 __attribute__((ext_vector_type(4)));
typedef float f32x2 __attribute__((ext_vector_type(2)));
typedef float f32x4 __attribute__((ext_vector_type(4)));
typedef unsigned v2u __attribute__((ext_vector_type(2)));
typedef unsigned v4u __attribute__((ext_vector_type(4)));
typedef unsigned v8u __attribute__((ext_vector_type(8)));
typedef unsigned short u16;

constexpr int S_ = 2048, D_ = 448, H_ = 16, NSEG = 64;
constexpr float LOG2E = 1.4426950408889634f;

constexpr size_t SZ_W = (size_t)16 * 28 * 14 * 1024;
constexpr size_t OFF_HOP = 0;
constexpr size_t M_STRIDE = 8192, S_STRIDE = 8192, P_STRIDE = 16384;
constexpr size_t OFF_M = OFF_HOP;
constexpr size_t OFF_S = OFF_M + 64 * M_STRIDE;
constexpr size_t OFF_P = OFF_S + 64 * S_STRIDE;
constexpr size_t HOP_BYTES = 64 * (M_STRIDE + S_STRIDE + P_STRIDE);
constexpr size_t OFF_CTL = OFF_HOP + HOP_BYTES;
constexpr size_t OFF_TW = OFF_CTL + 65536;
constexpr size_t OFF_YW = OFF_TW + SZ_W;
constexpr size_t OFF_GW = OFF_YW + SZ_W;
constexpr size_t OFF_QW = OFF_GW + SZ_W;
constexpr size_t OFF_VW = OFF_QW + (size_t)32 * 28 * 1024;
constexpr size_t OFF_WOF = OFF_VW + (size_t)16 * 28 * 1024;
constexpr size_t OFF_XP = OFF_WOF + (size_t)28 * 16 * 1024;
constexpr size_t OFF_GATE = OFF_XP + (size_t)128 * 14 * 1024;
constexpr size_t SEGB = (size_t)448 * 1024;
constexpr size_t OFF_GF = OFF_GATE + 8192;
constexpr size_t OFF_UF = OFF_GF + 64 * SEGB;
constexpr size_t OFF_QF = OFF_UF + 64 * SEGB;
constexpr size_t OFF_KF = OFF_QF + (size_t)2048 * 1024;
constexpr size_t OFF_VF = OFF_KF + (size_t)2048 * 1024;
constexpr size_t OFF_OF = OFF_VF + (size_t)2048 * 1024;
constexpr size_t OFF_GM = OFF_OF + (size_t)2048 * 1024;
constexpr size_t OFF_YM = OFF_GM + (size_t)64 * 8 * 14 * 1024;
constexpr size_t WS_END = OFF_YM + (size_t)64 * 28 * 8 * 512;

struct Params {
    const u16* x; const u16* wqkv; const u16* wo; const u16* mem0;
    const u16* Wq; const u16* Wk; const u16* Wv; const u16* Wo; const u16* gate_w;
    u16* out; unsigned char* ws;
};

__device__ __forceinline__ unsigned pack2(float a, float b) {
    return __builtin_bit_cast(unsigned, __builtin_convertvector(f32x2{a, b}, bf16x2));
}
__device__ __forceinline__ float bf2f(u16 v) { return __builtin_bit_cast(float, (unsigned)v << 16); }
__device__ __forceinline__ f32x4 mfma32(v4u a, v4u b, f32x4 c) {
    return __builtin_amdgcn_mfma_f32_16x16x32_bf16(__builtin_bit_cast(bf16x8, a), __builtin_bit_cast(bf16x8, b), c, 0, 0, 0);
}
__device__ __forceinline__ f32x4 mfma16(v2u a, v2u b, f32x4 c) {
    return __builtin_amdgcn_mfma_f32_16x16x16bf16_1k(__builtin_bit_cast(s16x4, a), __builtin_bit_cast(s16x4, b), c, 0, 0, 0);
}
__device__ __forceinline__ f32x4 smfmac(v4u a, v8u b, f32x4 c, int idx) {
    return __builtin_amdgcn_smfmac_f32_16x16x64_bf16(__builtin_bit_cast(bf16x8, a), __builtin_bit_cast(bf16x16, b), c, idx, 0, 0);
}
__device__ __forceinline__ v4u ldg16(const void* p) { return *(const GAS v4u*)p; }
__device__ __forceinline__ v2u ldg8(const void* p) { return *(const GAS v2u*)p; }
__device__ __forceinline__ void stg16(void* p, v4u v) { *(GAS v4u*)p = v; }
__device__ __forceinline__ void stg8(void* p, v2u v) { *(GAS v2u*)p = v; }
__device__ __forceinline__ v2u tr_read(const LAS void* p) {
    return __builtin_bit_cast(v2u, __builtin_amdgcn_ds_read_tr16_b64_v4i16((LAS s16x4*)p));
}
__device__ __forceinline__ float ror8(float v) {
    return __builtin_bit_cast(float, __builtin_amdgcn_update_dpp(0, __builtin_bit_cast(int, v), 0x128, 0xf, 0xf, false));
}
__device__ __forceinline__ unsigned fbits(float v) { return __builtin_bit_cast(unsigned, v); }
__device__ __forceinline__ float bitsf(unsigned v) { return __builtin_bit_cast(float, v); }
__device__ __forceinline__ float xg_max(float v) {
    const v2u a = __builtin_amdgcn_permlane16_swap(fbits(v), fbits(v), false, false);
    v = fmaxf(bitsf(a.x), bitsf(a.y));
    const v2u b = __builtin_amdgcn_permlane32_swap(fbits(v), fbits(v), false, false);
    return fmaxf(bitsf(b.x), bitsf(b.y));
}
__device__ __forceinline__ float xg_sum(float v) {
    const v2u a = __builtin_amdgcn_permlane16_swap(fbits(v), fbits(v), false, false);
    v = bitsf(a.x) + bitsf(a.y);
    const v2u b = __builtin_amdgcn_permlane32_swap(fbits(v), fbits(v), false, false);
    return bitsf(b.x) + bitsf(b.y);
}
__device__ __forceinline__ float fold32(float a, float b) {
    const v2u r = __builtin_amdgcn_permlane32_swap(fbits(a), fbits(b), false, false);
    return bitsf(r.x) + bitsf(r.y);
}
__device__ __forceinline__ float ex2(float v) { return __builtin_amdgcn_exp2f(v); }
__device__ __forceinline__ unsigned sane(unsigned v) { return v < 0xFFFFFFFEu ? v : 0xFFFFFFFEu; }

__global__ void __launch_bounds__(256) k_prep_w(Params p) {
    __shared__ __attribute__((aligned(16))) unsigned char lds[4 * 8192];
    const int b = blockIdx.x;
    const int h = (b & 7) + 8 * ((b >> 3) & 1);
    const int rest = b >> 4;
    const int fam = rest / 7, u7 = rest % 7;
    const int l = threadIdx.x & 63, w = __builtin_amdgcn_readfirstlane(threadIdx.x >> 6);
    const int n = l & 15, g = l >> 4;
    const u16* srcA = fam == 0 ? p.Wq : (fam == 1 ? p.Wk : p.Wv);
    v4u a[4][8];
#pragma unroll
    for (int zz = 0; zz < 4; ++zz)
#pragma unroll
        for (int es = 0; es < 8; ++es)
            a[zz][es] = ldg16(srcA + ((size_t)(64 * u7 + 16 * zz + n) * 16 + h) * 256 + 32 * es + 8 * g);
    const float scale = fam == 2 ? 1.0f : LOG2E / 16.0f;
    for (int ci = 0; ci < 7; ++ci) {
        const int ct = 7 * w + ci;
        v4u bq[8];
        if (fam == 2) {
            LAS unsigned char* L = (LAS unsigned char*)lds + w * 8192;
            v4u t[8];
#pragma unroll
            for (int i = 0; i < 8; ++i) t[i] = ldg16(p.Wo + ((size_t)h * 256 + 32 * i + (l >> 1)) * 448 + 16 * ct + 8 * (l & 1));
#pragma unroll
            for (int i = 0; i < 8; ++i) *(LAS v4u*)(L + i * 1024 + l * 16) = t[i];
            __builtin_amdgcn_fence(__ATOMIC_RELEASE, "wavefront");
            __builtin_amdgcn_wave_barrier();
#pragma unroll
            for (int es = 0; es < 8; ++es) {
                const v2u lo = tr_read(L + es * 1024 + (8 * g + (n >> 2)) * 32 + (l & 3) * 8);
                const v2u hi = tr_read(L + es * 1024 + (8 * g + 4 + (n >> 2)) * 32 + (l & 3) * 8);
                bq[es] = v4u{lo.x, lo.y, hi.x, hi.y};
            }
            __builtin_amdgcn_fence(__ATOMIC_RELEASE, "wavefront");
            __builtin_amdgcn_wave_barrier();
        } else {
            int c;
            if (fam == 0) c = 64 * (ct >> 2) + 16 * ((ct >> 1) & 1) + 2 * (ct & 1) + 4 * (n >> 2) + 32 * ((n >> 1) & 1) + (n & 1);
            else c = 32 * (ct >> 1) + 8 * (n >> 2) + 4 * (ct & 1) + (n & 3);
            const u16* srcB = fam == 0 ? p.Wk : p.Wq;
#pragma unroll
            for (int es = 0; es < 8; ++es) bq[es] = ldg16(srcB + ((size_t)c * 16 + h) * 256 + 32 * es + 8 * g);
        }
        f32x4 acc[4];
#pragma unroll
        for (int zz = 0; zz < 4; ++zz) {
            acc[zz] = f32x4{0.f, 0.f, 0.f, 0.f};
#pragma unroll
            for (int es = 0; es < 8; ++es) acc[zz] = mfma32(a[zz][es], bq[es], acc[zz]);
        }
        size_t base;
        if (fam == 0) base = OFF_TW + ((size_t)(h * 28 + ct) * 7 + u7) * 2048;
        else if (fam == 1) base = OFF_GW + ((size_t)(h * 28 + ct) * 7 + u7) * 2048;
        else base = OFF_YW + ((size_t)(ct * 16 + h) * 7 + u7) * 2048;
#pragma unroll
        for (int z = 0; z < 2; ++z) {
            const f32x4 lo = acc[z] * scale, hi = acc[z + 2] * scale;
            stg16(p.ws + base + z * 1024 + l * 16, v4u{pack2(lo[0], lo[1]), pack2(hi[0], hi[1]), pack2(lo[2], lo[3]), pack2(hi[2], hi[3])});
        }
    }
}

__device__ __forceinline__ int porder(int f, int g, int e) {
    return 64 * (f >> 1) + 16 * (f & 1) + 4 * g + 32 * ((e >> 1) & 1) + 2 * (e >> 2) + (e & 1);
}
constexpr int NU_XP = 128 * 14 * 64, NU_QW = 32 * 28 * 64, NU_VW = 16 * 28 * 64, NU_WOF = 28 * 16 * 64, NU_HOP = (int)(HOP_BYTES / 16);
constexpr int NU_MISC = NU_XP + NU_QW + NU_VW + NU_WOF + NU_HOP;
__global__ void __launch_bounds__(256) k_prep_misc(Params p) {
    int i = blockIdx.x * 256 + threadIdx.x;
    if (blockIdx.x >= NU_MISC / 256) {
        const int row = (blockIdx.x - NU_MISC / 256) * 4 + (threadIdx.x >> 6), l = threadIdx.x & 63;
        float s = 0.f;
        if (l < 56) {
            const v4u xv = ldg16(p.x + (size_t)row * 448 + l * 8), gv = ldg16(p.gate_w + l * 8);
#pragma unroll
            for (int j = 0; j < 4; ++j) {
                s += bitsf(xv[j] << 16) * bitsf(gv[j] << 16);
                s += bitsf(xv[j] & 0xFFFF0000u) * bitsf(gv[j] & 0xFFFF0000u);
            }
        }
#pragma unroll
        for (int o = 32; o >= 1; o >>= 1) s += __shfl_xor(s, o);
        if (l == 0) ((float*)(p.ws + OFF_GATE))[row] = 1.0f / (1.0f + __expf(-s));
        return;
    }
    const int l = i & 63, n = l & 15, g = l >> 4;
    if (i < NU_XP) {
        const int fr = i >> 6, rt = fr / 14, f = fr % 14;
        const u16* row = p.x + (size_t)(rt * 16 + n) * 448 + 64 * (f >> 1) + 16 * (f & 1) + 4 * g;
        const v2u lo = ldg8(row), hi = ldg8(row + 32);
        stg16(p.ws + OFF_XP + (size_t)i * 16, v4u{lo.x, hi.x, lo.y, hi.y});
        return;
    }
    i -= NU_XP;
    if (i < NU_QW + NU_VW) {
        const bool isv = i >= NU_QW;
        const int ii = isv ? i - NU_QW : i;
        const int fr = ii >> 6, unit = fr / 28, t = (fr % 28) / 14, f = fr % 14;
        int d, col;
        if (!isv) { d = 8 * (n >> 2) + 4 * t + (n & 3); col = (unit >> 4) * 448 + (unit & 15) * 28 + d; }
        else { d = 16 * t + n; col = 896 + unit * 28 + d; }
        unsigned o[4] = {0u, 0u, 0u, 0u};
        if (d < 28) {
#pragma unroll
            for (int e = 0; e < 8; ++e) o[e >> 1] |= (unsigned)p.wqkv[(size_t)porder(f, g, e) * 1344 + col] << (16 * (e & 1));
        }
        stg16(p.ws + OFF_QW + (size_t)i * 16, v4u{o[0], o[1], o[2], o[3]});
        return;
    }
    i -= NU_QW + NU_VW;
    if (i < NU_WOF) {
        const int fr = i >> 6, v = fr >> 4, h = fr & 15;
        unsigned o[4] = {0u, 0u, 0u, 0u};
#pragma unroll
        for (int e = 0; e < 8; ++e) {
            const int d = (e < 4) ? 4 * g + e : 16 + 4 * g + e - 4;
            if (d < 28) o[e >> 1] |= (unsigned)p.wo[(size_t)(h * 28 + d) * 448 + 16 * v + n] << (16 * (e & 1));
        }
        stg16(p.ws + OFF_WOF + (size_t)i * 16, v4u{o[0], o[1], o[2], o[3]});
        return;
    }
    i -= NU_WOF;
    v4u val = v4u{0xFFFFFFFFu, 0xFFFFFFFFu, 0xFFFFFFFFu, 0xFFFFFFFFu};
    if (i < 448) {
        const v4u m = ldg16(p.mem0 + (size_t)(i & 7) * 448 + (i >> 3) * 8);
        val = v4u{sane(m.x), sane(m.y), sane(m.z), sane(m.w)};
    }
    stg16(p.ws + OFF_HOP + (size_t)i * 16, val);
}

template <bool MEMROWS>
__global__ void __launch_bounds__(512, 2) k_gemm(Params p) {
    const int l = threadIdx.x & 63, w = __builtin_amdgcn_readfirstlane(threadIdx.x >> 6);
    const int n = l & 15, g = l >> 4;
    const int unit = blockIdx.x * 2 + (w >> 2);
    const unsigned char* wsrc;
    if (unit < 224) wsrc = p.ws + OFF_GW + (size_t)unit * 28 * 1024;
    else if (unit < 448) wsrc = p.ws + OFF_YW + (size_t)(unit - 224) * 28 * 1024;
    else wsrc = p.ws + OFF_QW + (size_t)(unit - 448) * 28 * 1024;
    v4u wt[2][14];
#pragma unroll
    for (int t = 0; t < 2; ++t)
#pragma unroll
        for (int f = 0; f < 14; ++f) wt[t][f] = ldg16(wsrc + (t * 14 + f) * 1024 + l * 16);
    const bool gtype = unit < 224 || (unit >= 448 && unit < 480);
    const float oscale = (unit >= 448 && unit < 464) ? LOG2E * 0.18898223650461362f : 1.0f;
    constexpr int NRT = MEMROWS ? 32 : 128;
    for (int rt = (w & 3); rt < NRT; rt += 4) {
        v4u xf[14];
        if (!MEMROWS) {
#pragma unroll
            for (int f = 0; f < 14; ++f) xf[f] = ldg16(p.ws + OFF_XP + ((size_t)rt * 14 + f) * 1024 + l * 16);
        } else {
            const unsigned char* mb = p.ws + OFF_M + (size_t)(2 * rt + (n >> 3)) * M_STRIDE + (n & 7) * 16 + (g >> 1) * 128 + (g & 1) * 8;
#pragma unroll
            for (int f = 0; f < 14; ++f) {
                const v2u lo = ldg8(mb + (f >> 1) * 1024 + (f & 1) * 256), hi = ldg8(mb + (f >> 1) * 1024 + (f & 1) * 256 + 512);
                xf[f] = v4u{lo.x, hi.x, lo.y, hi.y};
            }
        }
        f32x4 a0 = {0.f, 0.f, 0.f, 0.f}, a1 = a0;
        if (gtype) {
#pragma unroll
            for (int f = 0; f < 14; ++f) { a0 = mfma32(wt[0][f], xf[f], a0); a1 = mfma32(wt[1][f], xf[f], a1); }
        } else {
#pragma unroll
            for (int f = 0; f < 14; ++f) { a0 = mfma32(xf[f], wt[0][f], a0); a1 = mfma32(xf[f], wt[1][f], a1); }
        }
        a0 *= oscale; a1 *= oscale;
        const v4u o = v4u{pack2(a0[0], a0[1]), pack2(a0[2], a0[3]), pack2(a1[0], a1[1]), pack2(a1[2], a1[3])};
        if (!MEMROWS) {
            const int seg = rt >> 1, tau = rt & 1;
            if (unit < 224) { const int h = unit / 14, ks = unit % 14; stg16(p.ws + OFF_GF + seg * SEGB + (size_t)((h * 2 + tau) * 14 + ks) * 1024 + l * 16, o); }
            else if (unit < 448) stg16(p.ws + OFF_UF + seg * SEGB + (size_t)((unit - 224) * 2 + tau) * 1024 + l * 16, o);
            else if (unit < 480) stg16(p.ws + OFF_QF + (size_t)((unit - 448) >> 4) * 2048 * 1024 + (size_t)(rt * 16 + (unit & 15)) * 1024 + l * 16, o);
            else {
                unsigned char* d = p.ws + OFF_VF + (size_t)((seg * 16 + (unit - 480)) * 2) * 1024 + l * 16 + tau * 8;
                stg8(d, v2u{o.x, o.y}); stg8(d + 1024, v2u{o.z, o.w});
            }
        } else {
            if (unit < 224) {
                const int h = unit / 14, ks = unit % 14, seg = 2 * rt + (n >> 3);
                stg16(p.ws + OFF_GM + (size_t)((seg * 8 + (h >> 1)) * 14 + ks) * 1024 + (((h & 1) * 8 + (n & 7)) + 16 * g) * 16, o);
            } else {
                const int seg = 2 * rt + (g >> 1);
                unsigned char* d = p.ws + OFF_YM + (size_t)(seg * 224 + (unit - 224)) * 512 + (n + 16 * (g & 1)) * 8;
                stg8(d, v2u{o.x, o.y}); stg8(d + 256, v2u{o.z, o.w});
            }
        }
    }
}

constexpr int YREG = 6;
constexpr int L_YW = 0, L_MST = 8 * (14 - YREG) * 2048, L_TIM = L_MST + 7168, L_RED = L_TIM + 7168, L_REC_END = L_RED + 4096;
constexpr unsigned SPIN_CAP = 1u << 20;

__device__ __forceinline__ bool fresh(v4u x) {
    const unsigned m = max(max(x.x, x.y), max(x.z, x.w));
    return __all(m != 0xFFFFFFFFu);
}
#define BAR() do { asm volatile("s_waitcnt lgkmcnt(0)" ::: "memory"); __builtin_amdgcn_s_barrier(); asm volatile("" ::: "memory"); } while (0)

template <bool SPECIAL, bool VAL>
__device__ __forceinline__ void rec_wave(const Params& p, LAS unsigned char* L, const int u, const int w) {
    const int l = threadIdx.x & 63;
    const int n = l & 15, g = l >> 4, i8 = l & 7, hh = (l >> 3) & 1;
    const int half = u & 1;
    const __amdgpu_buffer_rsrc_t rs = __builtin_amdgcn_make_buffer_rsrc(p.ws, 0, (int)WS_END, 0x00020000);
    const int idx = hh ? 0xEEEE : 0x4444;

    v8u tw[SPECIAL ? 1 : 14];
    v4u gx[SPECIAL ? 14 : 1];
    v8u yw[YREG];
    if constexpr (!SPECIAL) {
        const unsigned char* src = p.ws + OFF_TW + ((size_t)(u * 14 + 2 * w) * 7) * 2048 + l * 16;
#pragma unroll
        for (int f = 0; f < 14; ++f) {
            const v4u a = ldg16(src + f * 2048), b = ldg16(src + f * 2048 + 1024);
            tw[f] = v8u{a.x, a.y, a.z, a.w, b.x, b.y, b.z, b.w};
        }
    } else {
        const unsigned char* src = p.ws + OFF_GF + (size_t)u * 14 * 1024 + l * 16;
#pragma unroll
        for (int f = 0; f < 14; ++f) gx[f] = ldg16(src + f * 1024);
    }
    if constexpr (VAL) {
        const unsigned char* src = p.ws + OFF_YW + ((size_t)(u * 16 + 2 * w) * 7) * 2048 + l * 16;
#pragma unroll
        for (int f = 0; f < 14; ++f) {
            const v4u a = ldg16(src + f * 2048), b = ldg16(src + f * 2048 + 1024);
            if (f < YREG) yw[f] = v8u{a.x, a.y, a.z, a.w, b.x, b.y, b.z, b.w};
            else {
                *(LAS v4u*)(L + L_YW + (w * (14 - YREG) + (f - YREG)) * 2048 + l * 16) = a;
                *(LAS v4u*)(L + L_YW + (w * (14 - YREG) + (f - YREG)) * 2048 + 1024 + l * 16) = b;
            }
        }
    }
    const int moff = (int)OFF_M + w * 1024 + hh * 512 + g * 128 + i8 * 16;
    const int mst_off = (int)OFF_M + u * 256 + ((l >> 2) & 1) * 128 + (l >> 3) * 16 + (l & 3) * 4;
    const int soff = (int)OFF_S + ((2 * w + hh) * 2 + (g >> 1)) * 256 + i8 * 32 + (g & 1) * 16;
    const int sst_off = (int)OFF_S + u * 256 + i8 * 32 + (g & 1) * 16 + (g >> 1) * 8;
    const int poff = (int)OFF_P + (2 * w + hh) * 1024 + (g * 8 + i8) * 16;
    const int pst_off = (int)OFF_P + u * 512 + (g * 8 + i8) * 16;
    const int uoff = (u * 8 + w) * 2048 + l * 16;
    const int goff = u * 14 * 1024 + l * 16;
    unsigned spins = 0;

    for (int s = 0; s < NSEG - 1; ++s) {
        if constexpr (!SPECIAL) {
            v4u x;
            for (;;) {
                asm volatile("" ::: "memory");
                x = __builtin_amdgcn_raw_buffer_load_b128(rs, moff, s * (int)M_STRIDE, 16);
                if (fresh(x)) break;
                if (++spins > SPIN_CAP) return;
            }
            *(LAS v4u*)(L + L_MST + w * 1024 + l * 16) = x;
        }
        BAR();
        v4u mp[7];
#pragma unroll
        for (int f = 0; f < 7; ++f) mp[f] = *(const LAS v4u*)(L + L_MST + f * 1024 + l * 16);
        v4u uf0, uf1;
        if constexpr (VAL) {
            uf0 = __builtin_amdgcn_raw_buffer_load_b128(rs, uoff, (int)(OFF_UF + s * SEGB), 0);
            uf1 = __builtin_amdgcn_raw_buffer_load_b128(rs, uoff + 1024, (int)(OFF_UF + s * SEGB), 0);
        }
        if constexpr (!SPECIAL) {
            f32x4 a0 = {0.f, 0.f, 0.f, 0.f}, a1 = a0;
#pragma unroll
            for (int f = 0; f < 7; ++f) { a0 = smfmac(mp[f], tw[f], a0, idx); a1 = smfmac(mp[f], tw[7 + f], a1, idx); }
            *(LAS v2u*)(L + L_TIM + (2 * w) * 512 + n * 32 + g * 8) = v2u{pack2(a0[0], a0[1]), pack2(a0[2], a0[3])};
            *(LAS v2u*)(L + L_TIM + (2 * w + 1) * 512 + n * 32 + g * 8) = v2u{pack2(a1[0], a1[1]), pack2(a1[2], a1[3])};
            BAR();
        } else {
            f32x4 ae = {0.f, 0.f, 0.f, 0.f}, ao = ae;
#pragma unroll
            for (int f = 0; f < 7; ++f) {
                ae = mfma32(gx[2 * f], mp[f], ae);
                ao = mfma32(gx[2 * f + 1], mp[f], ao);
            }
            float sx[4];
#pragma unroll
            for (int r = 0; r < 4; ++r) sx[r] = ae[r] + ror8(ao[r]);
            const float mx = xg_max(fmaxf(fmaxf(sx[0], sx[1]), fmaxf(sx[2], sx[3])));
#pragma unroll
            for (int r = 0; r < 4; ++r) sx[r] = ex2(sx[r] - mx);
            const float lx = xg_sum((sx[0] + sx[1]) + (sx[2] + sx[3]));
            if (hh == 0)
                __builtin_amdgcn_raw_buffer_store_b128(v4u{sane(pack2(sx[0], sx[1])), sane(pack2(sx[2], sx[3])), sane(fbits(mx)), sane(fbits(lx))},
                                                       rs, pst_off, s * (int)P_STRIDE, 16);
#pragma unroll
            for (int f = 0; f < 14; ++f) gx[f] = __builtin_amdgcn_raw_buffer_load_b128(rs, goff + f * 1024, (int)(OFF_GF + (s + 1) * SEGB), 0);
            BAR();
            v2u t[14];
#pragma unroll
            for (int k = 0; k < 14; ++k) t[k] = tr_read(L + L_TIM + k * 512 + l * 8);
            f32x4 sa = {0.f, 0.f, 0.f, 0.f};
            if (half == 0) {
#pragma unroll
                for (int b = 0; b < 3; ++b)
                    sa = smfmac(mp[b], v8u{t[4 * b].x, t[4 * b].y, t[4 * b + 1].x, t[4 * b + 1].y, t[4 * b + 2].x, t[4 * b + 2].y, t[4 * b + 3].x, t[4 * b + 3].y}, sa, idx);
                sa = smfmac(mp[3], v8u{t[12].x, t[12].y, t[13].x, t[13].y, 0u, 0u, 0u, 0u}, sa, idx);
            } else {
                sa = smfmac(mp[3], v8u{0u, 0u, 0u, 0u, t[0].x, t[0].y, t[1].x, t[1].y}, sa, idx);
#pragma unroll
                for (int b = 0; b < 3; ++b)
                    sa = smfmac(mp[4 + b], v8u{t[4 * b + 2].x, t[4 * b + 2].y, t[4 * b + 3].x, t[4 * b + 3].y, t[4 * b + 4].x, t[4 * b + 4].y, t[4 * b + 5].x, t[4 * b + 5].y}, sa, idx);
            }
            float f0 = fold32(sa[0], sa[2]), f1 = fold32(sa[1], sa[3]);
            f0 += ror8(f0); f1 += ror8(f1);
            if (hh == 0) __builtin_amdgcn_raw_buffer_store_b64(v2u{sane(fbits(f0)), sane(fbits(f1))}, rs, sst_off, s * (int)S_STRIDE, 16);
        }
        if constexpr (VAL) {
            f32x4 y0 = {0.f, 0.f, 0.f, 0.f}, y1 = y0;
#pragma unroll
            for (int f = 0; f < 14; ++f) {
                v8u wv;
                if (f < YREG) wv = yw[f];
                else {
                    const v4u a = *(const LAS v4u*)(L + L_YW + (w * (14 - YREG) + (f - YREG)) * 2048 + l * 16);
                    const v4u b = *(const LAS v4u*)(L + L_YW + (w * (14 - YREG) + (f - YREG)) * 2048 + 1024 + l * 16);
                    wv = v8u{a.x, a.y, a.z, a.w, b.x, b.y, b.z, b.w};
                }
                if (f < 7) y0 = smfmac(mp[f], wv, y0, idx); else y1 = smfmac(mp[f - 7], wv, y1, idx);
            }
            const v2u yop = v2u{pack2(fold32(y0[0], y0[2]), fold32(y0[1], y0[3])), pack2(fold32(y1[0], y1[2]), fold32(y1[1], y1[3]))};
            v4u p0, p1;
            for (;;) {
                asm volatile("" ::: "memory");
                p0 = __builtin_amdgcn_raw_buffer_load_b128(rs, poff, s * (int)P_STRIDE, 16);
                p1 = __builtin_amdgcn_raw_buffer_load_b128(rs, poff + 512, s * (int)P_STRIDE, 16);
                if (fresh(p0) && fresh(p1)) break;
                if (++spins > SPIN_CAP) return;
            }
            const f32x4 z4 = {0.f, 0.f, 0.f, 0.f};
            const f32x4 o0 = mfma32(uf0, hh ? v4u{0u, 0u, p0.x, p0.y} : v4u{p0.x, p0.y, 0u, 0u}, z4);
            const f32x4 o1 = mfma32(uf1, hh ? v4u{0u, 0u, p1.x, p1.y} : v4u{p1.x, p1.y, 0u, 0u}, z4);
            const float mx = fmaxf(bitsf(p0.z), bitsf(p1.z));
            const float g0 = ex2(bitsf(p0.z) - mx), g1 = ex2(bitsf(p1.z) - mx);
            const f32x4 ox = o0 * g0 + o1 * g1;
            const float lx = g0 * bitsf(p0.w) + g1 * bitsf(p1.w);
            v4u sv;
            for (;;) {
                asm volatile("" ::: "memory");
                sv = __builtin_amdgcn_raw_buffer_load_b128(rs, soff, s * (int)S_STRIDE, 16);
                if (fresh(sv)) break;
                if (++spins > SPIN_CAP) return;
            }
            const float t0 = fold32(bitsf(sv.x), bitsf(sv.z)), t1 = fold32(bitsf(sv.y), bitsf(sv.w));
            const float m = xg_max(fmaxf(fmaxf(t0, t1), mx));
            const unsigned pk = pack2(ex2(t0 - m), ex2(t1 - m));
            const float beta = ex2(mx - m);
            const v2u bop = hh ? v2u{0u, pk} : v2u{pk, 0u};
            const f32x4 ls = mfma16(v2u{0x3F803F80u, 0x3F803F80u}, bop, f32x4{beta * lx, 0.f, 0.f, 0.f});
            f32x4 d = mfma16(yop, bop, ox * beta);
            const float r = __builtin_amdgcn_rcpf(ls[0]);
            d *= r;
#pragma unroll
            for (int k = 0; k < 4; ++k) d[k] += ror8(d[k]);
            if (hh == 0) *(LAS f32x4*)(L + L_RED + w * 512 + i8 * 64 + g * 16) = d;
            BAR();
            if (!SPECIAL && w == 0) {
                f32x2 q[8];
#pragma unroll
                for (int k = 0; k < 8; ++k) q[k] = *(const LAS f32x2*)(L + L_RED + k * 512 + l * 8);
                const f32x2 tot = ((q[0] + q[1]) + (q[2] + q[3])) + ((q[4] + q[5]) + (q[6] + q[7]));
                __builtin_amdgcn_raw_buffer_store_b32(sane(pack2(tot[0], tot[1])), rs, mst_off, (s + 1) * (int)M_STRIDE, 16);
            }
        } else {
            BAR();
        }
    }
}

__global__ void __launch_bounds__(512, 2) k_rec(Params p) {
    extern __shared__ __attribute__((aligned(16))) unsigned char lds_[];
    if ((blockIdx.x & 7) != 0) return;
    LAS unsigned char* L = (LAS unsigned char*)lds_;
    const int u = blockIdx.x >> 3;
    const int w = __builtin_amdgcn_readfirstlane(threadIdx.x >> 6);
    if (u < 28) { if (w < 7) rec_wave<false, true>(p, L, u, w); else rec_wave<true, true>(p, L, u, w); }
    else { if (w < 7) rec_wave<false, false>(p, L, u, w); else rec_wave<true, false>(p, L, u, w); }
}

__global__ void __launch_bounds__(256) k_attn_a(Params p) {
    const int l = threadIdx.x & 63, w = __builtin_amdgcn_readfirstlane(threadIdx.x >> 6);
    const int n = l & 15, g = l >> 4;
    const int item = blockIdx.x * 4 + w, rt = item >> 4, h = item & 15;
    const int s = rt >> 1, q = rt * 16 + n;
    const v4u qf = ldg16(p.ws + OFF_QF + (size_t)(rt * 16 + h) * 1024 + l * 16);
    f32x4 sc[18];
    const f32x4 z4 = {0.f, 0.f, 0.f, 0.f};
    const float NEG = -3.0e38f;
#pragma unroll
    for (int blk = 0; blk < 9; ++blk) {
        const int b = s - 8 + blk;
        if (b >= 0) {
#pragma unroll
            for (int t = 0; t < 2; ++t) {
                const v4u kf = ldg16(p.ws + OFF_KF + (size_t)((2 * b + t) * 16 + h) * 1024 + l * 16);
                f32x4 a = mfma32(kf, qf, z4);
                if (blk == 0 || blk == 8) {
#pragma unroll
                    for (int r = 0; r < 4; ++r) {
                        const int dq = q - (32 * b + 16 * t + 4 * g + r);
                        a[r] = (dq >= 0 && dq < 256) ? a[r] : NEG;
                    }
                }
                sc[2 * blk + t] = a;
            }
        } else {
            sc[2 * blk] = f32x4{NEG, NEG, NEG, NEG}; sc[2 * blk + 1] = sc[2 * blk];
        }
    }
    float m = NEG;
#pragma unroll
    for (int k = 0; k < 18; ++k) m = fmaxf(m, fmaxf(fmaxf(sc[k][0], sc[k][1]), fmaxf(sc[k][2], sc[k][3])));
    m = xg_max(m);
    float sum = 0.f;
    f32x4 o0 = z4, o1 = z4;
#pragma unroll
    for (int blk = 0; blk < 9; ++blk) {
        const int b = s - 8 + blk;
        if (b >= 0) {
            float e[8];
#pragma unroll
            for (int r = 0; r < 4; ++r) { e[r] = ex2(sc[2 * blk][r] - m); e[4 + r] = ex2(sc[2 * blk + 1][r] - m); }
            sum += ((e[0] + e[1]) + (e[2] + e[3])) + ((e[4] + e[5]) + (e[6] + e[7]));
            const v4u pb = v4u{pack2(e[0], e[1]), pack2(e[2], e[3]), pack2(e[4], e[5]), pack2(e[6], e[7])};
            const unsigned char* vp = p.ws + OFF_VF + (size_t)((b * 16 + h) * 2) * 1024 + l * 16;
            o0 = mfma32(ldg16(vp), pb, o0);
            o1 = mfma32(ldg16(vp + 1024), pb, o1);
        }
    }
    sum = xg_sum(sum);
    const float f = ((const float*)(p.ws + OFF_GATE))[q] / sum;
    o0 *= f; o1 *= f;
    stg16(p.ws + OFF_OF + (size_t)(rt * 16 + h) * 1024 + l * 16, v4u{pack2(o0[0], o0[1]), pack2(o0[2], o0[3]), pack2(o1[0], o1[1]), pack2(o1[2], o1[3])});
}

__global__ void __launch_bounds__(512, 2) k_x2(Params p) {
    __shared__ __attribute__((aligned(16))) unsigned char lds_[8 * 4096];
    LAS unsigned char* L = (LAS unsigned char*)lds_;
    const int s = blockIdx.x;
    const int l = threadIdx.x & 63, w = __builtin_amdgcn_readfirstlane(threadIdx.x >> 6);
    const int n = l & 15, g = l >> 4;
    const f32x4 z4 = {0.f, 0.f, 0.f, 0.f};
    const float NEG = -3.0e38f;
    {
        f32x4 sxx[2][3], sxm[2];
#pragma unroll
        for (int a = 0; a < 2; ++a) { sxm[a] = z4; sxx[a][0] = z4; sxx[a][1] = z4; sxx[a][2] = z4; }
        const unsigned char* gp = p.ws + OFF_GF + s * SEGB + (size_t)(2 * w) * 28 * 1024 + l * 16;
        const unsigned char* gm = p.ws + OFF_GM + (size_t)((s * 8 + w) * 14) * 1024 + l * 16;
        const u16* xr = p.x + (size_t)(32 * s + n) * 448 + 8 * g;
#pragma unroll 2
        for (int ks = 0; ks < 14; ++ks) {
            const v4u x0 = ldg16(xr + 32 * ks), x1 = ldg16(xr + 16 * 448 + 32 * ks);
            const v4u m = ldg16(gm + ks * 1024);
            sxm[0] = mfma32(m, x0, sxm[0]); sxm[1] = mfma32(m, x1, sxm[1]);
#pragma unroll
            for (int a = 0; a < 2; ++a) {
                const v4u k0 = ldg16(gp + (a * 28 + ks) * 1024), k1 = ldg16(gp + (a * 28 + 14 + ks) * 1024);
                sxx[a][0] = mfma32(k0, x0, sxx[a][0]);
                sxx[a][1] = mfma32(k0, x1, sxx[a][1]);
                sxx[a][2] = mfma32(k1, x1, sxx[a][2]);
            }
        }
        unsigned px[3][4]; unsigned pm[2][2];
#pragma unroll
        for (int qt = 0; qt < 2; ++qt) {
            const float gate = ((const float*)(p.ws + OFF_GATE))[32 * s + 16 * qt + n];
            float em[4] = {0.f, 0.f, 0.f, 0.f};
#pragma unroll
            for (int a = 0; a < 2; ++a) {
                f32x4 dg = sxx[a][qt == 0 ? 0 : 2];
#pragma unroll
                for (int r = 0; r < 4; ++r) dg[r] = (4 * g + r <= n) ? dg[r] : NEG;
                const bool mine = (g >> 1) == a;
                float m = fmaxf(fmaxf(dg[0], dg[1]), fmaxf(dg[2], dg[3]));
                if (qt == 1) m = fmaxf(m, fmaxf(fmaxf(sxx[a][1][0], sxx[a][1][1]), fmaxf(sxx[a][1][2], sxx[a][1][3])));
                const float mm = fmaxf(fmaxf(sxm[qt][0], sxm[qt][1]), fmaxf(sxm[qt][2], sxm[qt][3]));
                m = xg_max(mine ? fmaxf(m, mm) : m);
                float ed[4], ef[4], e2[4];
                float sum = 0.f;
#pragma unroll
                for (int r = 0; r < 4; ++r) {
                    ed[r] = ex2(dg[r] - m); sum += ed[r];
                    if (qt == 1) { ef[r] = ex2(sxx[a][1][r] - m); sum += ef[r]; }
                    e2[r] = mine ? ex2(sxm[qt][r] - m) : 0.f; sum += e2[r];
                }
                sum = xg_sum(sum);
                const float f = (1.0f - gate) / sum;
                const int di = qt == 0 ? 0 : 2;
                px[di][2 * a] = pack2(ed[0] * f, ed[1] * f); px[di][2 * a + 1] = pack2(ed[2] * f, ed[3] * f);
                if (qt == 1) { px[1][2 * a] = pack2(ef[0] * f, ef[1] * f); px[1][2 * a + 1] = pack2(ef[2] * f, ef[3] * f); }
#pragma unroll
                for (int r = 0; r < 4; ++r) em[r] += e2[r] * f;
            }
            pm[qt][0] = pack2(em[0], em[1]); pm[qt][1] = pack2(em[2], em[3]);
        }
#pragma unroll
        for (int k = 0; k < 3; ++k) *(LAS v4u*)(L + w * 4096 + k * 1024 + l * 16) = v4u{px[k][0], px[k][1], px[k][2], px[k][3]};
        *(LAS v4u*)(L + w * 4096 + 3072 + l * 16) = v4u{pm[0][0], pm[0][1], pm[1][0], pm[1][1]};
    }
    __syncthreads();
    for (int v = w; v < 28; v += 8) {
        f32x4 o0 = z4, o1 = z4;
        const unsigned char* up = p.ws + OFF_UF + s * SEGB + (size_t)(v * 16) * 1024 + l * 16;
        const unsigned char* yp = p.ws + OFF_YM + (size_t)(s * 224 + v * 8) * 512 + l * 8;
        const unsigned char* wp = p.ws + OFF_WOF + (size_t)(v * 16) * 1024 + l * 16;
        const unsigned char* op = p.ws + OFF_OF + (size_t)(2 * s * 16) * 1024 + l * 16;
#pragma unroll 2
        for (int pr = 0; pr < 8; ++pr) {
            const v4u u0 = ldg16(up + (2 * pr) * 1024), u1 = ldg16(up + (2 * pr + 1) * 1024);
            const v2u y = ldg8(yp + pr * 512);
            const v4u pa = *(const LAS v4u*)(L + pr * 4096 + l * 16), pb = *(const LAS v4u*)(L + pr * 4096 + 1024 + l * 16),
                      pc = *(const LAS v4u*)(L + pr * 4096 + 2048 + l * 16), pmm = *(const LAS v4u*)(L + pr * 4096 + 3072 + l * 16);
            o0 = mfma32(u0, pa, o0);
            o1 = mfma32(u0, pb, o1);
            o1 = mfma32(u1, pc, o1);
            o0 = mfma16(y, v2u{pmm.x, pmm.y}, o0);
            o1 = mfma16(y, v2u{pmm.z, pmm.w}, o1);
#pragma unroll
            for (int a = 0; a < 2; ++a) {
                const v4u wf = ldg16(wp + (2 * pr + a) * 1024);
                o0 = mfma32(wf, ldg16(op + (2 * pr + a) * 1024), o0);
                o1 = mfma32(wf, ldg16(op + (16 + 2 * pr + a) * 1024), o1);
            }
        }
        stg8(p.out + (size_t)(32 * s + n) * 448 + 16 * v + 4 * g, v2u{pack2(o0[0], o0[1]), pack2(o0[2], o0[3])});
        stg8(p.out + (size_t)(32 * s + 16 + n) * 448 + 16 * v + 4 * g, v2u{pack2(o1[0], o1[1]), pack2(o1[2], o1[3])});
    }
}

extern "C" void kernel_launch(void* const* d_in, const int* in_sizes, int n_in, void* d_out, int out_size, void* d_ws, size_t ws_size,
                              hipStream_t stream) {
    Params p;
    p.x = (const u16*)d_in[0]; p.wqkv = (const u16*)d_in[1]; p.wo = (const u16*)d_in[2]; p.mem0 = (const u16*)d_in[4];
    p.Wq = (const u16*)d_in[5]; p.Wk = (const u16*)d_in[6]; p.Wv = (const u16*)d_in[7]; p.Wo = (const u16*)d_in[8];
    p.gate_w = (const u16*)d_in[10];
    p.out = (u16*)d_out; p.ws = (unsigned char*)d_ws;
    (void)hipFuncSetAttribute((const void*)k_rec, hipFuncAttributeMaxDynamicSharedMemorySize, L_REC_END);
    k_prep_w<<<336, 256, 0, stream>>>(p);
    k_prep_misc<<<NU_MISC / 256 + 512, 256, 0, stream>>>(p);
    k_gemm<false><<<248, 512, 0, stream>>>(p);
    k_rec<<<256, 512, L_REC_END, stream>>>(p);
    k_gemm<true><<<224, 512, 0, stream>>>(p);
    k_attn_a<<<512, 256, 0, stream>>>(p);
    k_x2<<<64, 512, 0, stream>>>(p);
}
```

```cpp
#include <hip/hip_runtime.h>
#include <hip/hip_bf16.h>
#include <stdint.h>

#define GAS __attribute__((address_space(1)))
#define LAS __attribute__((address_space(3)))
typedef __bf16 bf16x2 __attribute__((ext_vector_type(2)));
typedef __bf16 bf16x4 __attribute__((ext_vector_type(4)));
typedef __bf16 bf16x8 __attribute__((ext_vector_type(8)));
typedef __bf16 bf16x16 __attribute__((ext_vector_type(16)));
typedef short s16x4 __attribute__((ext_vector_type(4)));
typedef float f32x2 __attribute__((ext_vector_type(2)));
typedef float f32x4 __attribute__((ext_vector_type(4)));
typedef unsigned v2u __attribute__((ext_vector_type(2)));
typedef unsigned v4u __attribute__((ext_vector_type(4)));
typedef unsigned v8u __attribute__((ext_vector_type(8)));
typedef unsigned short u16;

constexpr int S_ = 2048, D_ = 448, H_ = 16, NSEG = 64;
constexpr float LOG2E = 1.4426950408889634f;

constexpr size_t SZ_W = (size_t)16 * 28 * 14 * 1024;
constexpr size_t OFF_HOP = 0;
constexpr int PAGE = 2 << 20, CH = 16384, NCHUNK = 5, STEPB = NCHUNK * CH, NPAGE = 3, SPP = 24;
constexpr int C_M = 0, C_S = CH, C_P = 2 * CH, C_M2 = 3 * CH, C_M3 = 4 * CH;
constexpr size_t HOP_BYTES = (size_t)NPAGE * PAGE;
constexpr size_t OFF_CTL = OFF_HOP + HOP_BYTES;
constexpr size_t CTL_BYTES = 16384;
constexpr int CT_PROG = (int)OFF_CTL + 256, CT_OFC = (int)OFF_CTL + 4096;
constexpr size_t OFF_TW = OFF_CTL + CTL_BYTES;
constexpr size_t OFF_YW = OFF_TW + SZ_W;
constexpr size_t OFF_GW = OFF_YW + SZ_W;
constexpr size_t OFF_QW = OFF_GW + SZ_W;
constexpr size_t OFF_VW = OFF_QW + (size_t)32 * 28 * 1024;
constexpr size_t OFF_WOF = OFF_VW + (size_t)16 * 28 * 1024;
constexpr size_t OFF_XP = OFF_WOF + (size_t)28 * 16 * 1024;
constexpr size_t OFF_GATE = OFF_XP + (size_t)128 * 14 * 1024;
constexpr size_t SEGB = (size_t)448 * 1024;
constexpr size_t OFF_GF = OFF_GATE + 8192;
constexpr size_t OFF_UF = OFF_GF + 64 * SEGB;
constexpr size_t OFF_QF = OFF_UF + 64 * SEGB;
constexpr size_t OFF_KF = OFF_QF + (size_t)2048 * 1024;
constexpr size_t OFF_VF = OFF_KF + (size_t)2048 * 1024;
constexpr size_t OFF_OF = OFF_VF + (size_t)2048 * 1024;
constexpr size_t OFF_SXM = OFF_OF + (size_t)2048 * 1024;
constexpr size_t OFF_YM = OFF_SXM + (size_t)64 * 32 * 1024;
constexpr size_t WS_END = OFF_YM + (size_t)64 * 224 * 512;

struct Params {
    const u16* x; const u16* wqkv; const u16* wo; const u16* mem0;
    const u16* Wq; const u16* Wk; const u16* Wv; const u16* Wo; const u16* gate_w;
    u16* out; unsigned char* ws;
};
__device__ __forceinline__ unsigned char* align_ws(unsigned char* w) {
    return w + ((0 - (size_t)w) & (size_t)(PAGE - 1));
}
__device__ __forceinline__ unsigned load_parmask(const unsigned char* ws) {
    const unsigned* c = (const unsigned*)(ws + OFF_CTL);
    unsigned m = 0;
#pragma unroll
    for (int k = 0; k < NPAGE; ++k) m |= (c[k] & 1u) << k;
    return __builtin_amdgcn_readfirstlane(m);
}
__device__ __forceinline__ int hop_off(int s, unsigned parmask) {
    const int pg = s / SPP, sl = s - pg * SPP;
    return pg * PAGE + sl * STEPB + (int)((parmask >> pg) & 1u) * 8192;
}

__device__ __forceinline__ unsigned pack2(float a, float b) {
    return __builtin_bit_cast(unsigned, __builtin_convertvector(f32x2{a, b}, bf16x2));
}
__device__ __forceinline__ float bf2f(u16 v) { return __builtin_bit_cast(float, (unsigned)v << 16); }
__device__ __forceinline__ f32x4 mfma32(v4u a, v4u b, f32x4 c) {
    return __builtin_amdgcn_mfma_f32_16x16x32_bf16(__builtin_bit_cast(bf16x8, a), __builtin_bit_cast(bf16x8, b), c, 0, 0, 0);
}
__device__ __forceinline__ f32x4 mfma16(v2u a, v2u b, f32x4 c) {
    return __builtin_amdgcn_mfma_f32_16x16x16bf16_1k(__builtin_bit_cast(s16x4, a), __builtin_bit_cast(s16x4, b), c, 0, 0, 0);
}
__device__ __forceinline__ f32x4 smfmac(v4u a, v8u b, f32x4 c, int idx) {
    return __builtin_amdgcn_smfmac_f32_16x16x64_bf16(__builtin_bit_cast(bf16x8, a), __builtin_bit_cast(bf16x16, b), c, idx, 0, 0);
}
__device__ __forceinline__ v4u ldg16(const void* p) { return *(const GAS v4u*)p; }
__device__ __forceinline__ v2u ldg8(const void* p) { return *(const GAS v2u*)p; }
__device__ __forceinline__ void stg16(void* p, v4u v) { *(GAS v4u*)p = v; }
__device__ __forceinline__ void stg8(void* p, v2u v) { *(GAS v2u*)p = v; }
__device__ __forceinline__ v2u tr_read(const LAS void* p) {
    return __builtin_bit_cast(v2u, __builtin_amdgcn_ds_read_tr16_b64_v4i16((LAS s16x4*)p));
}
__device__ __forceinline__ float ror8(float v) {
    return __builtin_bit_cast(float, __builtin_amdgcn_update_dpp(0, __builtin_bit_cast(int, v), 0x128, 0xf, 0xf, false));
}
__device__ __forceinline__ unsigned fbits(float v) { return __builtin_bit_cast(unsigned, v); }
__device__ __forceinline__ float bitsf(unsigned v) { return __builtin_bit_cast(float, v); }
__device__ __forceinline__ float xg_max(float v) {
    const v2u a = __builtin_amdgcn_permlane16_swap(fbits(v), fbits(v), false, false);
    v = fmaxf(bitsf(a.x), bitsf(a.y));
    const v2u b = __builtin_amdgcn_permlane32_swap(fbits(v), fbits(v), false, false);
    return fmaxf(bitsf(b.x), bitsf(b.y));
}
__device__ __forceinline__ float xg_sum(float v) {
    const v2u a = __builtin_amdgcn_permlane16_swap(fbits(v), fbits(v), false, false);
    v = bitsf(a.x) + bitsf(a.y);
    const v2u b = __builtin_amdgcn_permlane32_swap(fbits(v), fbits(v), false, false);
    return bitsf(b.x) + bitsf(b.y);
}
__device__ __forceinline__ float fold32(float a, float b) {
    const v2u r = __builtin_amdgcn_permlane32_swap(fbits(a), fbits(b), false, false);
    return bitsf(r.x) + bitsf(r.y);
}
__device__ __forceinline__ float ex2(float v) { return __builtin_amdgcn_exp2f(v); }
__device__ __forceinline__ unsigned sane(unsigned v) { return v < 0xFFFFFFFEu ? v : 0xFFFFFFFEu; }

__device__ __forceinline__ int porder(int f, int g, int e) {
    return 64 * (f >> 1) + 16 * (f & 1) + 4 * g + 32 * ((e >> 1) & 1) + 2 * (e >> 2) + (e & 1);
}
constexpr int NU_HOP = 64 * NCHUNK * 16;
__device__ __forceinline__ void misc_wave(const Params& p, const int mw, const int l) {
    const int n = l & 15, g = l >> 4;
    v2u xl[4], xh[4];
#pragma unroll
    for (int k = 0; k < 4; ++k) {
        const int i = mw * 4 + k, rt = i / 14, f = i % 14;
        const u16* row = p.x + (size_t)(rt * 16 + n) * 448 + 64 * (f >> 1) + 16 * (f & 1) + 4 * g;
        xl[k] = ldg8(row); xh[k] = ldg8(row + 32);
    }
    unsigned qv[3][8];
    bool qok[3];
#pragma unroll
    for (int k = 0; k < 3; ++k) {
        const int i = mw * 3 + k;
        const bool isv = i >= 896;
        const int fr = isv ? i - 896 : i;
        const int unit = fr / 28, t = (fr % 28) / 14, f = fr % 14;
        const int d = isv ? 16 * t + n : 8 * (n >> 2) + 4 * t + (n & 3);
        const int col = (isv ? 896 + unit * 28 : (unit >> 4) * 448 + (unit & 15) * 28) + (d < 28 ? d : 0);
        qok[k] = d < 28;
#pragma unroll
        for (int e = 0; e < 8; ++e) qv[k][e] = p.wqkv[(size_t)porder(f, g, e) * 1344 + col];
    }
    unsigned wv[8];
#pragma unroll
    for (int e = 0; e < 8; ++e) {
        const int d = (e < 4) ? 4 * g + e : 16 + 4 * g + e - 4;
        wv[e] = p.wo[(size_t)((mw & 15) * 28 + (d < 28 ? d : 0)) * 448 + 16 * (mw >> 4) + n];
        if (d >= 28) wv[e] = 0u;
    }
    v4u gxv[7], ggv[7];
    const int row = (mw & 255) * 8 + (l >> 3), c0 = (l & 7) * 8;
#pragma unroll
    for (int k = 0; k < 7; ++k) { gxv[k] = ldg16(p.x + (size_t)row * 448 + k * 64 + c0); ggv[k] = ldg16(p.gate_w + k * 64 + c0); }
    for (int i = mw; i < NU_HOP; i += 448) {
        const int st = i / (NCHUNK * 16), ck = (i >> 4) % NCHUNK, kb = i & 15;
        v4u val = v4u{0xFFFFFFFFu, 0xFFFFFFFFu, 0xFFFFFFFFu, 0xFFFFFFFFu};
        if (st == 0 && ck != 1 && ck != 2 && (kb & 7) < 7) {
            const int im = (kb & 7) * 64 + l;
            const v4u m = ldg16(p.mem0 + (size_t)(im & 7) * 448 + (im >> 3) * 8);
            val = v4u{sane(m.x), sane(m.y), sane(m.z), sane(m.w)};
        }
        stg16(p.ws + OFF_HOP + (size_t)hop_off(st, 0u) + ck * CH + kb * 1024 + l * 16, val);
    }
    for (int i = mw; i < (int)((WS_END - OFF_SXM) / 1024); i += 448)
        stg16(p.ws + OFF_SXM + (size_t)i * 1024 + l * 16, v4u{0xFFFFFFFFu, 0xFFFFFFFFu, 0xFFFFFFFFu, 0xFFFFFFFFu});
#pragma unroll
    for (int k = 0; k < 4; ++k) stg16(p.ws + OFF_XP + (size_t)(mw * 4 + k) * 1024 + l * 16, v4u{xl[k].x, xh[k].x, xl[k].y, xh[k].y});
#pragma unroll
    for (int k = 0; k < 3; ++k) {
        const v4u o = v4u{qv[k][0] | (qv[k][1] << 16), qv[k][2] | (qv[k][3] << 16), qv[k][4] | (qv[k][5] << 16), qv[k][6] | (qv[k][7] << 16)};
        stg16(p.ws + OFF_QW + (size_t)(mw * 3 + k) * 1024 + l * 16, qok[k] ? o : v4u{0u, 0u, 0u, 0u});
    }
    stg16(p.ws + OFF_WOF + (size_t)mw * 1024 + l * 16, v4u{wv[0] | (wv[1] << 16), wv[2] | (wv[3] << 16), wv[4] | (wv[5] << 16), wv[6] | (wv[7] << 16)});
    float s = 0.f;
#pragma unroll
    for (int k = 0; k < 7; ++k)
#pragma unroll
        for (int j = 0; j < 4; ++j) {
            s += bitsf(gxv[k][j] << 16) * bitsf(ggv[k][j] << 16);
            s += bitsf(gxv[k][j] & 0xFFFF0000u) * bitsf(ggv[k][j] & 0xFFFF0000u);
        }
    s += __shfl_xor(s, 1); s += __shfl_xor(s, 2); s += __shfl_xor(s, 4);
    if ((l & 7) == 0 && mw < 256) ((float*)(p.ws + OFF_GATE))[row] = 1.0f / (1.0f + __expf(-s));
}

constexpr int K1_RS = 528, K1_OS = 144, K1_IMG = 36864, K1_IS = 136, K1_LDS = K1_IMG + 7 * 64 * K1_IS;
__global__ void __launch_bounds__(512, 2) k_prep(Params p) {
    extern __shared__ __attribute__((aligned(16))) unsigned char lds_[];
    LAS unsigned char* L = (LAS unsigned char*)lds_;
    p.ws = align_ws(p.ws);
    const int b = blockIdx.x, tid = threadIdx.x;
    const int l = tid & 63, w = __builtin_amdgcn_readfirstlane(tid >> 6);
    const int n = l & 15, g = l >> 4;
#define PT(k) do {} while (0)
#define PT0(k) do {} while (0)
    PT0(0);
    if (b >= 224) {
        const int k = b - 224;
        if ((k & 7) == 0 && w == 0) {
            const int pg = k >> 3;
            if (pg < NPAGE && l == 0) {
                GAS unsigned* a = (GAS unsigned*)(p.ws + OFF_HOP + (size_t)pg * PAGE + 126 * CH);
                unsigned best[4] = {~0u, ~0u, ~0u, ~0u};
                for (int it = 0; it < 3; ++it) {
#pragma unroll
                    for (int q = 0; q < 4; ++q) {
                        const unsigned long long t0 = __builtin_amdgcn_s_memtime();
                        __hip_atomic_store(a + q * 1024, 0xFFFFFFFFu, __ATOMIC_RELAXED, __HIP_MEMORY_SCOPE_AGENT);
                        asm volatile("s_waitcnt vmcnt(0)" ::: "memory");
                        const unsigned dt = (unsigned)(__builtin_amdgcn_s_memtime() - t0);
                        best[q] = min(best[q], dt);
                    }
                }
                ((unsigned*)(p.ws + OFF_CTL))[pg] = (best[0] + best[1] > best[2] + best[3]) ? 1u : 0u;
            }
            PT(3);
            if (pg == 3) { for (int i = l; i < (int)(CTL_BYTES - 256) / 16; i += 64) stg16(p.ws + OFF_CTL + 256 + i * 16, v4u{0u, 0u, 0u, 0u}); }
            return;
        }
        if ((k & 7) == 0) return;
        const int kk = k - (k >> 3) - 1;
        misc_wave(p, kk * 8 + w, l);
        PT(3);
        return;
    }
    const int r = b >> 3, h = (b & 7) + 8 * (r & 1), q = r >> 1, mat = q / 7, blk = q % 7;
    v4u t[4];
    if (mat == 0) {
#pragma unroll
        for (int k = 0; k < 4; ++k) { const int u = tid + 512 * k; t[k] = ldg16(p.Wq + ((size_t)(64 * blk + (u >> 5)) * 16 + h) * 256 + (u & 31) * 8); }
    } else {
#pragma unroll
        for (int k = 0; k < 4; ++k) { const int u = tid + 512 * k; t[k] = ldg16(p.Wo + ((size_t)h * 256 + (u >> 3)) * 448 + 64 * blk + (u & 7) * 8); }
    }
    __builtin_amdgcn_sched_barrier(0);
    v4u cr[4][8];
    {
        const u16* src = (mat == 0 ? p.Wk : p.Wv) + (size_t)h * 256 + 8 * g;
        const int r0 = 64 * (w < 7 ? w : 0) + 4 * (n >> 2) + 32 * ((n >> 1) & 1) + (n & 1);
        if (w < 7) {
#pragma unroll
            for (int es = 0; es < 8; ++es)
#pragma unroll
                for (int tt = 0; tt < 4; ++tt) cr[tt][es] = ldg16(src + (size_t)(r0 + 16 * (tt >> 1) + 2 * (tt & 1)) * 4096 + 32 * es);
        }
    }
    __builtin_amdgcn_sched_barrier(0);
    {
        const int rs_ = mat == 0 ? K1_RS : K1_OS, sh = mat == 0 ? 5 : 3;
#pragma unroll
        for (int k = 0; k < 4; ++k) { const int u = tid + 512 * k; *(LAS v4u*)(L + (u >> sh) * rs_ + (u & ((1 << sh) - 1)) * 16) = t[k]; }
    }
    asm volatile("s_waitcnt lgkmcnt(0)" ::: "memory");
    __builtin_amdgcn_s_barrier();
    asm volatile("" ::: "memory");
    PT0(1);
    if (w == 7) {
        misc_wave(p, 224 + b, l);
        PT(3);
        return;
    }
    f32x4 acc[4][4];
#pragma unroll
    for (int x = 0; x < 4; ++x)
#pragma unroll
        for (int y = 0; y < 4; ++y) acc[x][y] = f32x4{0.f, 0.f, 0.f, 0.f};
    if (mat == 0) {
#pragma unroll
        for (int es = 0; es < 8; ++es) {
            v4u af[4];
#pragma unroll
            for (int x = 0; x < 4; ++x) af[x] = *(const LAS v4u*)(L + (16 * x + n) * K1_RS + es * 64 + g * 16);
#pragma unroll
            for (int x = 0; x < 4; ++x)
#pragma unroll
                for (int y = 0; y < 4; ++y) acc[x][y] = mfma32(af[x], cr[y][es], acc[x][y]);
        }
        __builtin_amdgcn_sched_barrier(0); PT0(2); __builtin_amdgcn_sched_barrier(0);
        constexpr float SC = LOG2E / 16.0f;
        LAS unsigned char* img = L + K1_IMG + w * 64 * K1_IS;
#pragma unroll
        for (int y = 0; y < 4; ++y) {
            v2u pk[4];
#pragma unroll
            for (int x = 0; x < 4; ++x) {
                pk[x] = v2u{pack2(acc[x][y][0] * SC, acc[x][y][1] * SC), pack2(acc[x][y][2] * SC, acc[x][y][3] * SC)};
                *(LAS v2u*)(img + (16 * y + n) * K1_IS + x * 32 + g * 8) = pk[x];
            }
            unsigned char* d = p.ws + OFF_TW + ((size_t)(h * 28 + 4 * w + y) * 7 + blk) * 2048 + l * 16;
            stg16(d, v4u{pk[0].x, pk[2].x, pk[0].y, pk[2].y});
            stg16(d + 1024, v4u{pk[1].x, pk[3].x, pk[1].y, pk[3].y});
        }
        asm volatile("" ::: "memory");
#pragma unroll
        for (int j = 0; j < 4; ++j) {
            unsigned char* d = p.ws + OFF_GW + ((size_t)(h * 28 + 4 * blk + j) * 7 + w) * 2048 + l * 16;
            const LAS unsigned char* rp = img + (4 * g + (n >> 2)) * K1_IS + (j >> 1) * 64 + (l & 3) * 16 + (j & 1) * 8;
#pragma unroll
            for (int a = 0; a < 2; ++a) {
                const v2u lo = tr_read(rp + (32 * a) * K1_IS), hi = tr_read(rp + (32 * a + 16) * K1_IS);
                stg16(d + a * 1024, v4u{lo.x, lo.y, hi.x, hi.y});
            }
        }
    } else {
#pragma unroll
        for (int es = 0; es < 8; ++es) {
            v4u bfr[4];
#pragma unroll
            for (int y = 0; y < 4; ++y) {
                const LAS unsigned char* rp = L + (32 * es + 8 * g + (n >> 2)) * K1_OS + y * 32 + (l & 3) * 8;
                const v2u lo = tr_read(rp), hi = tr_read(rp + 4 * K1_OS);
                bfr[y] = v4u{lo.x, lo.y, hi.x, hi.y};
            }
#pragma unroll
            for (int x = 0; x < 4; ++x)
#pragma unroll
                for (int y = 0; y < 4; ++y) acc[x][y] = mfma32(cr[x][es], bfr[y], acc[x][y]);
        }
        __builtin_amdgcn_sched_barrier(0); PT0(2); __builtin_amdgcn_sched_barrier(0);
#pragma unroll
        for (int y = 0; y < 4; ++y) {
            unsigned char* d = p.ws + OFF_YW + ((size_t)((4 * blk + y) * 16 + h) * 7 + w) * 2048 + l * 16;
#pragma unroll
            for (int a = 0; a < 2; ++a)
                stg16(d + a * 1024, v4u{pack2(acc[2 * a][y][0], acc[2 * a][y][1]), pack2(acc[2 * a][y][2], acc[2 * a][y][3]),
                                        pack2(acc[2 * a + 1][y][0], acc[2 * a + 1][y][1]), pack2(acc[2 * a + 1][y][2], acc[2 * a + 1][y][3])});
        }
    }
    PT(3);
}

constexpr int L_MST = 0, L_TIM = L_MST + 2 * 7168, L_RED = L_TIM + 8192, L_REC_END = 96 * 1024;
#define DLY_M 2300
#define DLY_W 150
#define SPOS 1
#define DLY_X (DLY_M - 330)
#define PPOS 7
#define DLY_V 500
#define DLY_P 0
#define DLY_S 0
template <int C> __device__ __forceinline__ void delay_cycles() {
    if constexpr (C >= 64) __builtin_amdgcn_s_sleep(C / 64 > 127 ? 127 : C / 64);
    if constexpr ((C & 63) >= 4) asm volatile("s_nop %0" :: "i"(((C & 63) >> 2) - 1));
}

__device__ __forceinline__ bool fresh(v4u x) {
    const unsigned m = max(max(x.x, x.y), max(x.z, x.w));
    return __all(m != 0xFFFFFFFFu);
}
#define BAR() do { asm volatile("s_waitcnt lgkmcnt(0)" ::: "memory"); __builtin_amdgcn_s_barrier(); asm volatile("" ::: "memory"); } while (0)
#define FENCE() do { asm volatile("" ::: "memory"); __builtin_amdgcn_sched_barrier(0); } while (0)

#define STAMP(k) do {} while (0)
#define PROF_DECL
#define PROF_ACC do {} while (0)
#define PROF_OUT(slot) do {} while (0)
typedef _Float16 f16x2 __attribute__((ext_vector_type(2)));
__device__ __forceinline__ unsigned packh(float a, float b) { return __builtin_bit_cast(unsigned, __builtin_amdgcn_cvt_pkrtz(a, b)); }
__device__ __forceinline__ float hlo(unsigned v) { const f16x2 h = __builtin_bit_cast(f16x2, v); return (float)h.x; }
__device__ __forceinline__ float hhi(unsigned v) { const f16x2 h = __builtin_bit_cast(f16x2, v); return (float)h.y; }
constexpr unsigned SPIN_CAP = 1u << 20;

__device__ __forceinline__ float vmax(float a, float b) { float r; asm("v_max_f32 %0, %1, %2" : "=v"(r) : "v"(a), "v"(b)); return r; }
__device__ __forceinline__ float xg_max_raw(float v) {
    const v2u a = __builtin_amdgcn_permlane16_swap(fbits(v), fbits(v), false, false);
    v = vmax(bitsf(a.x), bitsf(a.y));
    const v2u b = __builtin_amdgcn_permlane32_swap(fbits(v), fbits(v), false, false);
    return vmax(bitsf(b.x), bitsf(b.y));
}
__device__ __forceinline__ float addh_lo(unsigned a, unsigned b) { float r; asm("v_fma_mix_f32 %0, %1, 1.0, %2 op_sel_hi:[1,0,1]" : "=v"(r) : "v"(a), "v"(b)); return r; }
__device__ __forceinline__ float addh_hi(unsigned a, unsigned b) { float r; asm("v_fma_mix_f32 %0, %1, 1.0, %2 op_sel:[1,0,1] op_sel_hi:[1,0,1]" : "=v"(r) : "v"(a), "v"(b)); return r; }
__device__ __forceinline__ float subh_lo(unsigned a, float b) { float r; asm("v_fma_mix_f32 %0, %1, 1.0, -%2 op_sel_hi:[1,0,0]" : "=v"(r) : "v"(a), "v"(b)); return r; }
__device__ __forceinline__ float subh_hi(unsigned a, float b) { float r; asm("v_fma_mix_f32 %0, %1, 1.0, -%2 op_sel:[1,0,0] op_sel_hi:[1,0,0]" : "=v"(r) : "v"(a), "v"(b)); return r; }
__device__ __forceinline__ float add_ror8(float a) { float r; asm("s_nop 1\n\tv_add_f32_dpp %0, %1, %1 row_ror:8 row_mask:0xf bank_mask:0xf" : "=v"(r) : "v"(a)); return r; }
#define UNLIKELY(x) __builtin_expect(!!(x), 0)
#define CNT(x) do {} while (0)

template <int NL>
__device__ __forceinline__ int wait_ready(const __amdgpu_buffer_rsrc_t rs, const int first, const int need, unsigned& mspin) {
    const int l = threadIdx.x & 63;
    int ready;
    for (;;) {
        asm volatile("" ::: "memory");
        const int v = (int)__builtin_amdgcn_raw_buffer_load_b32(rs, CT_PROG + (first + (l < NL ? l : 0)) * 4, 0, 16);
        ready = 1 << 20;
#pragma unroll
        for (int k = 0; k < NL; ++k) ready = min(ready, 3 * __builtin_amdgcn_readlane(v, k) + (k % 3));
        if (ready > need || ++mspin >= SPIN_CAP) break;
        __builtin_amdgcn_s_sleep(2);
    }
    asm volatile("" ::: "memory");
    return ready;
}

template <bool SPECIAL>
__device__ __forceinline__ void score_wave(const Params& p, LAS unsigned char* L, const int u, const int w) {
    const int l = threadIdx.x & 63;
    const int n = l & 15, g = l >> 4, i8 = l & 7, hh = (l >> 3) & 1;
    const int half = u & 1;
    const __amdgpu_buffer_rsrc_t rs = __builtin_amdgcn_make_buffer_rsrc(p.ws, 0, (int)WS_END, 0x00020000);
    const int idx = hh ? 0xEEEE : 0x4444;
    const unsigned parmask = load_parmask(p.ws);
    v8u tw[SPECIAL ? 1 : 14];
    v4u gx[SPECIAL ? 14 : 1];
    if constexpr (!SPECIAL) {
        const unsigned char* src = p.ws + OFF_TW + ((size_t)(u * 14 + 2 * w) * 7) * 2048 + l * 16;
#pragma unroll
        for (int f = 0; f < 14; ++f) {
            const v4u a = ldg16(src + f * 2048), b = ldg16(src + f * 2048 + 1024);
            tw[f] = v8u{a.x, a.y, a.z, a.w, b.x, b.y, b.z, b.w};
        }
    } else {
        *(LAS v4u*)(L + L_TIM + (half ? 0 : 14 * 512) + l * 16) = v4u{0u, 0u, 0u, 0u};
        __builtin_amdgcn_s_setprio(2);
    }
    const int moff = C_M + (SPECIAL ? 0 : w * 1024) + hh * 512 + g * 128 + i8 * 16;
    const int sst_off = hh ? 0x7FFFFFF0 : C_S + u * 128 + g * 32 + i8 * 4;
    const int pst_off = hh ? 0x7FFFFFF0 : C_P + u * 256 + (g * 8 + i8) * 8;
    const int goff = u * 14 * 1024 + l * 16;
    unsigned spins = 0, mspin = 0;
    int ready = 0;
    if constexpr (SPECIAL) {
        ready = wait_ready<6>(rs, 6 * (u >> 1), 0, mspin);
#pragma unroll
        for (int f = 0; f < 14; ++f) gx[f] = __builtin_amdgcn_raw_buffer_load_b128(rs, goff + f * 1024, (int)OFF_GF, 16);
    }
    PROF_DECL
    for (int s = 0; s < NSEG - 1; ++s) {
        const int ho = hop_off(s, parmask);
        FENCE();
        STAMP(0);
        if constexpr (!SPECIAL) {
            if (s) delay_cycles<DLY_M>();
            FENCE();
            v4u x;
            do { asm volatile("" ::: "memory"); x = __builtin_amdgcn_raw_buffer_load_b128(rs, moff, ho, 16); CNT(1); } while (!fresh(x) && ++mspin < SPIN_CAP);
            STAMP(1);
            *(LAS v4u*)(L + L_MST + w * 1024 + l * 16) = x;
        }
        v4u mp[7];
        BAR();
        STAMP(2);
        if constexpr (!SPECIAL) {
#pragma unroll
            for (int f = 0; f < 7; ++f) mp[f] = *(const LAS v4u*)(L + L_MST + f * 1024 + l * 16);
            FENCE();
            f32x4 a0 = {0.f, 0.f, 0.f, 0.f}, a1 = a0;
#pragma unroll
            for (int f = 0; f < 7; ++f) { a0 = smfmac(mp[f], tw[f], a0, idx); a1 = smfmac(mp[f], tw[7 + f], a1, idx); }
            *(LAS v2u*)(L + L_TIM + (2 * w + 2 * half) * 512 + n * 32 + g * 8) = v2u{pack2(a0[0], a0[1]), pack2(a0[2], a0[3])};
            *(LAS v2u*)(L + L_TIM + (2 * w + 2 * half + 1) * 512 + n * 32 + g * 8) = v2u{pack2(a1[0], a1[1]), pack2(a1[2], a1[3])};
            STAMP(3);
            BAR();
            STAMP(4);
        } else {
#pragma unroll
            for (int f = 0; f < 7; ++f) mp[f] = *(const LAS v4u*)(L + L_MST + f * 1024 + l * 16);
            FENCE();
            f32x4 ae = {0.f, 0.f, 0.f, 0.f}, ao = ae;
#pragma unroll
            for (int f = 0; f < 7; ++f) {
                ae = mfma32(gx[2 * f], mp[f], ae);
                ao = mfma32(gx[2 * f + 1], mp[f], ao);
            }
            float sx[4];
#pragma unroll
            for (int r = 0; r < 4; ++r) sx[r] = ae[r] + ror8(ao[r]);
            __builtin_amdgcn_raw_buffer_store_b64(v2u{sane(packh(sx[0], sx[1])), sane(packh(sx[2], sx[3]))}, rs, pst_off, ho, 16);
            FENCE();
            STAMP(3);
            v4u m2[4];
#pragma unroll
            for (int b = 0; b < 4; ++b) m2[b] = *(const LAS v4u*)(L + L_MST + (3 * half + b) * 1024 + l * 16);
            f32x4 sa = {0.f, 0.f, 0.f, 0.f};
            asm volatile("" : "+v"(sa));
            BAR();
            STAMP(4);
            v8u tb[4];
#pragma unroll
            for (int b = 0; b < 4; ++b) {
                const v2u t0 = tr_read(L + L_TIM + (4 * b) * 512 + l * 8), t1 = tr_read(L + L_TIM + (4 * b + 1) * 512 + l * 8),
                          t2 = tr_read(L + L_TIM + (4 * b + 2) * 512 + l * 8), t3 = tr_read(L + L_TIM + (4 * b + 3) * 512 + l * 8);
                tb[b] = v8u{t0.x, t0.y, t1.x, t1.y, t2.x, t2.y, t3.x, t3.y};
            }
            FENCE();
#pragma unroll
            for (int b = 0; b < 4; ++b) sa = smfmac(m2[b], tb[b], sa, idx);
            const float f0 = add_ror8(fold32(sa[0], sa[2])), f1 = add_ror8(fold32(sa[1], sa[3]));
            __builtin_amdgcn_raw_buffer_store_b32(sane(packh(f0, f1)), rs, sst_off, ho, 16);
            FENCE();
            STAMP(5);
            const int go = (int)(OFF_GF + (s + 1) * SEGB);
            if (UNLIKELY(ready <= s + 1)) ready = wait_ready<6>(rs, 6 * (u >> 1), s + 1, mspin);
#pragma unroll
            for (int f = 0; f < 14; ++f) gx[f] = __builtin_amdgcn_raw_buffer_load_b128(rs, goff + f * 1024, go, 16);
            FENCE();
        }
        PROF_ACC;
    }
    PROF_OUT(u * 8 + w);
}

template <bool LAST>
__device__ __forceinline__ void value_wave(const Params& p, LAS unsigned char* L, const int v, const int w) {
    const int l = threadIdx.x & 63;
    const int g = l >> 4, i8 = l & 7, hh = (l >> 3) & 1;
    const __amdgpu_buffer_rsrc_t rs = __builtin_amdgcn_make_buffer_rsrc(p.ws, 0, (int)WS_END, 0x00020000);
    const int idx = hh ? 0xEEEE : 0x4444;
    const unsigned parmask = load_parmask(p.ws);
    v8u yw[14];
    {
        const unsigned char* src = p.ws + OFF_YW + (size_t)(v * 8 + w) * 14 * 2048 + l * 16;
#pragma unroll
        for (int f = 0; f < 14; ++f) {
            const v4u a = ldg16(src + f * 2048), b = ldg16(src + f * 2048 + 1024);
            yw[f] = v8u{a.x, a.y, a.z, a.w, b.x, b.y, b.z, b.w};
        }
    }
    if (LAST) __builtin_amdgcn_s_setprio(2);
    const int moff = C_M2 + w * 1024 + hh * 512 + g * 128 + i8 * 16;
    const int mst_off = C_M + v * 256 + ((l >> 2) & 1) * 128 + (l >> 3) * 16 + (l & 3) * 4;
    const int soff = C_S + (2 * w + hh) * 256 + g * 32 + i8 * 4;
    const int poff = C_P + (2 * w + hh) * 512 + (g * 8 + i8) * 8;
    const int yst_off = (int)OFF_YM + (v * 8 + w) * 512 + l * 8;
    const int uoff = (v * 8 + w) * 2048 + l * 16;
    const unsigned mlo = hh ? 0u : 0xFFFFFFFFu, mhi = ~mlo;
    unsigned spins = 0, mspin = 0;
    PROF_DECL
    int ho = hop_off(0, parmask);
    int ready = wait_ready<3>(rs, 3 * (32 + v), 0, mspin);
    v4u ua = __builtin_amdgcn_raw_buffer_load_b128(rs, uoff, (int)OFF_UF, 16);
    v4u ub = ua;
    for (int s = 0; s < NSEG; ++s) {
        const int hn = hop_off(s + 1, parmask);
        const int uo = (int)(OFF_UF + (s + 1) * SEGB);
        if (UNLIKELY(ready <= s + 1 && s + 1 < NSEG)) ready = wait_ready<3>(rs, 3 * (32 + v), s + 1, mspin);
        FENCE();
        STAMP(0);
        if constexpr (!LAST) {
            delay_cycles<DLY_V>();
            FENCE();
            v4u x;
            do { asm volatile("" ::: "memory"); x = __builtin_amdgcn_raw_buffer_load_b128(rs, moff, ho, 16); CNT(1); } while (!fresh(x) && ++mspin < SPIN_CAP);
            STAMP(1);
            *(LAS v4u*)(L + L_MST + w * 1024 + l * 16) = x;
        }
        BAR();
        STAMP(2);
        v4u mp[7];
#pragma unroll
        for (int f = 0; f < 7; ++f) mp[f] = *(const LAS v4u*)(L + L_MST + f * 1024 + l * 16);
        ub = __builtin_amdgcn_raw_buffer_load_b128(rs, uoff + 1024, uo - (int)SEGB, 16);
        FENCE();
        f32x4 y0 = {0.f, 0.f, 0.f, 0.f}, y1 = y0;
        v2u p0, p1;
#pragma unroll
        for (int f = 0; f < 7; ++f) {
            if (f == PPOS) {
                FENCE();
                p0 = __builtin_amdgcn_raw_buffer_load_b64(rs, poff, ho, 16);
                p1 = __builtin_amdgcn_raw_buffer_load_b64(rs, poff + 256, ho, 16);
                FENCE();
            }
            y0 = smfmac(mp[f], yw[f], y0, idx); y1 = smfmac(mp[f], yw[7 + f], y1, idx);
        }
        const v2u yop = v2u{pack2(fold32(y0[0], y0[2]), fold32(y0[1], y0[3])), pack2(fold32(y1[0], y1[2]), fold32(y1[1], y1[3]))};
        FENCE();
        STAMP(3);
        if (s == NSEG - 1) { __builtin_amdgcn_raw_buffer_store_b64(v2u{sane(yop.x), sane(yop.y)}, rs, yst_off, s * 224 * 512, 16); break; }
        if (PPOS >= 7) {
            delay_cycles<DLY_P>();
            FENCE();
            p0 = __builtin_amdgcn_raw_buffer_load_b64(rs, poff, ho, 16);
            p1 = __builtin_amdgcn_raw_buffer_load_b64(rs, poff + 256, ho, 16);
            FENCE();
        }
        CNT(1 << 20);
        if (UNLIKELY(!fresh(v4u{p0.x, p0.y, p1.x, p1.y}))) {
            do {
                asm volatile("" ::: "memory");
                p0 = __builtin_amdgcn_raw_buffer_load_b64(rs, poff, ho, 16);
                p1 = __builtin_amdgcn_raw_buffer_load_b64(rs, poff + 256, ho, 16);
                CNT(1 << 20);
            } while (!fresh(v4u{p0.x, p0.y, p1.x, p1.y}) && ++mspin < SPIN_CAP);
        }
        STAMP(7);
        const f16x2 hm = __builtin_elementwise_max(__builtin_elementwise_max(__builtin_bit_cast(f16x2, p0.x), __builtin_bit_cast(f16x2, p0.y)),
                                                   __builtin_elementwise_max(__builtin_bit_cast(f16x2, p1.x), __builtin_bit_cast(f16x2, p1.y)));
        const float mx = xg_max_raw(vmax((float)hm.x, (float)hm.y));
        FENCE();
        STAMP(6);
        unsigned s0 = __builtin_amdgcn_raw_buffer_load_b32(rs, soff, ho, 16);
        unsigned s1 = __builtin_amdgcn_raw_buffer_load_b32(rs, soff + 128, ho, 16);
        FENCE();
        const unsigned e0 = pack2(ex2(subh_lo(p0.x, mx)), ex2(subh_hi(p0.x, mx))), e1 = pack2(ex2(subh_lo(p0.y, mx)), ex2(subh_hi(p0.y, mx))),
                       e2 = pack2(ex2(subh_lo(p1.x, mx)), ex2(subh_hi(p1.x, mx))), e3 = pack2(ex2(subh_lo(p1.y, mx)), ex2(subh_hi(p1.y, mx)));
        const f32x4 z4 = {0.f, 0.f, 0.f, 0.f};
        const v4u eop = v4u{e0, e1, e2, e3};
        const f32x4 oa = mfma32(ua, eop, z4);
        const f32x4 ob = mfma32(ub, eop, z4);
        const f32x4 lx4 = mfma32(v4u{0x3F803F80u, 0x3F803F80u, 0x3F803F80u, 0x3F803F80u}, eop, z4);
        f32x4 ox = hh ? ob : oa;
        float lx = lx4[0];
        asm volatile("" : "+v"(ox), "+v"(lx));
        FENCE();
        STAMP(8);
        while (!__all(max(s0, s1) != 0xFFFFFFFFu) && ++mspin < SPIN_CAP) {
            asm volatile("" ::: "memory");
            s0 = __builtin_amdgcn_raw_buffer_load_b32(rs, soff, ho, 16);
            s1 = __builtin_amdgcn_raw_buffer_load_b32(rs, soff + 128, ho, 16);
            CNT(1 << 10);
        }
        STAMP(9);
        FENCE();
        ua = __builtin_amdgcn_raw_buffer_load_b128(rs, uoff, uo, 16);
        FENCE();
        const float t0 = addh_lo(s0, s1), t1 = addh_hi(s0, s1);
        float m;
        asm("v_max3_f32 %0, %1, %2, %3" : "=v"(m) : "v"(t0), "v"(t1), "v"(mx));
        m = xg_max_raw(m);
        const unsigned pk = pack2(ex2(t0 - m), ex2(t1 - m));
        const float beta = ex2(mx - m);
        const v2u bop = v2u{pk & mlo, pk & mhi};
        const f32x4 ls = mfma16(v2u{0x3F803F80u, 0x3F803F80u}, bop, f32x4{beta * lx, 0.f, 0.f, 0.f});
        f32x4 d = mfma16(yop, bop, ox * beta);
        const float r = __builtin_amdgcn_rcpf(ls[0]);
        d *= r;
        asm("s_nop 1\n\tv_add_f32_dpp %0, %0, %0 row_ror:8 row_mask:0xf bank_mask:0xf\n\tv_add_f32_dpp %1, %1, %1 row_ror:8 row_mask:0xf bank_mask:0xf\n\t"
            "v_add_f32_dpp %2, %2, %2 row_ror:8 row_mask:0xf bank_mask:0xf\n\tv_add_f32_dpp %3, %3, %3 row_ror:8 row_mask:0xf bank_mask:0xf"
            : "+v"(d[0]), "+v"(d[1]), "+v"(d[2]), "+v"(d[3]));
        *(LAS f32x2*)(L + L_RED + w * 512 + i8 * 64 + g * 16 + hh * 8) = hh ? f32x2{d[2], d[3]} : f32x2{d[0], d[1]};
        STAMP(10);
        BAR();
        STAMP(11);
        if constexpr (!LAST) __builtin_amdgcn_raw_buffer_store_b64(v2u{sane(yop.x), sane(yop.y)}, rs, yst_off, s * 224 * 512, 16);
        if constexpr (LAST) {
            f32x2 q[8];
#pragma unroll
            for (int k = 0; k < 8; ++k) q[k] = *(const LAS f32x2*)(L + L_RED + k * 512 + l * 8);
            FENCE();
            const f32x2 tot = ((q[0] + q[1]) + (q[2] + q[3])) + ((q[4] + q[5]) + (q[6] + q[7]));
            const unsigned mv = sane(pack2(tot[0], tot[1]));
            __builtin_amdgcn_raw_buffer_store_b32(mv, rs, mst_off, hn, 16);
            __builtin_amdgcn_raw_buffer_store_b32(mv, rs, mst_off, hn + C_M2, 16);
            __builtin_amdgcn_raw_buffer_store_b32(mv, rs, mst_off, hn + C_M3, 16);
            FENCE();
            __builtin_amdgcn_raw_buffer_store_b64(v2u{sane(yop.x), sane(yop.y)}, rs, yst_off, s * 224 * 512, 16);
        }
        ho = hn;
        PROF_ACC;
    }
    PROF_OUT(256 + v * 8 + w);
}

__device__ __forceinline__ void glds16(const void* gsrc, unsigned lds_dst) {
    unsigned keep;
    asm volatile("s_mov_b32 %0, m0\n\ts_mov_b32 m0, %2\n\ts_nop 0\n\tglobal_load_lds_dwordx4 %1, off\n\ts_mov_b32 m0, %0"
                 : "=&s"(keep) : "v"(gsrc), "s"(lds_dst) : "memory");
}
__device__ __forceinline__ v4u ldc16(const __amdgpu_buffer_rsrc_t rs, int voff, int soff) { return __builtin_amdgcn_raw_buffer_load_b128(rs, voff, soff, 16); }
__device__ __forceinline__ v4u pack8(f32x4 a, f32x4 b) { return v4u{pack2(a[0], a[1]), pack2(a[2], a[3]), pack2(b[0], b[1]), pack2(b[2], b[3])}; }
constexpr int L_XB = 0, XBUF = 28672;

__device__ __forceinline__ void sweep_and_serve(const Params& p, LAS unsigned char* L, const __amdgpu_buffer_rsrc_t rs, const int grp, const int r, const int w) {
    const int l = threadIdx.x & 63, n = l & 15, g = l >> 4;
    const unsigned lbase = (unsigned)(size_t)L;
    const unsigned char* wsrc;
    int obase, ostride, osec;
    bool gtype;
    float oscale = 1.0f;
    const int ks = 7 * (grp & 1) + w;
    if (grp < 32) {
        gtype = true;
        if (w < 7) { wsrc = p.ws + OFF_GW + (size_t)((grp >> 1) * 14 + ks) * 28672; obase = (int)OFF_GF + ((grp >> 1) * 28 + ks) * 1024; ostride = (int)SEGB; osec = 14336; }
        else { wsrc = p.ws + OFF_QW + (size_t)grp * 28672; obase = (int)OFF_QF + (grp >> 4) * (2048 * 1024) + (grp & 15) * 1024; ostride = 32768; osec = 16384;
               if (grp < 16) oscale = LOG2E * 0.18898223650461362f; }
    } else {
        gtype = false; osec = 1024;
        if (grp < 60) { wsrc = p.ws + OFF_YW + (size_t)((grp - 32) * 8 + w) * 28672; obase = (int)OFF_UF + ((grp - 32) * 8 + w) * 2048; ostride = (int)SEGB; }
        else { wsrc = p.ws + OFF_QW + (size_t)(32 + (grp - 60) * 8 + w) * 28672; obase = (int)OFF_VF + ((grp - 60) * 8 + w) * 2048; ostride = 32768; }
    }
    const int npc = w < 4 ? 4 : 3, pc0 = w < 4 ? 4 * w : 16 + 3 * (w - 4);
    const unsigned char* xsrc = p.ws + OFF_XP + pc0 * 1024 + l * 16;
    for (int k = 0; k < npc; ++k) glds16(xsrc + (size_t)r * XBUF + k * 1024, lbase + L_XB + (pc0 + k) * 1024);
    v4u wt[2][14];
#pragma unroll
    for (int f = 0; f < 14; ++f) { wt[0][f] = ldg16(wsrc + f * 1024 + l * 16); wt[1][f] = ldg16(wsrc + (14 + f) * 1024 + l * 16); }
#pragma unroll
    for (int f = 0; f < 14; ++f) asm volatile("" : "+v"(wt[0][f]), "+v"(wt[1][f]));
    asm volatile("s_waitcnt vmcnt(0)" ::: "memory");
    BAR();
    const int nseg = (66 - r) / 3;
    for (int i = 0; i < nseg; ++i) {
        const int seg = r + 3 * i;
        if (i + 1 < nseg) for (int k = 0; k < npc; ++k) glds16(xsrc + (size_t)(seg + 3) * XBUF + k * 1024, lbase + L_XB + ((i + 1) & 1) * XBUF + (pc0 + k) * 1024);
        const LAS unsigned char* xb = L + L_XB + (i & 1) * XBUF + l * 16;
        f32x4 a[2][2];
#pragma unroll
        for (int tau = 0; tau < 2; ++tau) {
            f32x4 a0 = {0.f, 0.f, 0.f, 0.f}, a1 = a0;
            if (gtype) {
#pragma unroll
                for (int f = 0; f < 14; ++f) { const v4u xf = *(const LAS v4u*)(xb + (tau * 14 + f) * 1024); a0 = mfma32(wt[0][f], xf, a0); a1 = mfma32(wt[1][f], xf, a1); }
            } else {
#pragma unroll
                for (int f = 0; f < 14; ++f) { const v4u xf = *(const LAS v4u*)(xb + (tau * 14 + f) * 1024); a0 = mfma32(xf, wt[0][f], a0); a1 = mfma32(xf, wt[1][f], a1); }
            }
            a[tau][0] = a0 * oscale; a[tau][1] = a1 * oscale;
        }
        const v4u o0 = gtype ? pack8(a[0][0], a[0][1]) : pack8(a[0][0], a[1][0]);
        const v4u o1 = gtype ? pack8(a[1][0], a[1][1]) : pack8(a[0][1], a[1][1]);
        __builtin_amdgcn_raw_buffer_store_b128(o0, rs, l * 16, obase + seg * ostride, 16);
        __builtin_amdgcn_raw_buffer_store_b128(o1, rs, l * 16, obase + seg * ostride + osec, 16);
        const bool drain = (i == 0 || i + 1 == nseg);
        if (drain) asm volatile("s_waitcnt vmcnt(0)" ::: "memory"); else asm volatile("s_waitcnt vmcnt(2)" ::: "memory");
        BAR();
        if (threadIdx.x == 0) __builtin_amdgcn_raw_buffer_store_b32((unsigned)(drain ? i + 1 : i), rs, CT_PROG + (grp * 3 + r) * 4, 0, 16);
    }
    if (!(grp < 32 && r == 0)) return;
    const unsigned parmask = load_parmask(p.ws);
    unsigned mspin = 0;
    const int mrow = 4 * ((n >> 2) & 1) + 2 * (n >> 3) + (n & 1);
    const LAS unsigned char* mrd = L + (g >> 1) * 128 + mrow * 16 + (g & 1) * 8;
    for (int s = 0; s < NSEG; ++s) {
        v4u xa, xb2;
        if (w < 7) {
            const u16* xr = p.x + (size_t)(32 * s + n) * 448 + 32 * ks + 8 * g;
            xa = ldg16(xr); xb2 = ldg16(xr + 16 * 448);
            const int ho = hop_off(s, parmask) + C_M3;
            v4u x;
            for (;;) {
                asm volatile("" ::: "memory");
                x = ldc16(rs, w * 1024 + l * 16, ho);
                if (fresh(x) || ++mspin >= SPIN_CAP) break;
                __builtin_amdgcn_s_sleep(3);
            }
            *(LAS v4u*)(L + w * 1024 + l * 16) = x;
        }
        BAR();
        if (w < 7) {
            f32x4 a0 = {0.f, 0.f, 0.f, 0.f}, a1 = a0;
#pragma unroll
            for (int f = 0; f < 14; ++f) {
                const v2u lo = *(const LAS v2u*)(mrd + (f >> 1) * 1024 + (f & 1) * 256), hi = *(const LAS v2u*)(mrd + (f >> 1) * 1024 + (f & 1) * 256 + 512);
                const v4u mf = v4u{lo.x, hi.x, lo.y, hi.y};
                a0 = mfma32(wt[0][f], mf, a0); a1 = mfma32(wt[1][f], mf, a1);
            }
            const v4u gm = pack8(a0, a1);
            const f32x4 z4 = {0.f, 0.f, 0.f, 0.f};
            const f32x4 d0 = mfma32(gm, xa, z4), d1 = mfma32(gm, xb2, z4);
            *(LAS f32x4*)(L + 8192 + w * 1024 + l * 16) = f32x4{d0[0], d0[1], d1[0], d1[1]};
        }
        BAR();
        if (w == 7) {
            f32x4 q[7];
#pragma unroll
            for (int k = 0; k < 7; ++k) q[k] = *(const LAS f32x4*)(L + 8192 + k * 1024 + l * 16);
            const f32x4 t = ((q[0] + q[1]) + (q[2] + q[3])) + ((q[4] + q[5]) + q[6]);
            __builtin_amdgcn_raw_buffer_store_b128(v4u{sane(fbits(t[0])), sane(fbits(t[1])), sane(fbits(t[2])), sane(fbits(t[3]))}, rs, l * 16,
                                                   (int)OFF_SXM + (s * 32 + grp) * 1024, 16);
        }
    }
}

__device__ __forceinline__ void attn_item(const Params& p, const __amdgpu_buffer_rsrc_t rs, const int item) {
    const int l = threadIdx.x & 63, n = l & 15, g = l >> 4;
    const int rt = item >> 4, h = item & 15;
    const int s = rt >> 1, q = rt * 16 + n;
    const v4u qf = ldc16(rs, l * 16, (int)OFF_QF + (rt * 16 + h) * 1024);
    f32x4 sc[18];
    const f32x4 z4 = {0.f, 0.f, 0.f, 0.f};
    const float NEG = -3.0e38f;
    v4u kf[18];
#pragma unroll
    for (int k = 0; k < 18; ++k) { const int kt = max(2 * (s - 8) + k, 0); kf[k] = ldc16(rs, l * 16, (int)OFF_KF + (kt * 16 + h) * 1024); }
#pragma unroll
    for (int k = 0; k < 18; ++k) {
        const int kt = 2 * (s - 8) + k;
        f32x4 a = mfma32(kf[k], qf, z4);
        if (k < 2 || k >= 16) {
#pragma unroll
            for (int r = 0; r < 4; ++r) {
                const int dq = q - (16 * kt + 4 * g + r);
                a[r] = (dq >= 0 && dq < 256) ? a[r] : NEG;
            }
        }
        sc[k] = kt >= 0 ? a : f32x4{NEG, NEG, NEG, NEG};
    }
    FENCE();
    v4u vf[18];
#pragma unroll
    for (int k = 0; k < 18; ++k) { const int b = max(s - 8 + (k >> 1), 0); vf[k] = ldc16(rs, l * 16, (int)OFF_VF + ((b * 16 + h) * 2 + (k & 1)) * 1024); }
    float m = NEG;
#pragma unroll
    for (int k = 0; k < 18; ++k) m = fmaxf(m, fmaxf(fmaxf(sc[k][0], sc[k][1]), fmaxf(sc[k][2], sc[k][3])));
    m = xg_max(m);
    float sum = 0.f;
    f32x4 o0 = z4, o1 = z4;
#pragma unroll
    for (int blk = 0; blk < 9; ++blk) {
        f32x4 e0, e1;
#pragma unroll
        for (int r = 0; r < 4; ++r) { e0[r] = ex2(sc[2 * blk][r] - m); e1[r] = ex2(sc[2 * blk + 1][r] - m); }
        sum += ((e0[0] + e0[1]) + (e0[2] + e0[3])) + ((e1[0] + e1[1]) + (e1[2] + e1[3]));
        const v4u pb = pack8(e0, e1);
        o0 = mfma32(vf[2 * blk], pb, o0);
        o1 = mfma32(vf[2 * blk + 1], pb, o1);
    }
    sum = xg_sum(sum);
    const float f = ((const float*)(p.ws + OFF_GATE))[q] / sum;
    __builtin_amdgcn_raw_buffer_store_b128(pack8(o0 * f, o1 * f), rs, l * 16, (int)OFF_OF + (rt * 16 + h) * 1024, 16);
    asm volatile("s_waitcnt vmcnt(0)" ::: "memory");
    if (l == 0) __hip_atomic_fetch_add((GAS unsigned*)(p.ws + CT_OFC + s * 64), 1u, __ATOMIC_RELAXED, __HIP_MEMORY_SCOPE_AGENT);
}

__device__ __forceinline__ void out_item(const Params& p, LAS unsigned char* L, const __amdgpu_buffer_rsrc_t rs, const int s, const int q4, const int w, unsigned& mspin) {
    const int l = threadIdx.x & 63, n = l & 15, g = l >> 4;
    const f32x4 z4 = {0.f, 0.f, 0.f, 0.f};
    const float NEG = -3.0e38f;
    const int v = 7 * q4 + (w < 7 ? w : 0);
    if (w == 0) {
        for (;;) {
            asm volatile("" ::: "memory");
            const unsigned c = __builtin_amdgcn_raw_buffer_load_b32(rs, CT_OFC + s * 64, 0, 16);
            if (__builtin_amdgcn_readfirstlane(c) >= 32u || ++mspin >= SPIN_CAP) break;
            __builtin_amdgcn_s_sleep(4);
        }
    }
    BAR();
    f32x4 o0 = z4, o1 = z4;
    {
        const unsigned char* wp = p.ws + OFF_WOF + (size_t)(v * 16) * 1024 + l * 16;
#pragma unroll 4
        for (int h = 0; h < 16; ++h) {
            const v4u wf = ldg16(wp + h * 1024);
            o0 = mfma32(wf, ldc16(rs, l * 16, (int)OFF_OF + (2 * s * 16 + h) * 1024), o0);
            o1 = mfma32(wf, ldc16(rs, l * 16, (int)OFF_OF + ((2 * s + 1) * 16 + h) * 1024), o1);
        }
    }
    f32x4 sxx[2][3];
#pragma unroll
    for (int a = 0; a < 2; ++a) { sxx[a][0] = z4; sxx[a][1] = z4; sxx[a][2] = z4; }
    {
        const int gp = (int)OFF_GF + s * (int)SEGB + (2 * w) * 28 * 1024;
        const u16* xr = p.x + (size_t)(32 * s + n) * 448 + 8 * g;
#pragma unroll 2
        for (int ks = 0; ks < 14; ++ks) {
            const v4u x0 = ldg16(xr + 32 * ks), x1 = ldg16(xr + 16 * 448 + 32 * ks);
#pragma unroll
            for (int a = 0; a < 2; ++a) {
                const v4u k0 = ldc16(rs, l * 16, gp + (a * 28 + ks) * 1024), k1 = ldc16(rs, l * 16, gp + (a * 28 + 14 + ks) * 1024);
                sxx[a][0] = mfma32(k0, x0, sxx[a][0]);
                sxx[a][1] = mfma32(k0, x1, sxx[a][1]);
                sxx[a][2] = mfma32(k1, x1, sxx[a][2]);
            }
        }
    }
    v4u uf[16];
#pragma unroll
    for (int h = 0; h < 16; ++h) uf[h] = ldc16(rs, l * 16, (int)OFF_UF + s * (int)SEGB + (v * 16 + h) * 1024);
    const float gate0 = 1.0f - ((const float*)(p.ws + OFF_GATE))[32 * s + n], gate1 = 1.0f - ((const float*)(p.ws + OFF_GATE))[32 * s + 16 + n];
    float mxx[2][2];
#pragma unroll
    for (int a = 0; a < 2; ++a) {
#pragma unroll
        for (int r = 0; r < 4; ++r) { sxx[a][0][r] = (4 * g + r <= n) ? sxx[a][0][r] : NEG; sxx[a][2][r] = (4 * g + r <= n) ? sxx[a][2][r] : NEG; }
        mxx[a][0] = fmaxf(fmaxf(sxx[a][0][0], sxx[a][0][1]), fmaxf(sxx[a][0][2], sxx[a][0][3]));
        mxx[a][1] = fmaxf(fmaxf(fmaxf(sxx[a][1][0], sxx[a][1][1]), fmaxf(sxx[a][1][2], sxx[a][1][3])), fmaxf(fmaxf(sxx[a][2][0], sxx[a][2][1]), fmaxf(sxx[a][2][2], sxx[a][2][3])));
    }
    FENCE();
    v4u sm[4];
    for (;;) {
        asm volatile("" ::: "memory");
#pragma unroll
        for (int k = 0; k < 4; ++k) sm[k] = ldc16(rs, l * 16, (int)OFF_SXM + (s * 32 + 4 * w + k) * 1024);
        if (fresh(__builtin_elementwise_max(__builtin_elementwise_max(sm[0], sm[1]), __builtin_elementwise_max(sm[2], sm[3]))) || ++mspin >= SPIN_CAP) break;
        __builtin_amdgcn_s_sleep(6);
    }
    v2u yf[8];
#pragma unroll
    for (int k = 0; k < 8; ++k) yf[k] = __builtin_amdgcn_raw_buffer_load_b64(rs, l * 8, (int)OFF_YM + (s * 224 + v * 8 + k) * 512, 16);
    unsigned px[2][2][4]; unsigned pm[2][2];
#pragma unroll
    for (int qt = 0; qt < 2; ++qt) {
#pragma unroll
        for (int a = 0; a < 2; ++a) {
            const float t0 = bitsf(sm[2 * a][2 * qt]) + bitsf(sm[2 * a + 1][2 * qt]), t1 = bitsf(sm[2 * a][2 * qt + 1]) + bitsf(sm[2 * a + 1][2 * qt + 1]);
            const float m = xg_max(fmaxf(mxx[a][qt], fmaxf(t0, t1)));
            const f32x4 dg = sxx[a][qt == 0 ? 0 : 2];
            float ed[4], ef[4];
            const float e0 = ex2(t0 - m), e1 = ex2(t1 - m);
            float sum = e0 + e1;
#pragma unroll
            for (int r = 0; r < 4; ++r) {
                ed[r] = ex2(dg[r] - m); sum += ed[r];
                if (qt == 1) { ef[r] = ex2(sxx[a][1][r] - m); sum += ef[r]; }
            }
            sum = xg_sum(sum);
            const float f = (qt == 0 ? gate0 : gate1) * __builtin_amdgcn_rcpf(sum);
            if (qt == 0) { px[a][0][0] = pack2(ed[0] * f, ed[1] * f); px[a][0][1] = pack2(ed[2] * f, ed[3] * f); }
            else {
                px[a][1][0] = pack2(ef[0] * f, ef[1] * f); px[a][1][1] = pack2(ef[2] * f, ef[3] * f);
                px[a][1][2] = pack2(ed[0] * f, ed[1] * f); px[a][1][3] = pack2(ed[2] * f, ed[3] * f);
            }
            pm[qt][a] = pack2(e0 * f, e1 * f);
        }
    }
    *(LAS v4u*)(L + w * 4096 + l * 16) = v4u{px[0][0][0], px[0][0][1], px[1][0][0], px[1][0][1]};
    *(LAS v4u*)(L + w * 4096 + 1024 + l * 16) = v4u{px[0][1][0], px[0][1][1], px[0][1][2], px[0][1][3]};
    *(LAS v4u*)(L + w * 4096 + 2048 + l * 16) = v4u{px[1][1][0], px[1][1][1], px[1][1][2], px[1][1][3]};
    *(LAS v4u*)(L + w * 4096 + 3072 + l * 16) = v4u{pm[0][0], pm[0][1], pm[1][0], pm[1][1]};
    for (;;) {
        unsigned mm = 0u;
#pragma unroll
        for (int k = 0; k < 8; ++k) mm = max(mm, max(yf[k].x, yf[k].y));
        if (__all(mm != 0xFFFFFFFFu) || ++mspin >= SPIN_CAP) break;
        asm volatile("" ::: "memory");
#pragma unroll
        for (int k = 0; k < 8; ++k) yf[k] = __builtin_amdgcn_raw_buffer_load_b64(rs, l * 8, (int)OFF_YM + (s * 224 + v * 8 + k) * 512, 16);
    }
    BAR();
    if (w < 7) {
#pragma unroll
        for (int pp = 0; pp < 4; ++pp) {
            v4u pmm[2];
#pragma unroll
            for (int z = 0; z < 2; ++z) {
                const int pr = 2 * pp + z;
                const v4u u0 = uf[2 * pr], u1 = uf[2 * pr + 1];
                const v4u pa = *(const LAS v4u*)(L + pr * 4096 + l * 16), pb = *(const LAS v4u*)(L + pr * 4096 + 1024 + l * 16),
                          pc = *(const LAS v4u*)(L + pr * 4096 + 2048 + l * 16);
                pmm[z] = *(const LAS v4u*)(L + pr * 4096 + 3072 + l * 16);
                o1 = mfma32(u0, pb, o1);
                o0 = mfma32(v4u{u0.x, u0.y, u1.x, u1.y}, pa, o0);
                o1 = mfma32(u1, pc, o1);
            }
            const v4u yy = v4u{yf[2 * pp].x, yf[2 * pp].y, yf[2 * pp + 1].x, yf[2 * pp + 1].y};
            o0 = mfma32(yy, v4u{pmm[0].x, pmm[0].y, pmm[1].x, pmm[1].y}, o0);
            o1 = mfma32(yy, v4u{pmm[0].z, pmm[0].w, pmm[1].z, pmm[1].w}, o1);
        }
        stg8(p.out + (size_t)(32 * s + n) * 448 + 16 * v + 4 * g, v2u{pack2(o0[0], o0[1]), pack2(o0[2], o0[3])});
        stg8(p.out + (size_t)(32 * s + 16 + n) * 448 + 16 * v + 4 * g, v2u{pack2(o1[0], o1[1]), pack2(o1[2], o1[3])});
    }
}

__global__ void __launch_bounds__(512, 2) k_main(Params p) {
    extern __shared__ __attribute__((aligned(16))) unsigned char lds_[];
    p.ws = align_ws(p.ws);
    const int grp = blockIdx.x & 7, u = blockIdx.x >> 3;
    LAS unsigned char* L = (LAS unsigned char*)lds_;
    const int w = __builtin_amdgcn_readfirstlane(threadIdx.x >> 6);
    if (grp == 0) {
        if (w < 7) score_wave<false>(p, L, u, w); else score_wave<true>(p, L, u, w);
        return;
    }
    if (grp == 1) {
        if (u < 28) { if (w < 7) value_wave<false>(p, L, u, w); else value_wave<true>(p, L, u, w); }
        return;
    }
    const __amdgpu_buffer_rsrc_t rs = __builtin_amdgcn_make_buffer_rsrc(p.ws, 0, (int)WS_END, 0x00020000);
    const int k = (grp - 2) * 32 + u;
    if (k < 186) {
        sweep_and_serve(p, L, rs, k / 3, k % 3, w);
        if (k < 96 && k % 3 == 0) return;
    }
    const int wk = k < 96 ? (k / 3) * 2 + (k % 3 - 1) : k - 32;
    unsigned mspin = 0;
    if (w == 0) {
        const int l = threadIdx.x & 63;
        for (;;) {
            asm volatile("" ::: "memory");
            bool ok = true;
#pragma unroll
            for (int j = 0; j < 3; ++j) {
                const int id = min(l + 64 * j, 185);
                ok &= 3 * (int)__builtin_amdgcn_raw_buffer_load_b32(rs, CT_PROG + id * 4, 0, 16) + id % 3 >= NSEG;
            }
            if (__all(ok) || ++mspin >= SPIN_CAP) break;
            __builtin_amdgcn_s_sleep(8);
        }
    }
    BAR();
    for (int it = wk * 8 + w; it < 2048; it += 1280) attn_item(p, rs, it);
    for (int it = wk; it < 256; it += 160) out_item(p, L, rs, it >> 2, it & 3, w, mspin);
}

extern "C" void kernel_launch(void* const* d_in, const int* in_sizes, int n_in, void* d_out, int out_size, void* d_ws, size_t ws_size,
                              hipStream_t stream) {
    Params p;
    p.x = (const u16*)d_in[0]; p.wqkv = (const u16*)d_in[1]; p.wo = (const u16*)d_in[2]; p.mem0 = (const u16*)d_in[4];
    p.Wq = (const u16*)d_in[5]; p.Wk = (const u16*)d_in[6]; p.Wv = (const u16*)d_in[7]; p.Wo = (const u16*)d_in[8];
    p.gate_w = (const u16*)d_in[10];
    p.out = (u16*)d_out; p.ws = (unsigned char*)d_ws;
    (void)hipFuncSetAttribute((const void*)k_prep, hipFuncAttributeMaxDynamicSharedMemorySize, K1_LDS);
    (void)hipFuncSetAttribute((const void*)k_main, hipFuncAttributeMaxDynamicSharedMemorySize, L_REC_END);
    k_prep<<<256, 512, K1_LDS, stream>>>(p);
    k_main<<<256, 512, L_REC_END, stream>>>(p);
}
```

```cpp
#include <hip/hip_runtime.h>
#include <hip/hip_bf16.h>
#include <stdint.h>

#define GAS __attribute__((address_space(1)))
#define LAS __attribute__((address_space(3)))
typedef __bf16 bf16x2 __attribute__((ext_vector_type(2)));
typedef __bf16 bf16x4 __attribute__((ext_vector_type(4)));
typedef __bf16 bf16x8 __attribute__((ext_vector_type(8)));
typedef __bf16 bf16x16 __attribute__((ext_vector_type(16)));
typedef short s16x4 __attribute__((ext_vector_type(4)));
typedef float f32x2 __attribute__((ext_vector_type(2)));
typedef float f32x4 __attribute__((ext_vector_type(4)));
typedef unsigned v2u __attribute__((ext_vector_type(2)));
typedef unsigned v4u __attribute__((ext_vector_type(4)));
typedef unsigned v8u __attribute__((ext_vector_type(8)));
typedef unsigned short u16;

constexpr int S_ = 2048, D_ = 448, H_ = 16, NSEG = 64;
constexpr float LOG2E = 1.4426950408889634f;

constexpr size_t SZ_W = (size_t)16 * 28 * 14 * 1024;
constexpr size_t OFF_HOP = 0;
constexpr int PAGE = 2 << 20, CH = 16384, NCHUNK = 9, STEPB = NCHUNK * CH, NPAGE = 5, SPP = 14;
constexpr int C_M = 0, C_M2 = 4 * CH, C_M3 = 5 * CH, C_S = 6 * CH, C_P = 7 * CH;
constexpr size_t HOP_BYTES = (size_t)NPAGE * PAGE;
constexpr size_t OFF_CTL = OFF_HOP + HOP_BYTES;
constexpr size_t CTL_BYTES = 36864;
constexpr int CT_DONE = (int)OFF_CTL + 256, CT_OFC = (int)OFF_CTL + 512, CT_PROG = CT_OFC + 64 * 128;
constexpr size_t OFF_TW = OFF_CTL + CTL_BYTES;
constexpr size_t OFF_YW = OFF_TW + SZ_W;
constexpr size_t OFF_GW = OFF_YW + SZ_W;
constexpr size_t OFF_QW = OFF_GW + SZ_W;
constexpr size_t OFF_VW = OFF_QW + (size_t)32 * 28 * 1024;
constexpr size_t OFF_WOF = OFF_VW + (size_t)16 * 28 * 1024;
constexpr size_t OFF_XP = OFF_WOF + (size_t)28 * 16 * 1024;
constexpr size_t OFF_GATE = OFF_XP + (size_t)128 * 14 * 1024;
constexpr size_t SEGB = (size_t)448 * 1024;
constexpr size_t OFF_GF = OFF_GATE + 8192;
constexpr size_t OFF_UF = OFF_GF + 64 * SEGB;
constexpr size_t OFF_QF = OFF_UF + 64 * SEGB;
constexpr size_t OFF_KF = OFF_QF + (size_t)2048 * 1024;
constexpr size_t OFF_VF = OFF_KF + (size_t)2048 * 1024;
constexpr size_t OFF_OF = OFF_VF + (size_t)2048 * 1024;
constexpr size_t OFF_SXM = OFF_OF + (size_t)2048 * 1024;
constexpr size_t OFF_YM = OFF_SXM + (size_t)64 * 32 * 1024;
constexpr size_t WS_END = OFF_YM + (size_t)64 * 224 * 512;

struct Params {
    const u16* x; const u16* wqkv; const u16* wo; const u16* mem0;
    const u16* Wq; const u16* Wk; const u16* Wv; const u16* Wo; const u16* gate_w;
    u16* out; unsigned char* ws;
};
__device__ __forceinline__ unsigned char* align_ws(unsigned char* w) {
    return w + ((0 - (size_t)w) & (size_t)(PAGE - 1));
}
__device__ __forceinline__ unsigned xcc_id() { return (unsigned)__builtin_amdgcn_s_getreg(20 | (3 << 11)) & 0xfu; }
__device__ __forceinline__ unsigned load_parmask(const unsigned char* ws) {
    const unsigned* c = (const unsigned*)(ws + OFF_CTL);
    unsigned m = 0;
#pragma unroll
    for (int k = 0; k < NPAGE; ++k) m |= (c[k] & 1u) << k;
    return __builtin_amdgcn_readfirstlane(m);
}
__device__ __forceinline__ int hop_off(int s, unsigned parmask) {
    const int pg = s / SPP, sl = s - pg * SPP;
    return pg * PAGE + sl * STEPB + (int)((parmask >> pg) & 1u) * 8192;
}

__device__ __forceinline__ unsigned pack2(float a, float b) {
    return __builtin_bit_cast(unsigned, __builtin_convertvector(f32x2{a, b}, bf16x2));
}
__device__ __forceinline__ float bf2f(u16 v) { return __builtin_bit_cast(float, (unsigned)v << 16); }
__device__ __forceinline__ f32x4 mfma32(v4u a, v4u b, f32x4 c) {
    return __builtin_amdgcn_mfma_f32_16x16x32_bf16(__builtin_bit_cast(bf16x8, a), __builtin_bit_cast(bf16x8, b), c, 0, 0, 0);
}
__device__ __forceinline__ f32x4 mfma16(v2u a, v2u b, f32x4 c) {
    return __builtin_amdgcn_mfma_f32_16x16x16bf16_1k(__builtin_bit_cast(s16x4, a), __builtin_bit_cast(s16x4, b), c, 0, 0, 0);
}
__device__ __forceinline__ f32x4 smfmac(v4u a, v8u b, f32x4 c, int idx) {
    return __builtin_amdgcn_smfmac_f32_16x16x64_bf16(__builtin_bit_cast(bf16x8, a), __builtin_bit_cast(bf16x16, b), c, idx, 0, 0);
}
__device__ __forceinline__ v4u ldg16(const void* p) { return *(const GAS v4u*)p; }
__device__ __forceinline__ v2u ldg8(const void* p) { return *(const GAS v2u*)p; }
__device__ __forceinline__ void stg16(void* p, v4u v) { *(GAS v4u*)p = v; }
__device__ __forceinline__ void stg8(void* p, v2u v) { *(GAS v2u*)p = v; }
__device__ __forceinline__ v2u tr_read(const LAS void* p) {
    return __builtin_bit_cast(v2u, __builtin_amdgcn_ds_read_tr16_b64_v4i16((LAS s16x4*)p));
}
__device__ __forceinline__ float ror8(float v) {
    return __builtin_bit_cast(float, __builtin_amdgcn_update_dpp(0, __builtin_bit_cast(int, v), 0x128, 0xf, 0xf, false));
}
__device__ __forceinline__ unsigned fbits(float v) { return __builtin_bit_cast(unsigned, v); }
__device__ __forceinline__ float bitsf(unsigned v) { return __builtin_bit_cast(float, v); }
__device__ __forceinline__ float xg_max(float v) {
    const v2u a = __builtin_amdgcn_permlane16_swap(fbits(v), fbits(v), false, false);
    v = fmaxf(bitsf(a.x), bitsf(a.y));
    const v2u b = __builtin_amdgcn_permlane32_swap(fbits(v), fbits(v), false, false);
    return fmaxf(bitsf(b.x), bitsf(b.y));
}
__device__ __forceinline__ float xg_sum(float v) {
    const v2u a = __builtin_amdgcn_permlane16_swap(fbits(v), fbits(v), false, false);
    v = bitsf(a.x) + bitsf(a.y);
    const v2u b = __builtin_amdgcn_permlane32_swap(fbits(v), fbits(v), false, false);
    return bitsf(b.x) + bitsf(b.y);
}
__device__ __forceinline__ float fold32(float a, float b) {
    const v2u r = __builtin_amdgcn_permlane32_swap(fbits(a), fbits(b), false, false);
    return bitsf(r.x) + bitsf(r.y);
}
__device__ __forceinline__ float ex2(float v) { return __builtin_amdgcn_exp2f(v); }
__device__ __forceinline__ unsigned sane(unsigned v) { return v < 0xFFFFFFFEu ? v : 0xFFFFFFFEu; }

__device__ __forceinline__ int porder(int f, int g, int e) {
    return 64 * (f >> 1) + 16 * (f & 1) + 4 * g + 32 * ((e >> 1) & 1) + 2 * (e >> 2) + (e & 1);
}
constexpr int NU_HOP = 64 * NCHUNK * 16;
__device__ __forceinline__ void misc_wave(const Params& p, const int mw, const int l) {
    const int n = l & 15, g = l >> 4;
    v2u xl[4], xh[4];
#pragma unroll
    for (int k = 0; k < 4; ++k) {
        const int i = mw * 4 + k, rt = i / 14, f = i % 14;
        const u16* row = p.x + (size_t)(rt * 16 + n) * 448 + 64 * (f >> 1) + 16 * (f & 1) + 4 * g;
        xl[k] = ldg8(row); xh[k] = ldg8(row + 32);
    }
    unsigned qv[3][8];
    bool qok[3];
#pragma unroll
    for (int k = 0; k < 3; ++k) {
        const int i = mw * 3 + k;
        const bool isv = i >= 896;
        const int fr = isv ? i - 896 : i;
        const int unit = fr / 28, t = (fr % 28) / 14, f = fr % 14;
        const int d = isv ? 16 * t + n : 8 * (n >> 2) + 4 * t + (n & 3);
        const int col = (isv ? 896 + unit * 28 : (unit >> 4) * 448 + (unit & 15) * 28) + (d < 28 ? d : 0);
        qok[k] = d < 28;
#pragma unroll
        for (int e = 0; e < 8; ++e) qv[k][e] = p.wqkv[(size_t)porder(f, g, e) * 1344 + col];
    }
    unsigned wv[8];
#pragma unroll
    for (int e = 0; e < 8; ++e) {
        const int d = (e < 4) ? 4 * g + e : 16 + 4 * g + e - 4;
        wv[e] = p.wo[(size_t)((mw & 15) * 28 + (d < 28 ? d : 0)) * 448 + 16 * (mw >> 4) + n];
        if (d >= 28) wv[e] = 0u;
    }
    v4u gxv[7], ggv[7];
    const int row = (mw & 255) * 8 + (l >> 3), c0 = (l & 7) * 8;
#pragma unroll
    for (int k = 0; k < 7; ++k) { gxv[k] = ldg16(p.x + (size_t)row * 448 + k * 64 + c0); ggv[k] = ldg16(p.gate_w + k * 64 + c0); }
    for (int i = mw; i < NU_HOP; i += 448) {
        const int st = i / (NCHUNK * 16), ck = (i >> 4) % NCHUNK, kb = i & 15;
        v4u val = v4u{0xFFFFFFFFu, 0xFFFFFFFFu, 0xFFFFFFFFu, 0xFFFFFFFFu};
        if (st == 0 && ck < 6 && (kb & 7) < 7) {
            const int im = (kb & 7) * 64 + l;
            const v4u m = ldg16(p.mem0 + (size_t)(im & 7) * 448 + (im >> 3) * 8);
            val = v4u{sane(m.x), sane(m.y), sane(m.z), sane(m.w)};
        }
        stg16(p.ws + OFF_HOP + (size_t)hop_off(st, 0u) + ck * CH + kb * 1024 + l * 16, val);
    }
    for (int i = mw; i < (int)((WS_END - OFF_SXM) / 1024); i += 448)
        stg16(p.ws + OFF_SXM + (size_t)i * 1024 + l * 16, v4u{0xFFFFFFFFu, 0xFFFFFFFFu, 0xFFFFFFFFu, 0xFFFFFFFFu});
#pragma unroll
    for (int k = 0; k < 4; ++k) stg16(p.ws + OFF_XP + (size_t)(mw * 4 + k) * 1024 + l * 16, v4u{xl[k].x, xh[k].x, xl[k].y, xh[k].y});
#pragma unroll
    for (int k = 0; k < 3; ++k) {
        const v4u o = v4u{qv[k][0] | (qv[k][1] << 16), qv[k][2] | (qv[k][3] << 16), qv[k][4] | (qv[k][5] << 16), qv[k][6] | (qv[k][7] << 16)};
        stg16(p.ws + OFF_QW + (size_t)(mw * 3 + k) * 1024 + l * 16, qok[k] ? o : v4u{0u, 0u, 0u, 0u});
    }
    stg16(p.ws + OFF_WOF + (size_t)mw * 1024 + l * 16, v4u{wv[0] | (wv[1] << 16), wv[2] | (wv[3] << 16), wv[4] | (wv[5] << 16), wv[6] | (wv[7] << 16)});
    float s = 0.f;
#pragma unroll
    for (int k = 0; k < 7; ++k)
#pragma unroll
        for (int j = 0; j < 4; ++j) {
            s += bitsf(gxv[k][j] << 16) * bitsf(ggv[k][j] << 16);
            s += bitsf(gxv[k][j] & 0xFFFF0000u) * bitsf(ggv[k][j] & 0xFFFF0000u);
        }
    s += __shfl_xor(s, 1); s += __shfl_xor(s, 2); s += __shfl_xor(s, 4);
    if ((l & 7) == 0 && mw < 256) ((float*)(p.ws + OFF_GATE))[row] = 1.0f / (1.0f + __expf(-s));
}

constexpr int K1_RS = 528, K1_OS = 144, K1_IMG = 36864, K1_IS = 136, K1_LDS = K1_IMG + 7 * 64 * K1_IS;
__global__ void __launch_bounds__(512, 2) k_prep(Params p) {
    extern __shared__ __attribute__((aligned(16))) unsigned char lds_[];
    LAS unsigned char* L = (LAS unsigned char*)lds_;
    p.ws = align_ws(p.ws);
    const int b = blockIdx.x, tid = threadIdx.x;
    const int l = tid & 63, w = __builtin_amdgcn_readfirstlane(tid >> 6);
    const int n = l & 15, g = l >> 4;
#define PT(k) do {} while (0)
#define PT0(k) do {} while (0)
    PT0(0);
    if (b >= 224) {
        const int k = b - 224;
        if ((k & 7) == 0 && w < 2) {
            const int pg = (k >> 3) + 4 * w;
            if (pg < NPAGE && l == 0) {
                GAS unsigned* a = (GAS unsigned*)(p.ws + OFF_HOP + (size_t)pg * PAGE + 126 * CH);
                unsigned best[4] = {~0u, ~0u, ~0u, ~0u};
                for (int it = 0; it < 3; ++it) {
#pragma unroll
                    for (int q = 0; q < 4; ++q) {
                        const unsigned long long t0 = __builtin_amdgcn_s_memtime();
                        __hip_atomic_store(a + q * 1024, 0xFFFFFFFFu, __ATOMIC_RELAXED, __HIP_MEMORY_SCOPE_AGENT);
                        asm volatile("s_waitcnt vmcnt(0)" ::: "memory");
                        const unsigned dt = (unsigned)(__builtin_amdgcn_s_memtime() - t0);
                        best[q] = min(best[q], dt);
                    }
                }
                ((unsigned*)(p.ws + OFF_CTL))[pg] = ((best[0] + best[1] > best[2] + best[3]) ? 1u : 0u) ^ ((xcc_id() >> 2) & 1u);
            }
            PT(3);
            if (pg == 7) {
                if (l == 0) ((unsigned*)(p.ws + OFF_CTL))[8] = xcc_id() & 7u;
                for (int i = l; i < (int)(CTL_BYTES - 256) / 16; i += 64) stg16(p.ws + OFF_CTL + 256 + i * 16, v4u{0u, 0u, 0u, 0u});
            }
            return;
        }
        if ((k & 7) == 0) return;
        const int kk = k - (k >> 3) - 1;
        misc_wave(p, kk * 8 + w, l);
        PT(3);
        return;
    }
    const int r = b >> 3, h = (b & 7) + 8 * (r & 1), q = r >> 1, mat = q / 7, blk = q % 7;
    v4u t[4];
    if (mat == 0) {
#pragma unroll
        for (int k = 0; k < 4; ++k) { const int u = tid + 512 * k; t[k] = ldg16(p.Wq + ((size_t)(64 * blk + (u >> 5)) * 16 + h) * 256 + (u & 31) * 8); }
    } else {
#pragma unroll
        for (int k = 0; k < 4; ++k) { const int u = tid + 512 * k; t[k] = ldg16(p.Wo + ((size_t)h * 256 + (u >> 3)) * 448 + 64 * blk + (u & 7) * 8); }
    }
    __builtin_amdgcn_sched_barrier(0);
    v4u cr[4][8];
    {
        const u16* src = (mat == 0 ? p.Wk : p.Wv) + (size_t)h * 256 + 8 * g;
        const int r0 = 64 * (w < 7 ? w : 0) + 4 * (n >> 2) + 32 * ((n >> 1) & 1) + (n & 1);
        if (w < 7) {
#pragma unroll
            for (int es = 0; es < 8; ++es)
#pragma unroll
                for (int tt = 0; tt < 4; ++tt) cr[tt][es] = ldg16(src + (size_t)(r0 + 16 * (tt >> 1) + 2 * (tt & 1)) * 4096 + 32 * es);
        }
    }
    __builtin_amdgcn_sched_barrier(0);
    {
        const int rs_ = mat == 0 ? K1_RS : K1_OS, sh = mat == 0 ? 5 : 3;
#pragma unroll
        for (int k = 0; k < 4; ++k) { const int u = tid + 512 * k; *(LAS v4u*)(L + (u >> sh) * rs_ + (u & ((1 << sh) - 1)) * 16) = t[k]; }
    }
    asm volatile("s_waitcnt lgkmcnt(0)" ::: "memory");
    __builtin_amdgcn_s_barrier();
    asm volatile("" ::: "memory");
    PT0(1);
    if (w == 7) {
        misc_wave(p, 224 + b, l);
        PT(3);
        return;
    }
    f32x4 acc[4][4];
#pragma unroll
    for (int x = 0; x < 4; ++x)
#pragma unroll
        for (int y = 0; y < 4; ++y) acc[x][y] = f32x4{0.f, 0.f, 0.f, 0.f};
    if (mat == 0) {
#pragma unroll
        for (int es = 0; es < 8; ++es) {
            v4u af[4];
#pragma unroll
            for (int x = 0; x < 4; ++x) af[x] = *(const LAS v4u*)(L + (16 * x + n) * K1_RS + es * 64 + g * 16);
#pragma unroll
            for (int x = 0; x < 4; ++x)
#pragma unroll
                for (int y = 0; y < 4; ++y) acc[x][y] = mfma32(af[x], cr[y][es], acc[x][y]);
        }
        __builtin_amdgcn_sched_barrier(0); PT0(2); __builtin_amdgcn_sched_barrier(0);
        constexpr float SC = LOG2E / 16.0f;
        LAS unsigned char* img = L + K1_IMG + w * 64 * K1_IS;
#pragma unroll
        for (int y = 0; y < 4; ++y) {
            v2u pk[4];
#pragma unroll
            for (int x = 0; x < 4; ++x) {
                pk[x] = v2u{pack2(acc[x][y][0] * SC, acc[x][y][1] * SC), pack2(acc[x][y][2] * SC, acc[x][y][3] * SC)};
                *(LAS v2u*)(img + (16 * y + n) * K1_IS + x * 32 + g * 8) = pk[x];
            }
            unsigned char* d = p.ws + OFF_TW + ((size_t)(h * 28 + 4 * w + y) * 7 + blk) * 2048 + l * 16;
            stg16(d, v4u{pk[0].x, pk[2].x, pk[0].y, pk[2].y});
            stg16(d + 1024, v4u{pk[1].x, pk[3].x, pk[1].y, pk[3].y});
        }
        asm volatile("" ::: "memory");
#pragma unroll
        for (int j = 0; j < 4; ++j) {
            unsigned char* d = p.ws + OFF_GW + ((size_t)(h * 28 + 4 * blk + j) * 7 + w) * 2048 + l * 16;
            const LAS unsigned char* rp = img + (4 * g + (n >> 2)) * K1_IS + (j >> 1) * 64 + (l & 3) * 16 + (j & 1) * 8;
#pragma unroll
            for (int a = 0; a < 2; ++a) {
                const v2u lo = tr_read(rp + (32 * a) * K1_IS), hi = tr_read(rp + (32 * a + 16) * K1_IS);
                stg16(d + a * 1024, v4u{lo.x, lo.y, hi.x, hi.y});
            }
        }
    } else {
#pragma unroll
        for (int es = 0; es < 8; ++es) {
            v4u bfr[4];
#pragma unroll
            for (int y = 0; y < 4; ++y) {
                const LAS unsigned char* rp = L + (32 * es + 8 * g + (n >> 2)) * K1_OS + y * 32 + (l & 3) * 8;
                const v2u lo = tr_read(rp), hi = tr_read(rp + 4 * K1_OS);
                bfr[y] = v4u{lo.x, lo.y, hi.x, hi.y};
            }
#pragma unroll
            for (int x = 0; x < 4; ++x)
#pragma unroll
                for (int y = 0; y < 4; ++y) acc[x][y] = mfma32(cr[x][es], bfr[y], acc[x][y]);
        }
        __builtin_amdgcn_sched_barrier(0); PT0(2); __builtin_amdgcn_sched_barrier(0);
#pragma unroll
        for (int y = 0; y < 4; ++y) {
            unsigned char* d = p.ws + OFF_YW + ((size_t)((4 * blk + y) * 16 + h) * 7 + w) * 2048 + l * 16;
#pragma unroll
            for (int a = 0; a < 2; ++a)
                stg16(d + a * 1024, v4u{pack2(acc[2 * a][y][0], acc[2 * a][y][1]), pack2(acc[2 * a][y][2], acc[2 * a][y][3]),
                                        pack2(acc[2 * a + 1][y][0], acc[2 * a + 1][y][1]), pack2(acc[2 * a + 1][y][2], acc[2 * a + 1][y][3])});
        }
    }
    PT(3);
}

constexpr int L_MST = 0, L_TIM = L_MST + 2 * 7168, L_RED = L_TIM + 8192, L_REC_END = 96 * 1024;
#define DLY_M 1024
#define DM0 15
#define EARLY_ACC 2
#define LATE_ADD 3
#define DLY_Q 0
#define PF_AHEAD 2
#define PF_GAP 6
#define DLY_G 300
#define DLY_3 192
#define GAP_M 150
#define PPOS 6
#define SPOS 1
#define DLY_V 240
#define DLY_S 100
__device__ __forceinline__ void delay_var(int n) {
    asm volatile("s_cmp_lt_i32 %0, 1\n\ts_cbranch_scc1 2f\n1:\n\ts_nop 5\n\ts_sub_i32 %0, %0, 1\n\ts_cmp_gt_i32 %0, 0\n\ts_cbranch_scc1 1b\n2:" : "+s"(n) :: "scc", "memory");
}
template <int C> __device__ __forceinline__ void delay_cycles() {
    if constexpr (C >= 64) __builtin_amdgcn_s_sleep(C / 64 > 127 ? 127 : C / 64);
    if constexpr ((C & 63) >= 4) asm volatile("s_nop %0" :: "i"(((C & 63) >> 2) - 1));
}

__device__ __forceinline__ bool fresh(v4u x) {
    const unsigned m = max(max(x.x, x.y), max(x.z, x.w));
    return __all(m != 0xFFFFFFFFu);
}
#define BAR() do { asm volatile("s_waitcnt lgkmcnt(0)" ::: "memory"); __builtin_amdgcn_s_barrier(); asm volatile("" ::: "memory"); } while (0)
#define FENCE() do { asm volatile("" ::: "memory"); __builtin_amdgcn_sched_barrier(0); } while (0)

#define STAMP(k) do {} while (0)
#define PROF_DECL
#define PROF_ACC do {} while (0)
#define PROF_OUT(slot) do {} while (0)
typedef _Float16 f16x2 __attribute__((ext_vector_type(2)));
__device__ __forceinline__ unsigned packh(float a, float b) { return __builtin_bit_cast(unsigned, __builtin_amdgcn_cvt_pkrtz(a, b)); }
__device__ __forceinline__ float hlo(unsigned v) { const f16x2 h = __builtin_bit_cast(f16x2, v); return (float)h.x; }
__device__ __forceinline__ float hhi(unsigned v) { const f16x2 h = __builtin_bit_cast(f16x2, v); return (float)h.y; }
constexpr unsigned SPIN_CAP = 1u << 20;

__device__ __forceinline__ float vmax(float a, float b) { float r; asm("v_max_f32 %0, %1, %2" : "=v"(r) : "v"(a), "v"(b)); return r; }
__device__ __forceinline__ float xg_max_raw(float v) {
    const v2u a = __builtin_amdgcn_permlane16_swap(fbits(v), fbits(v), false, false);
    v = vmax(bitsf(a.x), bitsf(a.y));
    const v2u b = __builtin_amdgcn_permlane32_swap(fbits(v), fbits(v), false, false);
    return vmax(bitsf(b.x), bitsf(b.y));
}
__device__ __forceinline__ float addh_lo(unsigned a, unsigned b) { float r; asm("v_fma_mix_f32 %0, %1, 1.0, %2 op_sel_hi:[1,0,1]" : "=v"(r) : "v"(a), "v"(b)); return r; }
__device__ __forceinline__ float addh_hi(unsigned a, unsigned b) { float r; asm("v_fma_mix_f32 %0, %1, 1.0, %2 op_sel:[1,0,1] op_sel_hi:[1,0,1]" : "=v"(r) : "v"(a), "v"(b)); return r; }
__device__ __forceinline__ float subh_lo(unsigned a, float b) { float r; asm("v_fma_mix_f32 %0, %1, 1.0, -%2 op_sel_hi:[1,0,0]" : "=v"(r) : "v"(a), "v"(b)); return r; }
__device__ __forceinline__ float subh_hi(unsigned a, float b) { float r; asm("v_fma_mix_f32 %0, %1, 1.0, -%2 op_sel:[1,0,0] op_sel_hi:[1,0,0]" : "=v"(r) : "v"(a), "v"(b)); return r; }
__device__ __forceinline__ float add_ror8(float a) { float r; asm("s_nop 1\n\tv_add_f32_dpp %0, %1, %1 row_ror:8 row_mask:0xf bank_mask:0xf" : "=v"(r) : "v"(a)); return r; }
#define UNLIKELY(x) __builtin_expect(!!(x), 0)
#define CNT(x) do {} while (0)

#define TLS(k) do {} while (0)
template <int NL>
__device__ __forceinline__ int wait_ready(const __amdgpu_buffer_rsrc_t rs, const int first, const int need, unsigned& mspin) {
    const int l = threadIdx.x & 63;
    int ready;
    for (;;) {
        asm volatile("" ::: "memory");
        const int v = (int)__builtin_amdgcn_raw_buffer_load_b32(rs, CT_PROG + (first + (l < NL ? l : 0)) * 128, 0, 16);
        ready = 1 << 20;
#pragma unroll
        for (int k = 0; k < NL; ++k) ready = min(ready, 3 * __builtin_amdgcn_readlane(v, k) + (k % 3));
        if (ready > need || ++mspin >= SPIN_CAP) break;
        __builtin_amdgcn_s_sleep(2);
    }
    asm volatile("" ::: "memory");
    return ready;
}

template <bool SPECIAL>
__device__ __forceinline__ void score_wave(const Params& p, LAS unsigned char* L, const int u, const int w) {
    const int l = threadIdx.x & 63;
    const int n = l & 15, g = l >> 4, i8 = l & 7, hh = (l >> 3) & 1;
    const int half = u & 1;
    const __amdgpu_buffer_rsrc_t rs = __builtin_amdgcn_make_buffer_rsrc(p.ws, 0, (int)WS_END, 0x00020000);
    const int idx = hh ? 0xEEEE : 0x4444;
    const unsigned parmask = load_parmask(p.ws);
    v8u tw[SPECIAL ? 1 : 14];
    v4u gx[SPECIAL ? 14 : 1];
    if constexpr (!SPECIAL) {
        const unsigned char* src = p.ws + OFF_TW + ((size_t)(u * 14 + 2 * w) * 7) * 2048 + l * 16;
#pragma unroll
        for (int f = 0; f < 14; ++f) {
            const v4u a = ldg16(src + f * 2048), b = ldg16(src + f * 2048 + 1024);
            tw[f] = v8u{a.x, a.y, a.z, a.w, b.x, b.y, b.z, b.w};
        }
    } else {
        *(LAS v4u*)(L + L_TIM + (half ? 0 : 14 * 512) + l * 16) = v4u{0u, 0u, 0u, 0u};
        __builtin_amdgcn_s_setprio(2);
    }
    const int moff = C_M + (SPECIAL ? 0 : w * 1024) + hh * 512 + g * 128 + i8 * 16;
    const int sst_off = C_S + hh * 4096 + u * 128 + i8 * 16 + (g & 1) * 8 + (g >> 1) * 4;
    const int pst_off = C_P + hh * CH + u * 256 + i8 * 32 + g * 8;
    const int goff = ((u & ~1) + ((n >> 2) & 1)) * 14336 + (8 * half + 4 * (n >> 3) + (n & 3) + 16 * g) * 16;
    unsigned spins = 0, mspin = 0;
    int ready = 0, dm = DM0, early = 0;
    if constexpr (SPECIAL) {
        ready = wait_ready<6>(rs, 6 * (u >> 1), 0, mspin);
#pragma unroll
        for (int f = 0; f < 14; ++f) gx[f] = __builtin_amdgcn_raw_buffer_load_b128(rs, goff + f * 1024, (int)OFF_GF, 16);
    }
    PROF_DECL
    for (int s = 0; s < NSEG - 1; ++s) {
        const int ho = hop_off(s, parmask);
        v4u x1, x2, x3 = v4u{0u, 0u, 0u, 0u};
        FENCE();
        STAMP(0);
        if constexpr (!SPECIAL) {
            if (s) { delay_cycles<DLY_M>(); delay_var(dm); }
            FENCE();
            v4u x = __builtin_amdgcn_raw_buffer_load_b128(rs, moff, ho, 16);
            FENCE(); delay_cycles<GAP_M>(); FENCE();
            x1 = __builtin_amdgcn_raw_buffer_load_b128(rs, moff, ho + CH, 16);
            FENCE(); delay_cycles<GAP_M>(); FENCE();
            x2 = __builtin_amdgcn_raw_buffer_load_b128(rs, moff, ho + 2 * CH, 16);
            FENCE();
            if (!fresh(x)) {
                FENCE();
                x = __builtin_amdgcn_raw_buffer_load_b128(rs, moff, ho + 3 * CH, 16);
                FENCE();
                CNT(1);
                if (fresh(x1)) { x3 = x; x = x1; }
                else {
                    CNT(1);
                    if (fresh(x2)) { x3 = x; x = x2; }
                    else {
                        CNT(1);
                        while (!fresh(x) && ++mspin < SPIN_CAP) {
                            asm volatile("" ::: "memory");
                            x = __builtin_amdgcn_raw_buffer_load_b128(rs, moff, ho + 3 * CH, 16);
                            CNT(1);
                        }
                    }
                }
            }
            STAMP(1);
            *(LAS v4u*)(L + L_MST + w * 1024 + l * 16) = x;
        }
        v4u mp[7];
        BAR();
        STAMP(2);
        if constexpr (!SPECIAL) {
#pragma unroll
            for (int f = 0; f < 7; ++f) mp[f] = *(const LAS v4u*)(L + L_MST + f * 1024 + l * 16);
            FENCE();
            if (w == 3) delay_cycles<DLY_3>();
            FENCE();
            f32x4 a0 = {0.f, 0.f, 0.f, 0.f}, a1 = a0;
#pragma unroll
            for (int f = 0; f < 7; ++f) { a0 = smfmac(mp[f], tw[f], a0, idx); a1 = smfmac(mp[f], tw[7 + f], a1, idx); }
            *(LAS v2u*)(L + L_TIM + (2 * w + 2 * half) * 512 + n * 32 + g * 8) = v2u{pack2(a0[0], a0[1]), pack2(a0[2], a0[3])};
            *(LAS v2u*)(L + L_TIM + (2 * w + 2 * half + 1) * 512 + n * 32 + g * 8) = v2u{pack2(a1[0], a1[1]), pack2(a1[2], a1[3])};
            STAMP(3);
            BAR();
            STAMP(4);
            asm volatile("" :: "v"(x1), "v"(x2), "v"(x3));
            TLS(8 + s);
        } else {
#pragma unroll
            for (int f = 0; f < 7; ++f) mp[f] = *(const LAS v4u*)(L + L_MST + f * 1024 + l * 16);
            FENCE();
            f32x4 ae = {0.f, 0.f, 0.f, 0.f}, ao = ae;
#pragma unroll
            for (int f = 0; f < 7; ++f) {
                ae = mfma32(gx[2 * f], mp[f], ae);
                ao = mfma32(gx[2 * f + 1], mp[f], ao);
            }
            f32x4 sx = hh ? ao : ae;
            asm("s_nop 1\n\tv_add_f32_dpp %0, %0, %0 row_ror:8 row_mask:0xf bank_mask:0xf\n\tv_add_f32_dpp %1, %1, %1 row_ror:8 row_mask:0xf bank_mask:0xf\n\t"
                "v_add_f32_dpp %2, %2, %2 row_ror:8 row_mask:0xf bank_mask:0xf\n\tv_add_f32_dpp %3, %3, %3 row_ror:8 row_mask:0xf bank_mask:0xf"
                : "+v"(sx[0]), "+v"(sx[1]), "+v"(sx[2]), "+v"(sx[3]));
            const v2u pv = v2u{sane(packh(sx[0], sx[1])), sane(packh(sx[2], sx[3]))};
            __builtin_amdgcn_raw_buffer_store_b64(pv, rs, pst_off, ho, 16);
            FENCE();
            STAMP(3);
            v4u m2[4];
#pragma unroll
            for (int b = 0; b < 4; ++b) m2[b] = *(const LAS v4u*)(L + L_MST + (3 * half + b) * 1024 + l * 16);
            f32x4 sa = {0.f, 0.f, 0.f, 0.f};
            asm volatile("" : "+v"(sa));
            BAR();
            STAMP(4);
            v8u tb[4];
#pragma unroll
            for (int b = 0; b < 4; ++b) {
                const v2u t0 = tr_read(L + L_TIM + (4 * b) * 512 + l * 8), t1 = tr_read(L + L_TIM + (4 * b + 1) * 512 + l * 8),
                          t2 = tr_read(L + L_TIM + (4 * b + 2) * 512 + l * 8), t3 = tr_read(L + L_TIM + (4 * b + 3) * 512 + l * 8);
                tb[b] = v8u{t0.x, t0.y, t1.x, t1.y, t2.x, t2.y, t3.x, t3.y};
            }
            FENCE();
#pragma unroll
            for (int b = 0; b < 4; ++b) sa = smfmac(m2[b], tb[b], sa, idx);
            const float f0 = add_ror8(fold32(sa[0], sa[2])), f1 = add_ror8(fold32(sa[1], sa[3]));
            const unsigned sv = sane(packh(f0, f1));
            __builtin_amdgcn_raw_buffer_store_b32(sv, rs, sst_off, ho, 16);
            FENCE();
            STAMP(5);
            delay_cycles<DLY_G>();
            const int go = (int)(OFF_GF + (s + 1) * SEGB);
            if (UNLIKELY(ready <= s + 1)) ready = wait_ready<6>(rs, 6 * (u >> 1), s + 1, mspin);
#pragma unroll
            for (int f = 0; f < 14; ++f) gx[f] = __builtin_amdgcn_raw_buffer_load_b128(rs, goff + f * 1024, go, 16);
            FENCE();
        }
        PROF_ACC;
    }
    PROF_OUT(u * 8 + w);
}

template <bool LAST>
__device__ __forceinline__ void value_wave(const Params& p, LAS unsigned char* L, const int v, const int w) {
    const int l = threadIdx.x & 63;
    const int g = l >> 4, i8 = l & 7, hh = (l >> 3) & 1;
    const __amdgpu_buffer_rsrc_t rs = __builtin_amdgcn_make_buffer_rsrc(p.ws, 0, (int)WS_END, 0x00020000);
    const int idx = hh ? 0xEEEE : 0x4444;
    const unsigned parmask = load_parmask(p.ws);
    v8u yw[14];
    {
        const unsigned char* src = p.ws + OFF_YW + (size_t)(v * 8 + w) * 14 * 2048 + l * 16;
#pragma unroll
        for (int f = 0; f < 14; ++f) {
            const v4u a = ldg16(src + f * 2048), b = ldg16(src + f * 2048 + 1024);
            yw[f] = v8u{a.x, a.y, a.z, a.w, b.x, b.y, b.z, b.w};
        }
    }
    if (LAST) __builtin_amdgcn_s_setprio(2);
    const int moff = C_M2 + w * 1024 + hh * 512 + g * 128 + i8 * 16;
    const int mst_off = C_M + v * 256 + ((l >> 2) & 1) * 128 + (l >> 3) * 16 + (l & 3) * 4;
    const int soff = C_S + ((2 * w + hh) * 2 + (g >> 1)) * 128 + i8 * 16 + (g & 1) * 8;
    const int poff = C_P + ((2 * w + hh) * 2 + (g >> 1)) * 256 + i8 * 32 + (g & 1) * 16;
    const int yst_off = (int)OFF_YM + (v * 8 + w) * 512 + l * 8;
    const int uoff = (v * 8 + w) * 2048 + l * 16;
    const unsigned mlo = hh ? 0u : 0xFFFFFFFFu, mhi = ~mlo;
    unsigned spins = 0, mspin = 0;
    PROF_DECL
    int ho = hop_off(0, parmask);
    int ready = wait_ready<3>(rs, 3 * (32 + v), 0, mspin);
    v4u ua = __builtin_amdgcn_raw_buffer_load_b128(rs, uoff, (int)OFF_UF, 16);
    v4u ub = ua;
    for (int s = 0; s < NSEG; ++s) {
        const int hn = hop_off(s + 1, parmask);
        const int uo = (int)(OFF_UF + (s + 1) * SEGB);
        if (UNLIKELY(ready <= s + 1 && s + 1 < NSEG)) ready = wait_ready<3>(rs, 3 * (32 + v), s + 1, mspin);
        FENCE();
        STAMP(0);
        if constexpr (!LAST) {
            delay_cycles<DLY_V>();
            FENCE();
            v4u x;
            do { asm volatile("" ::: "memory"); x = __builtin_amdgcn_raw_buffer_load_b128(rs, moff, ho, 16); CNT(1); } while (!fresh(x) && ++mspin < SPIN_CAP);
            STAMP(1);
            *(LAS v4u*)(L + L_MST + w * 1024 + l * 16) = x;
        }
        BAR();
        STAMP(2);
        v4u mp[7];
#pragma unroll
        for (int f = 0; f < 7; ++f) mp[f] = *(const LAS v4u*)(L + L_MST + f * 1024 + l * 16);
        ub = __builtin_amdgcn_raw_buffer_load_b128(rs, uoff + 1024, uo - (int)SEGB, 16);
        FENCE();
        f32x4 y0 = {0.f, 0.f, 0.f, 0.f}, y1 = y0;
        v4u pv;
#pragma unroll
        for (int f = 0; f < 7; ++f) {
            if (f == PPOS) { FENCE(); pv = __builtin_amdgcn_raw_buffer_load_b128(rs, poff, ho, 16); FENCE(); }
            y0 = smfmac(mp[f], yw[f], y0, idx); y1 = smfmac(mp[f], yw[7 + f], y1, idx);
        }
        if (PPOS == 7) { FENCE(); pv = __builtin_amdgcn_raw_buffer_load_b128(rs, poff, ho, 16); FENCE(); }
        const v2u yop = v2u{pack2(fold32(y0[0], y0[2]), fold32(y0[1], y0[3])), pack2(fold32(y1[0], y1[2]), fold32(y1[1], y1[3]))};
        FENCE();
        STAMP(3);
        if (s == NSEG - 1) { __builtin_amdgcn_raw_buffer_store_b64(v2u{sane(yop.x), sane(yop.y)}, rs, yst_off, s * 224 * 512, 16); break; }
        delay_cycles<DLY_Q>();
        FENCE();
        const v4u pq = __builtin_amdgcn_raw_buffer_load_b128(rs, poff, ho + CH, 16);
        FENCE();
        v2u sv, sw2;
        if (!fresh(pv)) {
            pv = pq; CNT(1 << 20);
            while (!fresh(pv) && ++mspin < SPIN_CAP) {
                asm volatile("" ::: "memory");
                pv = __builtin_amdgcn_raw_buffer_load_b128(rs, poff, ho + CH, 16);
                CNT(1 << 20);
            }
        }
        STAMP(7);
        FENCE(); STAMP(6); sv = __builtin_amdgcn_raw_buffer_load_b64(rs, soff, ho, 16); FENCE();
        const f16x2 hm = __builtin_elementwise_max(__builtin_elementwise_max(__builtin_bit_cast(f16x2, pv.x), __builtin_bit_cast(f16x2, pv.y)),
                                                   __builtin_elementwise_max(__builtin_bit_cast(f16x2, pv.z), __builtin_bit_cast(f16x2, pv.w)));
        const float mx = xg_max_raw(vmax((float)hm.x, (float)hm.y));
        const unsigned e0 = pack2(ex2(subh_lo(pv.x, mx)), ex2(subh_hi(pv.x, mx))), e1 = pack2(ex2(subh_lo(pv.y, mx)), ex2(subh_hi(pv.y, mx)));
        FENCE(); sw2 = __builtin_amdgcn_raw_buffer_load_b64(rs, soff, ho + 4096, 16); FENCE();
        const unsigned e2 = pack2(ex2(subh_lo(pv.z, mx)), ex2(subh_hi(pv.z, mx))), e3 = pack2(ex2(subh_lo(pv.w, mx)), ex2(subh_hi(pv.w, mx)));
        const f32x4 z4 = {0.f, 0.f, 0.f, 0.f};
        const v4u eop = v4u{e0, e1, e2, e3};
        const f32x4 oa = mfma32(ua, eop, z4);
        const f32x4 ob = mfma32(ub, eop, z4);
        const f32x4 lx4 = mfma32(v4u{0x3F803F80u, 0x3F803F80u, 0x3F803F80u, 0x3F803F80u}, eop, z4);
        f32x4 ox = hh ? ob : oa;
        float lx = lx4[0];
        asm volatile("" : "+v"(ox), "+v"(lx));
        FENCE();
        STAMP(8);
        if (!__all(max(sv.x, sv.y) != 0xFFFFFFFFu)) {
            sv = sw2; CNT(1 << 10);
            while (!__all(max(sv.x, sv.y) != 0xFFFFFFFFu) && ++mspin < SPIN_CAP) {
                asm volatile("" ::: "memory");
                sv = __builtin_amdgcn_raw_buffer_load_b64(rs, soff, ho + 4096, 16);
                CNT(1 << 10);
            }
        }
        STAMP(9);
        const v2u sw = __builtin_amdgcn_permlane32_swap(sv.x, sv.y, false, false);
        const float t0 = addh_lo(sw.x, sw.y), t1 = addh_hi(sw.x, sw.y);
        float m;
        asm("v_max3_f32 %0, %1, %2, %3" : "=v"(m) : "v"(t0), "v"(t1), "v"(mx));
        m = xg_max_raw(m);
        const unsigned pk = pack2(ex2(t0 - m), ex2(t1 - m));
        const float beta = ex2(mx - m);
        const v2u bop = v2u{pk & mlo, pk & mhi};
        const f32x4 ls = mfma16(v2u{0x3F803F80u, 0x3F803F80u}, bop, f32x4{beta * lx, 0.f, 0.f, 0.f});
        f32x4 d = mfma16(yop, bop, ox * beta);
        const float r = __builtin_amdgcn_rcpf(ls[0]);
        d *= r;
        asm("s_nop 1\n\tv_add_f32_dpp %0, %0, %0 row_ror:8 row_mask:0xf bank_mask:0xf\n\tv_add_f32_dpp %1, %1, %1 row_ror:8 row_mask:0xf bank_mask:0xf\n\t"
            "v_add_f32_dpp %2, %2, %2 row_ror:8 row_mask:0xf bank_mask:0xf\n\tv_add_f32_dpp %3, %3, %3 row_ror:8 row_mask:0xf bank_mask:0xf"
            : "+v"(d[0]), "+v"(d[1]), "+v"(d[2]), "+v"(d[3]));
        *(LAS f32x2*)(L + L_RED + w * 512 + i8 * 64 + g * 16 + hh * 8) = hh ? f32x2{d[2], d[3]} : f32x2{d[0], d[1]};
        STAMP(10);
        FENCE();
        if constexpr (!LAST) ua = __builtin_amdgcn_raw_buffer_load_b128(rs, uoff, uo, 16);
        FENCE();
        BAR();
        STAMP(11);
        if constexpr (!LAST) delay_cycles<256>();
        if constexpr (!LAST) asm volatile("" :: "v"(sw2), "v"(pq));
        if constexpr (!LAST) __builtin_amdgcn_raw_buffer_store_b64(v2u{sane(yop.x), sane(yop.y)}, rs, yst_off, s * 224 * 512, 16);
        if constexpr (LAST) {
            f32x2 q[8];
#pragma unroll
            for (int k = 0; k < 8; ++k) q[k] = *(const LAS f32x2*)(L + L_RED + k * 512 + l * 8);
            FENCE();
            const f32x2 tot = ((q[0] + q[1]) + (q[2] + q[3])) + ((q[4] + q[5]) + (q[6] + q[7]));
            const unsigned mv = sane(pack2(tot[0], tot[1]));
            __builtin_amdgcn_raw_buffer_store_b32(mv, rs, mst_off, hn, 16);
            __builtin_amdgcn_raw_buffer_store_b32(mv, rs, mst_off, hn + CH, 16);
            __builtin_amdgcn_raw_buffer_store_b32(mv, rs, mst_off, hn + C_M2, 16);
            __builtin_amdgcn_raw_buffer_store_b32(mv, rs, mst_off, hn + 2 * CH, 16);
            __builtin_amdgcn_raw_buffer_store_b32(mv, rs, mst_off, hn + 3 * CH, 16);
            __builtin_amdgcn_raw_buffer_store_b32(mv, rs, mst_off, hn + C_M3, 16);
            FENCE();
            asm volatile("" :: "v"(sw2), "v"(pq));
            ua = __builtin_amdgcn_raw_buffer_load_b128(rs, uoff, uo, 16);
            __builtin_amdgcn_raw_buffer_store_b64(v2u{sane(yop.x), sane(yop.y)}, rs, yst_off, s * 224 * 512, 16);
        }
        ho = hn;
        if constexpr (LAST) PROF_ACC;
    }
    PROF_OUT(256 + v * 8 + w);
}

__device__ __forceinline__ void prefetch_wave(const Params& p, const int j, const int w) {
    const int l = threadIdx.x & 63;
    const __amdgpu_buffer_rsrc_t rs = __builtin_amdgcn_make_buffer_rsrc(p.ws, 0, (int)WS_END, 0x00020000);
    const unsigned parmask = load_parmask(p.ws);
    unsigned mspin = 0;
    if (w == 7) return;
    const int v = 7 * j + w;
    int ready = 0;
    for (int s = 1; s < NSEG; ++s) {
        if (s > PF_AHEAD) {
            const int ho = hop_off(s - PF_AHEAD, parmask) + C_M3;
            for (;;) {
                asm volatile("" ::: "memory");
                const unsigned x = __builtin_amdgcn_raw_buffer_load_b32(rs, v * 256, ho, 16);
                if (__builtin_amdgcn_readfirstlane(x) != 0xFFFFFFFFu || ++mspin >= SPIN_CAP) break;
                __builtin_amdgcn_s_sleep(8);
            }
        }
        if (ready <= s) ready = wait_ready<3>(rs, 3 * (32 + v), s, mspin);
        unsigned t[4];
#pragma unroll
        for (int k = 0; k < 4; ++k) {
            t[k] = __builtin_amdgcn_raw_buffer_load_b32(rs, v * 16384 + k * 4096 + l * 64, (int)(OFF_UF + s * SEGB), 16);
            FENCE();
            __builtin_amdgcn_s_sleep(PF_GAP);
        }
        asm volatile("" :: "v"(t[0]), "v"(t[1]), "v"(t[2]), "v"(t[3]));
    }
}

__device__ __forceinline__ void glds16(const void* gsrc, unsigned lds_dst) {
    unsigned keep;
    asm volatile("s_mov_b32 %0, m0\n\ts_mov_b32 m0, %2\n\ts_nop 0\n\tglobal_load_lds_dwordx4 %1, off\n\ts_mov_b32 m0, %0"
                 : "=&s"(keep) : "v"(gsrc), "s"(lds_dst) : "memory");
}
__device__ __forceinline__ v4u ldc16(const __amdgpu_buffer_rsrc_t rs, int voff, int soff) { return __builtin_amdgcn_raw_buffer_load_b128(rs, voff, soff, 16); }
__device__ __forceinline__ v4u pack8(f32x4 a, f32x4 b) { return v4u{pack2(a[0], a[1]), pack2(a[2], a[3]), pack2(b[0], b[1]), pack2(b[2], b[3])}; }
constexpr int L_XB = 0, XBUF = 28672;

__device__ __forceinline__ void sweep_and_serve(const Params& p, LAS unsigned char* L, const __amdgpu_buffer_rsrc_t rs, const int grp, const int r, const int w) {
    const int l = threadIdx.x & 63, n = l & 15, g = l >> 4;
    const unsigned lbase = (unsigned)(size_t)L;
    const unsigned char* wsrc;
    int obase, ostride, osec;
    bool gtype;
    float oscale = 1.0f;
    const int ks = 7 * (grp & 1) + w;
    if (grp < 32) {
        gtype = true;
        if (w < 7) { wsrc = p.ws + OFF_GW + (size_t)((grp >> 1) * 14 + ks) * 28672; obase = (int)OFF_GF + ((grp >> 1) * 28 + ks) * 1024; ostride = (int)SEGB; osec = 14336; }
        else { wsrc = p.ws + OFF_QW + (size_t)grp * 28672; obase = (int)OFF_QF + (grp >> 4) * (2048 * 1024) + (grp & 15) * 1024; ostride = 32768; osec = 16384;
               if (grp < 16) oscale = LOG2E * 0.18898223650461362f; }
    } else {
        gtype = false; osec = 1024;
        if (grp < 60) { wsrc = p.ws + OFF_YW + (size_t)((grp - 32) * 8 + w) * 28672; obase = (int)OFF_UF + ((grp - 32) * 8 + w) * 2048; ostride = (int)SEGB; }
        else { wsrc = p.ws + OFF_QW + (size_t)(32 + (grp - 60) * 8 + w) * 28672; obase = (int)OFF_VF + ((grp - 60) * 8 + w) * 2048; ostride = 32768; }
    }
    const int npc = w < 4 ? 4 : 3, pc0 = w < 4 ? 4 * w : 16 + 3 * (w - 4);
    const unsigned char* xsrc = p.ws + OFF_XP + pc0 * 1024 + l * 16;
    for (int k = 0; k < npc; ++k) glds16(xsrc + (size_t)r * XBUF + k * 1024, lbase + L_XB + (pc0 + k) * 1024);
    v4u wt[2][14];
#pragma unroll
    for (int f = 0; f < 14; ++f) { wt[0][f] = ldg16(wsrc + f * 1024 + l * 16); wt[1][f] = ldg16(wsrc + (14 + f) * 1024 + l * 16); }
#pragma unroll
    for (int f = 0; f < 14; ++f) asm volatile("" : "+v"(wt[0][f]), "+v"(wt[1][f]));
    asm volatile("s_waitcnt vmcnt(0)" ::: "memory");
    BAR();
    TLS(1);
    const int nseg = (66 - r) / 3;
    for (int i = 0; i < nseg; ++i) {
        const int seg = r + 3 * i;
        if (i + 1 < nseg) for (int k = 0; k < npc; ++k) glds16(xsrc + (size_t)(seg + 3) * XBUF + k * 1024, lbase + L_XB + ((i + 1) & 1) * XBUF + (pc0 + k) * 1024);
        const LAS unsigned char* xb = L + L_XB + (i & 1) * XBUF + l * 16;
        f32x4 a[2][2];
#pragma unroll
        for (int tau = 0; tau < 2; ++tau) {
            f32x4 a0 = {0.f, 0.f, 0.f, 0.f}, a1 = a0;
            if (gtype) {
#pragma unroll
                for (int f = 0; f < 14; ++f) { const v4u xf = *(const LAS v4u*)(xb + (tau * 14 + f) * 1024); a0 = mfma32(wt[0][f], xf, a0); a1 = mfma32(wt[1][f], xf, a1); }
            } else {
#pragma unroll
                for (int f = 0; f < 14; ++f) { const v4u xf = *(const LAS v4u*)(xb + (tau * 14 + f) * 1024); a0 = mfma32(xf, wt[0][f], a0); a1 = mfma32(xf, wt[1][f], a1); }
            }
            a[tau][0] = a0 * oscale; a[tau][1] = a1 * oscale;
        }
        const v4u o0 = gtype ? pack8(a[0][0], a[0][1]) : pack8(a[0][0], a[1][0]);
        const v4u o1 = gtype ? pack8(a[1][0], a[1][1]) : pack8(a[0][1], a[1][1]);
        __builtin_amdgcn_raw_buffer_store_b128(o0, rs, l * 16, obase + seg * ostride, 16);
        __builtin_amdgcn_raw_buffer_store_b128(o1, rs, l * 16, obase + seg * ostride + osec, 16);
        const bool drain = (i == 0 || i + 1 == nseg);
        if (drain) asm volatile("s_waitcnt vmcnt(0)" ::: "memory"); else asm volatile("s_waitcnt vmcnt(2)" ::: "memory");
        BAR();
        if (threadIdx.x == 0) __builtin_amdgcn_raw_buffer_store_b32((unsigned)(drain ? i + 1 : i), rs, CT_PROG + (grp * 3 + r) * 128, 0, 16);
        if (i == 0) TLS(2);
    }
    if (threadIdx.x == 0) __hip_atomic_fetch_add((GAS unsigned*)(p.ws + CT_DONE), 1u, __ATOMIC_RELAXED, __HIP_MEMORY_SCOPE_AGENT);
    TLS(3);
    if (!(grp < 32 && r == 0)) return;
    const unsigned parmask = load_parmask(p.ws);
    unsigned mspin = 0;
    const int mrow = 4 * ((n >> 2) & 1) + 2 * (n >> 3) + (n & 1);
    const LAS unsigned char* mrd = L + (g >> 1) * 128 + mrow * 16 + (g & 1) * 8;
    const u16* xr = p.x + (size_t)n * 448 + 32 * (w < 7 ? ks : 0) + 8 * g;
    v4u mx_ = ldc16(rs, (w < 7 ? w : 0) * 1024 + l * 16, hop_off(0, parmask) + C_M3);
    for (int s = 0; s < NSEG; ++s) {
        v4u xa, xb2;
        if (w < 7) {
            xa = ldg16(xr + (size_t)(32 * s) * 448); xb2 = ldg16(xr + (size_t)(32 * s + 16) * 448);
            const int ho = hop_off(s, parmask) + C_M3;
            while (!fresh(mx_) && ++mspin < SPIN_CAP) {
                __builtin_amdgcn_s_sleep(2);
                asm volatile("" ::: "memory");
                mx_ = ldc16(rs, w * 1024 + l * 16, ho);
            }
            *(LAS v4u*)(L + w * 1024 + l * 16) = mx_;
            FENCE();
            mx_ = ldc16(rs, w * 1024 + l * 16, hop_off(s + 1 < NSEG ? s + 1 : s, parmask) + C_M3);
        }
        BAR();
        if (w < 7) {
            v2u lo[14], hi[14];
#pragma unroll
            for (int f = 0; f < 14; ++f) {
                lo[f] = *(const LAS v2u*)(mrd + (f >> 1) * 1024 + (f & 1) * 256); hi[f] = *(const LAS v2u*)(mrd + (f >> 1) * 1024 + (f & 1) * 256 + 512);
            }
            FENCE();
            f32x4 a0 = {0.f, 0.f, 0.f, 0.f}, a1 = a0;
#pragma unroll
            for (int f = 0; f < 14; ++f) {
                const v4u mf = v4u{lo[f].x, hi[f].x, lo[f].y, hi[f].y};
                a0 = mfma32(wt[0][f], mf, a0); a1 = mfma32(wt[1][f], mf, a1);
            }
            const v4u gm = pack8(a0, a1);
            const f32x4 z4 = {0.f, 0.f, 0.f, 0.f};
            const f32x4 d0 = mfma32(gm, xa, z4), d1 = mfma32(gm, xb2, z4);
            *(LAS f32x4*)(L + 8192 + w * 1024 + l * 16) = f32x4{d0[0], d0[1], d1[0], d1[1]};
        }
        BAR();
        TLS(8 + s);
        if (w == 7) {
            f32x4 q[7];
#pragma unroll
            for (int k = 0; k < 7; ++k) q[k] = *(const LAS f32x4*)(L + 8192 + k * 1024 + l * 16);
            const f32x4 t = ((q[0] + q[1]) + (q[2] + q[3])) + ((q[4] + q[5]) + q[6]);
            __builtin_amdgcn_raw_buffer_store_b128(v4u{sane(fbits(t[0])), sane(fbits(t[1])), sane(fbits(t[2])), sane(fbits(t[3]))}, rs, l * 16,
                                                   (int)OFF_SXM + (s * 32 + grp) * 1024, 16);
        }
    }
}

__device__ __forceinline__ void attn_item(const Params& p, const __amdgpu_buffer_rsrc_t rs, const int item) {
    const int l = threadIdx.x & 63, n = l & 15, g = l >> 4;
    const int rt = item >> 4, h = item & 15;
    const int s = rt >> 1, q = rt * 16 + n;
    const v4u qf = ldc16(rs, l * 16, (int)OFF_QF + (rt * 16 + h) * 1024);
    f32x4 sc[18];
    const f32x4 z4 = {0.f, 0.f, 0.f, 0.f};
    const float NEG = -3.0e38f;
    v4u kf[18];
#pragma unroll
    for (int k = 0; k < 18; ++k) { const int kt = max(2 * (s - 8) + k, 0); kf[k] = ldc16(rs, l * 16, (int)OFF_KF + (kt * 16 + h) * 1024); }
#pragma unroll
    for (int k = 0; k < 18; ++k) {
        const int kt = 2 * (s - 8) + k;
        f32x4 a = mfma32(kf[k], qf, z4);
        if (k < 2 || k >= 16) {
#pragma unroll
            for (int r = 0; r < 4; ++r) {
                const int dq = q - (16 * kt + 4 * g + r);
                a[r] = (dq >= 0 && dq < 256) ? a[r] : NEG;
            }
        }
        sc[k] = kt >= 0 ? a : f32x4{NEG, NEG, NEG, NEG};
    }
    FENCE();
    v4u vf[18];
#pragma unroll
    for (int k = 0; k < 18; ++k) { const int b = max(s - 8 + (k >> 1), 0); vf[k] = ldc16(rs, l * 16, (int)OFF_VF + ((b * 16 + h) * 2 + (k & 1)) * 1024); }
    float m = NEG;
#pragma unroll
    for (int k = 0; k < 18; ++k) m = fmaxf(m, fmaxf(fmaxf(sc[k][0], sc[k][1]), fmaxf(sc[k][2], sc[k][3])));
    m = xg_max(m);
    float sum = 0.f;
    f32x4 o0 = z4, o1 = z4;
#pragma unroll
    for (int blk = 0; blk < 9; ++blk) {
        f32x4 e0, e1;
#pragma unroll
        for (int r = 0; r < 4; ++r) { e0[r] = ex2(sc[2 * blk][r] - m); e1[r] = ex2(sc[2 * blk + 1][r] - m); }
        sum += ((e0[0] + e0[1]) + (e0[2] + e0[3])) + ((e1[0] + e1[1]) + (e1[2] + e1[3]));
        const v4u pb = pack8(e0, e1);
        o0 = mfma32(vf[2 * blk], pb, o0);
        o1 = mfma32(vf[2 * blk + 1], pb, o1);
    }
    sum = xg_sum(sum);
    const float f = ((const float*)(p.ws + OFF_GATE))[q] / sum;
    __builtin_amdgcn_raw_buffer_store_b128(pack8(o0 * f, o1 * f), rs, l * 16, (int)OFF_OF + (rt * 16 + h) * 1024, 16);
    asm volatile("s_waitcnt vmcnt(0)" ::: "memory");
    if (l == 0) __hip_atomic_fetch_add((GAS unsigned*)(p.ws + CT_OFC + s * 128), 1u, __ATOMIC_RELAXED, __HIP_MEMORY_SCOPE_AGENT);
}

__device__ __forceinline__ void out_item(const Params& p, LAS unsigned char* L, const __amdgpu_buffer_rsrc_t rs, const int s, const int q4, const int w, unsigned& mspin) {
    const int l = threadIdx.x & 63, n = l & 15, g = l >> 4;
    const f32x4 z4 = {0.f, 0.f, 0.f, 0.f};
    const float NEG = -3.0e38f;
    const int v = 7 * q4 + (w < 7 ? w : 0);
    f32x4 sxx[2][3];
#pragma unroll
    for (int a = 0; a < 2; ++a) { sxx[a][0] = z4; sxx[a][1] = z4; sxx[a][2] = z4; }
    {
        const int gp = (int)OFF_GF + s * (int)SEGB + (2 * w) * 28 * 1024;
        const u16* xr = p.x + (size_t)(32 * s + n) * 448 + 8 * g;
#pragma unroll 1
        for (int kh = 0; kh < 2; ++kh) {
            v4u x0[7], x1[7], kk[4][7];
#pragma unroll
            for (int j = 0; j < 7; ++j) {
                const int ks = 7 * kh + j;
                x0[j] = ldg16(xr + 32 * ks); x1[j] = ldg16(xr + 16 * 448 + 32 * ks);
#pragma unroll
                for (int t = 0; t < 4; ++t) kk[t][j] = ldc16(rs, l * 16, gp + (t * 14 + ks) * 1024);
            }
            FENCE();
#pragma unroll
            for (int j = 0; j < 7; ++j)
#pragma unroll
                for (int a = 0; a < 2; ++a) {
                    sxx[a][0] = mfma32(kk[2 * a][j], x0[j], sxx[a][0]);
                    sxx[a][1] = mfma32(kk[2 * a][j], x1[j], sxx[a][1]);
                    sxx[a][2] = mfma32(kk[2 * a + 1][j], x1[j], sxx[a][2]);
                }
            FENCE();
        }
    }
    if (w == 0) {
        for (;;) {
            asm volatile("" ::: "memory");
            const unsigned c = __builtin_amdgcn_raw_buffer_load_b32(rs, CT_OFC + s * 128, 0, 16);
            if (__builtin_amdgcn_readfirstlane(c) >= 32u || ++mspin >= SPIN_CAP) break;
            __builtin_amdgcn_s_sleep(4);
        }
    }
    BAR();
    f32x4 o0 = z4, o1 = z4;
    {
        const unsigned char* wp = p.ws + OFF_WOF + (size_t)(v * 16) * 1024 + l * 16;
#pragma unroll 1
        for (int hb = 0; hb < 16; hb += 8) {
            v4u wf[8], oa[8], ob[8];
#pragma unroll
            for (int h = 0; h < 8; ++h) {
                wf[h] = ldg16(wp + (hb + h) * 1024);
                oa[h] = ldc16(rs, l * 16, (int)OFF_OF + (2 * s * 16 + hb + h) * 1024);
                ob[h] = ldc16(rs, l * 16, (int)OFF_OF + ((2 * s + 1) * 16 + hb + h) * 1024);
            }
            FENCE();
#pragma unroll
            for (int h = 0; h < 8; ++h) { o0 = mfma32(wf[h], oa[h], o0); o1 = mfma32(wf[h], ob[h], o1); }
            FENCE();
        }
    }
    v4u uf[16];
#pragma unroll
    for (int h = 0; h < 16; ++h) uf[h] = ldc16(rs, l * 16, (int)OFF_UF + s * (int)SEGB + (v * 16 + h) * 1024);
    const float gate0 = 1.0f - ((const float*)(p.ws + OFF_GATE))[32 * s + n], gate1 = 1.0f - ((const float*)(p.ws + OFF_GATE))[32 * s + 16 + n];
    float mxx[2][2];
#pragma unroll
    for (int a = 0; a < 2; ++a) {
#pragma unroll
        for (int r = 0; r < 4; ++r) { sxx[a][0][r] = (4 * g + r <= n) ? sxx[a][0][r] : NEG; sxx[a][2][r] = (4 * g + r <= n) ? sxx[a][2][r] : NEG; }
        mxx[a][0] = fmaxf(fmaxf(sxx[a][0][0], sxx[a][0][1]), fmaxf(sxx[a][0][2], sxx[a][0][3]));
        mxx[a][1] = fmaxf(fmaxf(fmaxf(sxx[a][1][0], sxx[a][1][1]), fmaxf(sxx[a][1][2], sxx[a][1][3])), fmaxf(fmaxf(sxx[a][2][0], sxx[a][2][1]), fmaxf(sxx[a][2][2], sxx[a][2][3])));
    }
    FENCE();
    TLS(s < 40 ? 8 : 10);
    v4u sm[4];
    for (;;) {
        asm volatile("" ::: "memory");
#pragma unroll
        for (int k = 0; k < 4; ++k) sm[k] = ldc16(rs, l * 16, (int)OFF_SXM + (s * 32 + 4 * w + k) * 1024);
        if (fresh(__builtin_elementwise_max(__builtin_elementwise_max(sm[0], sm[1]), __builtin_elementwise_max(sm[2], sm[3]))) || ++mspin >= SPIN_CAP) break;
        __builtin_amdgcn_s_sleep(6);
    }
    TLS(s < 40 ? 9 : 11);
    v2u yf[8];
#pragma unroll
    for (int k = 0; k < 8; ++k) yf[k] = __builtin_amdgcn_raw_buffer_load_b64(rs, l * 8, (int)OFF_YM + (s * 224 + v * 8 + k) * 512, 16);
    unsigned px[2][2][4]; unsigned pm[2][2];
#pragma unroll
    for (int qt = 0; qt < 2; ++qt) {
#pragma unroll
        for (int a = 0; a < 2; ++a) {
            const float t0 = bitsf(sm[2 * a][2 * qt]) + bitsf(sm[2 * a + 1][2 * qt]), t1 = bitsf(sm[2 * a][2 * qt + 1]) + bitsf(sm[2 * a + 1][2 * qt + 1]);
            const float m = xg_max(fmaxf(mxx[a][qt], fmaxf(t0, t1)));
            const f32x4 dg = sxx[a][qt == 0 ? 0 : 2];
            float ed[4], ef[4];
            const float e0 = ex2(t0 - m), e1 = ex2(t1 - m);
            float sum = e0 + e1;
#pragma unroll
            for (int r = 0; r < 4; ++r) {
                ed[r] = ex2(dg[r] - m); sum += ed[r];
                if (qt == 1) { ef[r] = ex2(sxx[a][1][r] - m); sum += ef[r]; }
            }
            sum = xg_sum(sum);
            const float f = (qt == 0 ? gate0 : gate1) * __builtin_amdgcn_rcpf(sum);
            if (qt == 0) { px[a][0][0] = pack2(ed[0] * f, ed[1] * f); px[a][0][1] = pack2(ed[2] * f, ed[3] * f); }
            else {
                px[a][1][0] = pack2(ef[0] * f, ef[1] * f); px[a][1][1] = pack2(ef[2] * f, ef[3] * f);
                px[a][1][2] = pack2(ed[0] * f, ed[1] * f); px[a][1][3] = pack2(ed[2] * f, ed[3] * f);
            }
            pm[qt][a] = pack2(e0 * f, e1 * f);
        }
    }
    *(LAS v4u*)(L + w * 4096 + l * 16) = v4u{px[0][0][0], px[0][0][1], px[1][0][0], px[1][0][1]};
    *(LAS v4u*)(L + w * 4096 + 1024 + l * 16) = v4u{px[0][1][0], px[0][1][1], px[0][1][2], px[0][1][3]};
    *(LAS v4u*)(L + w * 4096 + 2048 + l * 16) = v4u{px[1][1][0], px[1][1][1], px[1][1][2], px[1][1][3]};
    *(LAS v4u*)(L + w * 4096 + 3072 + l * 16) = v4u{pm[0][0], pm[0][1], pm[1][0], pm[1][1]};
    for (;;) {
        unsigned mm = 0u;
#pragma unroll
        for (int k = 0; k < 8; ++k) mm = max(mm, max(yf[k].x, yf[k].y));
        if (__all(mm != 0xFFFFFFFFu) || ++mspin >= SPIN_CAP) break;
        asm volatile("" ::: "memory");
#pragma unroll
        for (int k = 0; k < 8; ++k) yf[k] = __builtin_amdgcn_raw_buffer_load_b64(rs, l * 8, (int)OFF_YM + (s * 224 + v * 8 + k) * 512, 16);
    }
    BAR();
    if (w < 7) {
#pragma unroll
        for (int pp = 0; pp < 4; ++pp) {
            v4u pmm[2];
#pragma unroll
            for (int z = 0; z < 2; ++z) {
                const int pr = 2 * pp + z;
                const v4u u0 = uf[2 * pr], u1 = uf[2 * pr + 1];
                const v4u pa = *(const LAS v4u*)(L + pr * 4096 + l * 16), pb = *(const LAS v4u*)(L + pr * 4096 + 1024 + l * 16),
                          pc = *(const LAS v4u*)(L + pr * 4096 + 2048 + l * 16);
                pmm[z] = *(const LAS v4u*)(L + pr * 4096 + 3072 + l * 16);
                o1 = mfma32(u0, pb, o1);
                o0 = mfma32(v4u{u0.x, u0.y, u1.x, u1.y}, pa, o0);
                o1 = mfma32(u1, pc, o1);
            }
            const v4u yy = v4u{yf[2 * pp].x, yf[2 * pp].y, yf[2 * pp + 1].x, yf[2 * pp + 1].y};
            o0 = mfma32(yy, v4u{pmm[0].x, pmm[0].y, pmm[1].x, pmm[1].y}, o0);
            o1 = mfma32(yy, v4u{pmm[0].z, pmm[0].w, pmm[1].z, pmm[1].w}, o1);
        }
        stg8(p.out + (size_t)(32 * s + n) * 448 + 16 * v + 4 * g, v2u{pack2(o0[0], o0[1]), pack2(o0[2], o0[3])});
        stg8(p.out + (size_t)(32 * s + 16 + n) * 448 + 16 * v + 4 * g, v2u{pack2(o1[0], o1[1]), pack2(o1[2], o1[3])});
    }
}

__global__ void __launch_bounds__(512, 2) k_main(Params p) {
    extern __shared__ __attribute__((aligned(16))) unsigned char lds_[];
    p.ws = align_ws(p.ws);
    const int rot = __builtin_amdgcn_readfirstlane(((const int*)(p.ws + OFF_CTL))[8]);
    const int grp = (blockIdx.x + rot) & 7, u = blockIdx.x >> 3;
    LAS unsigned char* L = (LAS unsigned char*)lds_;
    const int w = __builtin_amdgcn_readfirstlane(threadIdx.x >> 6);
    TLS(0);
    if (grp == 0) {
        if (w < 7) score_wave<false>(p, L, u, w); else score_wave<true>(p, L, u, w);
        TLS(7);
        return;
    }
    if (grp == 1) {
        if (u < 28) { if (w < 7) value_wave<false>(p, L, u, w); else value_wave<true>(p, L, u, w); }
        else prefetch_wave(p, u - 28, w);
        TLS(7);
        return;
    }
    const __amdgpu_buffer_rsrc_t rs = __builtin_amdgcn_make_buffer_rsrc(p.ws, 0, (int)WS_END, 0x00020000);
    const int k = (grp - 2) * 32 + u;
    if (k < 186) {
        sweep_and_serve(p, L, rs, k / 3, k % 3, w);
        if (k < 96 && k % 3 == 0) { TLS(7); return; }
    }
    const int wk = k < 96 ? (k / 3) * 2 + (k % 3 - 1) : k - 32;
    unsigned mspin = 0;
    if (w == 0) {
        for (;;) {
            asm volatile("" ::: "memory");
            const unsigned c = __builtin_amdgcn_raw_buffer_load_b32(rs, CT_DONE, 0, 16);
            if (__builtin_amdgcn_readfirstlane(c) >= 186u || ++mspin >= SPIN_CAP) break;
            __builtin_amdgcn_s_sleep(16);
        }
    }
    BAR();
    TLS(4);
    for (int it = wk * 8 + w; it < 2048; it += 1280) attn_item(p, rs, it);
    TLS(5);
    for (int it = wk; it < 256; it += 160) { out_item(p, L, rs, it >> 2, it & 3, w, mspin); TLS(it < 160 ? 6 : 7); }
}

extern "C" void kernel_launch(void* const* d_in, const int* in_sizes, int n_in, void* d_out, int out_size, void* d_ws, size_t ws_size,
                              hipStream_t stream) {
    Params p;
    p.x = (const u16*)d_in[0]; p.wqkv = (const u16*)d_in[1]; p.wo = (const u16*)d_in[2]; p.mem0 = (const u16*)d_in[4];
    p.Wq = (const u16*)d_in[5]; p.Wk = (const u16*)d_in[6]; p.Wv = (const u16*)d_in[7]; p.Wo = (const u16*)d_in[8];
    p.gate_w = (const u16*)d_in[10];
    p.out = (u16*)d_out; p.ws = (unsigned char*)d_ws;
    (void)hipFuncSetAttribute((const void*)k_prep, hipFuncAttributeMaxDynamicSharedMemorySize, K1_LDS);
    (void)hipFuncSetAttribute((const void*)k_main, hipFuncAttributeMaxDynamicSharedMemorySize, L_REC_END);
    k_prep<<<256, 512, K1_LDS, stream>>>(p);
    k_main<<<256, 512, L_REC_END, stream>>>(p);
}
```

```cpp
#include <hip/hip_runtime.h>
#include <hip/hip_bf16.h>
#include <stdint.h>

#define GAS __attribute__((address_space(1)))
#define LAS __attribute__((address_space(3)))
typedef __bf16 bf16x2 __attribute__((ext_vector_type(2)));
typedef __bf16 bf16x4 __attribute__((ext_vector_type(4)));
typedef __bf16 bf16x8 __attribute__((ext_vector_type(8)));
typedef __bf16 bf16x16 __attribute__((ext_vector_type(16)));
typedef short s16x4 __attribute__((ext_vector_type(4)));
typedef float f32x2 __attribute__((ext_vector_type(2)));
typedef float f32x4 __attribute__((ext_vector_type(4)));
typedef unsigned v2u __attribute__((ext_vector_type(2)));
typedef unsigned v4u __attribute__((ext_vector_type(4)));
typedef unsigned v8u __attribute__((ext_vector_type(8)));
typedef unsigned short u16;

constexpr int S_ = 2048, D_ = 448, H_ = 16, NSEG = 64;
constexpr float LOG2E = 1.4426950408889634f;

constexpr size_t SZ_W = (size_t)16 * 28 * 14 * 1024;
constexpr size_t OFF_HOP = 0;
constexpr int PAGE = 2 << 20, CH = 16384, NCHUNK = 9, STEPB = NCHUNK * CH, NPAGE = 5, SPP = 14;
constexpr int C_M = 0, C_M2 = 4 * CH, C_M3 = 5 * CH, C_S = 6 * CH, C_P = 7 * CH;
constexpr size_t HOP_BYTES = (size_t)NPAGE * PAGE;
constexpr size_t OFF_CTL = OFF_HOP + HOP_BYTES;
constexpr size_t CTL_BYTES = 36864;
constexpr int CT_DONE = (int)OFF_CTL + 256, CT_PZ = (int)OFF_CTL + 384, CT_OFC = (int)OFF_CTL + 512, CT_PROG = CT_OFC + 64 * 128;
constexpr size_t OFF_TW = OFF_CTL + CTL_BYTES;
constexpr size_t OFF_YW = OFF_TW + SZ_W;
constexpr size_t OFF_QW = OFF_YW + SZ_W;
constexpr size_t OFF_VW = OFF_QW + (size_t)32 * 28 * 1024;
constexpr size_t OFF_WOF = OFF_VW + (size_t)16 * 28 * 1024;
constexpr size_t OFF_XP = OFF_WOF + (size_t)28 * 16 * 1024;
constexpr size_t OFF_GATE = OFF_XP + (size_t)128 * 14 * 1024;
constexpr size_t SEGB = (size_t)448 * 1024;
constexpr size_t OFF_GF = OFF_GATE + 8192;
constexpr size_t OFF_UF = OFF_GF + 64 * SEGB;
constexpr size_t OFF_QF = OFF_UF + 64 * SEGB;
constexpr size_t OFF_KF = OFF_QF + (size_t)2048 * 1024;
constexpr size_t OFF_VF = OFF_KF + (size_t)2048 * 1024;
constexpr size_t OFF_OF = OFF_VF + (size_t)2048 * 1024;
constexpr size_t OFF_SXM = OFF_OF + (size_t)2048 * 1024;
constexpr size_t OFF_YM = OFF_SXM + (size_t)64 * 32 * 1024;
constexpr size_t WS_END = OFF_YM + (size_t)64 * 224 * 512;

struct Params {
    const u16* x; const u16* wqkv; const u16* wo; const u16* mem0;
    const u16* Wq; const u16* Wk; const u16* Wv; const u16* Wo; const u16* gate_w;
    u16* out; unsigned char* ws;
};
__device__ __forceinline__ unsigned char* align_ws(unsigned char* w) {
    return w + ((0 - (size_t)w) & (size_t)(PAGE - 1));
}
__device__ __forceinline__ unsigned xcc_id() { return (unsigned)__builtin_amdgcn_s_getreg(20 | (3 << 11)) & 0xfu; }
__device__ __forceinline__ unsigned load_parmask(const unsigned char* ws) {
    const unsigned* c = (const unsigned*)(ws + OFF_CTL);
    unsigned m = 0;
#pragma unroll
    for (int k = 0; k < NPAGE; ++k) m |= (c[k] & 1u) << k;
    return __builtin_amdgcn_readfirstlane(m);
}
__device__ __forceinline__ int hop_off(int s, unsigned parmask) {
    const int pg = s / SPP, sl = s - pg * SPP;
    return pg * PAGE + sl * STEPB + (int)((parmask >> pg) & 1u) * 8192;
}

__device__ __forceinline__ unsigned pack2(float a, float b) {
    return __builtin_bit_cast(unsigned, __builtin_convertvector(f32x2{a, b}, bf16x2));
}
__device__ __forceinline__ float bf2f(u16 v) { return __builtin_bit_cast(float, (unsigned)v << 16); }
__device__ __forceinline__ f32x4 mfma32(v4u a, v4u b, f32x4 c) {
    return __builtin_amdgcn_mfma_f32_16x16x32_bf16(__builtin_bit_cast(bf16x8, a), __builtin_bit_cast(bf16x8, b), c, 0, 0, 0);
}
__device__ __forceinline__ f32x4 mfma16(v2u a, v2u b, f32x4 c) {
    return __builtin_amdgcn_mfma_f32_16x16x16bf16_1k(__builtin_bit_cast(s16x4, a), __builtin_bit_cast(s16x4, b), c, 0, 0, 0);
}
__device__ __forceinline__ f32x4 smfmac(v4u a, v8u b, f32x4 c, int idx) {
    return __builtin_amdgcn_smfmac_f32_16x16x64_bf16(__builtin_bit_cast(bf16x8, a), __builtin_bit_cast(bf16x16, b), c, idx, 0, 0);
}
__device__ __forceinline__ v4u ldg16(const void* p) { return *(const GAS v4u*)p; }
__device__ __forceinline__ v2u ldg8(const void* p) { return *(const GAS v2u*)p; }
__device__ __forceinline__ void stg16(void* p, v4u v) { *(GAS v4u*)p = v; }
__device__ __forceinline__ void stg8(void* p, v2u v) { *(GAS v2u*)p = v; }
__device__ __forceinline__ v2u tr_read(const LAS void* p) {
    return __builtin_bit_cast(v2u, __builtin_amdgcn_ds_read_tr16_b64_v4i16((LAS s16x4*)p));
}
__device__ __forceinline__ float ror8(float v) {
    return __builtin_bit_cast(float, __builtin_amdgcn_update_dpp(0, __builtin_bit_cast(int, v), 0x128, 0xf, 0xf, false));
}
__device__ __forceinline__ unsigned fbits(float v) { return __builtin_bit_cast(unsigned, v); }
__device__ __forceinline__ float bitsf(unsigned v) { return __builtin_bit_cast(float, v); }
__device__ __forceinline__ float xg_max(float v) {
    const v2u a = __builtin_amdgcn_permlane16_swap(fbits(v), fbits(v), false, false);
    v = fmaxf(bitsf(a.x), bitsf(a.y));
    const v2u b = __builtin_amdgcn_permlane32_swap(fbits(v), fbits(v), false, false);
    return fmaxf(bitsf(b.x), bitsf(b.y));
}
__device__ __forceinline__ float xg_sum(float v) {
    const v2u a = __builtin_amdgcn_permlane16_swap(fbits(v), fbits(v), false, false);
    v = bitsf(a.x) + bitsf(a.y);
    const v2u b = __builtin_amdgcn_permlane32_swap(fbits(v), fbits(v), false, false);
    return bitsf(b.x) + bitsf(b.y);
}
__device__ __forceinline__ float fold32(float a, float b) {
    const v2u r = __builtin_amdgcn_permlane32_swap(fbits(a), fbits(b), false, false);
    return bitsf(r.x) + bitsf(r.y);
}
__device__ __forceinline__ float ex2(float v) { return __builtin_amdgcn_exp2f(v); }
__device__ __forceinline__ unsigned sane(unsigned v) { return v < 0xFFFFFFFEu ? v : 0xFFFFFFFEu; }

__device__ __forceinline__ int porder(int f, int g, int e) {
    return 64 * (f >> 1) + 16 * (f & 1) + 4 * g + 32 * ((e >> 1) & 1) + 2 * (e >> 2) + (e & 1);
}
constexpr int NU_HOP = SPP * NCHUNK * 16;
template <bool MIDBAR>
__device__ __forceinline__ void misc_wave(const Params& p, const int mw, const int l) {
    const int n = l & 15, g = l >> 4;
    v2u xl[4], xh[4];
#pragma unroll
    for (int k = 0; k < 4; ++k) {
        const int i = mw * 4 + k, rt = i / 14, f = i % 14;
        const u16* row = p.x + (size_t)(rt * 16 + n) * 448 + 64 * (f >> 1) + 16 * (f & 1) + 4 * g;
        xl[k] = ldg8(row); xh[k] = ldg8(row + 32);
    }
    unsigned qv[3][8];
    bool qok[3];
#pragma unroll
    for (int k = 0; k < 3; ++k) {
        const int i = mw * 3 + k;
        const bool isv = i >= 896;
        const int fr = isv ? i - 896 : i;
        const int unit = fr / 28, t = (fr % 28) / 14, f = fr % 14;
        const int d = isv ? 16 * t + n : 8 * (n >> 2) + 4 * t + (n & 3);
        const int col = (isv ? 896 + unit * 28 : (unit >> 4) * 448 + (unit & 15) * 28) + (d < 28 ? d : 0);
        qok[k] = d < 28;
#pragma unroll
        for (int e = 0; e < 8; ++e) qv[k][e] = p.wqkv[(size_t)porder(f, g, e) * 1344 + col];
    }
    unsigned wv[8];
#pragma unroll
    for (int e = 0; e < 8; ++e) {
        const int d = (e < 4) ? 4 * g + e : 16 + 4 * g + e - 4;
        wv[e] = p.wo[(size_t)((mw & 15) * 28 + (d < 28 ? d : 0)) * 448 + 16 * (mw >> 4) + n];
        if (d >= 28) wv[e] = 0u;
    }
    v4u gxv[7], ggv[7];
    const int row = (mw & 255) * 8 + (l >> 3), c0 = (l & 7) * 8;
#pragma unroll
    for (int k = 0; k < 7; ++k) { gxv[k] = ldg16(p.x + (size_t)row * 448 + k * 64 + c0); ggv[k] = ldg16(p.gate_w + k * 64 + c0); }
    for (int i = mw; i < NU_HOP; i += 448) {
        const int st = i / (NCHUNK * 16), ck = (i >> 4) % NCHUNK, kb = i & 15;
        v4u val = v4u{0xFFFFFFFFu, 0xFFFFFFFFu, 0xFFFFFFFFu, 0xFFFFFFFFu};
        if (st == 0 && ck < 6 && (kb & 7) < 7) {
            const int im = (kb & 7) * 64 + l;
            const v4u m = ldg16(p.mem0 + (size_t)(im & 7) * 448 + (im >> 3) * 8);
            val = v4u{sane(m.x), sane(m.y), sane(m.z), sane(m.w)};
        }
        stg16(p.ws + OFF_HOP + (size_t)hop_off(st, 0u) + ck * CH + kb * 1024 + l * 16, val);
    }
    if (MIDBAR) { __builtin_amdgcn_sched_barrier(0); __builtin_amdgcn_s_barrier(); __builtin_amdgcn_sched_barrier(0); }
    static_assert(SPP * 32 <= 448, "one KiB of the first page's S_xm slots per wave");
    if (mw < SPP * 32) stg16(p.ws + OFF_SXM + (size_t)mw * 1024 + l * 16, v4u{0xFFFFFFFFu, 0xFFFFFFFFu, 0xFFFFFFFFu, 0xFFFFFFFFu});
    if (mw < 224) stg16(p.ws + OFF_YM + (size_t)62 * 224 * 512 + (size_t)mw * 1024 + l * 16, v4u{0xFFFFFFFFu, 0xFFFFFFFFu, 0xFFFFFFFFu, 0xFFFFFFFFu});
#pragma unroll
    for (int k = 0; k < 4; ++k) stg16(p.ws + OFF_XP + (size_t)(mw * 4 + k) * 1024 + l * 16, v4u{xl[k].x, xh[k].x, xl[k].y, xh[k].y});
#pragma unroll
    for (int k = 0; k < 3; ++k) {
        const v4u o = v4u{qv[k][0] | (qv[k][1] << 16), qv[k][2] | (qv[k][3] << 16), qv[k][4] | (qv[k][5] << 16), qv[k][6] | (qv[k][7] << 16)};
        stg16(p.ws + OFF_QW + (size_t)(mw * 3 + k) * 1024 + l * 16, qok[k] ? o : v4u{0u, 0u, 0u, 0u});
    }
    stg16(p.ws + OFF_WOF + (size_t)mw * 1024 + l * 16, v4u{wv[0] | (wv[1] << 16), wv[2] | (wv[3] << 16), wv[4] | (wv[5] << 16), wv[6] | (wv[7] << 16)});
    float s = 0.f;
#pragma unroll
    for (int k = 0; k < 7; ++k)
#pragma unroll
        for (int j = 0; j < 4; ++j) {
            s += bitsf(gxv[k][j] << 16) * bitsf(ggv[k][j] << 16);
            s += bitsf(gxv[k][j] & 0xFFFF0000u) * bitsf(ggv[k][j] & 0xFFFF0000u);
        }
    s += __shfl_xor(s, 1); s += __shfl_xor(s, 2); s += __shfl_xor(s, 4);
    if ((l & 7) == 0 && mw < 256) ((float*)(p.ws + OFF_GATE))[row] = 1.0f / (1.0f + __expf(-s));
}

constexpr int K1_RS = 528, K1_OS = 144, K1_IMG = 36864, K1_IS = 136, K1_LDS = K1_IMG + 7 * 64 * K1_IS;
#define PT(k) do {} while (0)
#define PT0(k) do {} while (0)
template <int mat>
__device__ __forceinline__ void prep_heavy(const Params& p, LAS unsigned char* L, const int h, const int blk, const int w) {
    const int b = blockIdx.x, tid = threadIdx.x, l = tid & 63, n = l & 15, g = l >> 4;
    v4u t[4];
    if (mat == 0) {
#pragma unroll
        for (int k = 0; k < 4; ++k) { const int u = tid + 512 * k; t[k] = ldg16(p.Wq + ((size_t)(64 * blk + (u >> 5)) * 16 + h) * 256 + (u & 31) * 8); }
    } else {
#pragma unroll
        for (int k = 0; k < 4; ++k) { const int u = tid + 512 * k; t[k] = ldg16(p.Wo + ((size_t)h * 256 + (u >> 3)) * 448 + 64 * blk + (u & 7) * 8); }
    }
    __builtin_amdgcn_sched_barrier(0);
    const int rs_ = mat == 0 ? K1_RS : K1_OS, sh = mat == 0 ? 5 : 3;
    if (w == 7) {
#pragma unroll
        for (int k = 0; k < 4; ++k) { const int u = tid + 512 * k; *(LAS v4u*)(L + (u >> sh) * rs_ + (u & ((1 << sh) - 1)) * 16) = t[k]; }
        asm volatile("s_waitcnt lgkmcnt(0)" ::: "memory");
        misc_wave<true>(p, 224 + b, l);
        PT(3);
        return;
    }
    v4u cr[4][8];
    LAS unsigned char* img = L + K1_IMG + w * 64 * K1_IS;
    {
        const __amdgpu_buffer_rsrc_t wr = __builtin_amdgcn_make_buffer_rsrc((void*)(mat == 0 ? p.Wk : p.Wv), 0, 448 * 4096 * 2, 0x00020000);
        const int vo = ((64 * w + (l >> 5)) * 16 + h) * 512 + (l & 31) * 16;
#define K1_LOAD(tt) do { _Pragma("unroll") for (int j = 0; j < 8; ++j) \
            cr[tt][j] = __builtin_amdgcn_raw_buffer_load_b128(wr, vo, (16 * ((tt) >> 1) + 2 * ((tt) & 1) + 4 * (j >> 1) + 32 * (j & 1)) * 8192, 0); \
            __builtin_amdgcn_sched_barrier(0); } while (0)
#define K1_TURN(tt) do { \
        _Pragma("unroll") for (int j = 0; j < 8; ++j) *(LAS v4u*)(img + (2 * j + (l >> 5)) * K1_RS + (l & 31) * 16) = cr[tt][j]; \
        asm volatile("" ::: "memory"); \
        _Pragma("unroll") for (int es = 0; es < 8; ++es) cr[tt][es] = *(const LAS v4u*)(img + n * K1_RS + es * 64 + g * 16); \
        asm volatile("" ::: "memory"); __builtin_amdgcn_sched_barrier(0); } while (0)
        K1_LOAD(0); K1_LOAD(1); K1_LOAD(2);
#pragma unroll
        for (int k = 0; k < 4; ++k) { const int u = tid + 512 * k; *(LAS v4u*)(L + (u >> sh) * rs_ + (u & ((1 << sh) - 1)) * 16) = t[k]; }
        __builtin_amdgcn_sched_barrier(0);
        K1_TURN(0);
        K1_LOAD(3);
        K1_TURN(1); K1_TURN(2); K1_TURN(3);
    }
    asm volatile("s_waitcnt lgkmcnt(0)" ::: "memory");
    __builtin_amdgcn_s_barrier();
    asm volatile("" ::: "memory");
    PT0(1);
    f32x4 acc[4][4];
#pragma unroll
    for (int x = 0; x < 4; ++x)
#pragma unroll
        for (int y = 0; y < 4; ++y) acc[x][y] = f32x4{0.f, 0.f, 0.f, 0.f};
#pragma unroll
    for (int es = 0; es < 8; ++es) {
        v4u bfr[4];
#pragma unroll
        for (int y = 0; y < 4; ++y) {
            if constexpr (mat == 0) {
                bfr[y] = *(const LAS v4u*)(L + (16 * (y >> 1) + 2 * (y & 1) + 4 * (n >> 2) + 32 * ((n >> 1) & 1) + (n & 1)) * K1_RS + es * 64 + g * 16);
            } else {
                const LAS unsigned char* rp = L + (32 * es + 8 * g + (n >> 2)) * K1_OS + y * 32 + (l & 3) * 8;
                const v2u lo = tr_read(rp), hi = tr_read(rp + 4 * K1_OS);
                bfr[y] = v4u{lo.x, lo.y, hi.x, hi.y};
            }
        }
#pragma unroll
        for (int x = 0; x < 4; ++x)
#pragma unroll
            for (int y = 0; y < 4; ++y) acc[x][y] = mfma32(cr[x][es], bfr[y], acc[x][y]);
    }
    __builtin_amdgcn_sched_barrier(0); PT0(2); __builtin_amdgcn_sched_barrier(0);
    constexpr float SC = mat == 0 ? LOG2E / 16.0f : 1.0f;
#pragma unroll
    for (int y = 0; y < 4; ++y) {
        unsigned char* d = mat == 0 ? p.ws + OFF_TW + ((size_t)(h * 28 + 4 * blk + y) * 7 + w) * 2048 + l * 16
                                    : p.ws + OFF_YW + ((size_t)((4 * blk + y) * 16 + h) * 7 + w) * 2048 + l * 16;
#pragma unroll
        for (int a = 0; a < 2; ++a) {
            const f32x4 lo = acc[2 * a][y] * SC, hi = acc[2 * a + 1][y] * SC;
            stg16(d + a * 1024, v4u{pack2(lo[0], lo[1]), pack2(lo[2], lo[3]), pack2(hi[0], hi[1]), pack2(hi[2], hi[3])});
        }
    }
    PT(3);
}

__global__ void __launch_bounds__(512, 2) k_prep(Params p) {
    extern __shared__ __attribute__((aligned(16))) unsigned char lds_[];
    LAS unsigned char* L = (LAS unsigned char*)lds_;
    p.ws = align_ws(p.ws);
    const int b = blockIdx.x, tid = threadIdx.x;
    const int l = tid & 63, w = __builtin_amdgcn_readfirstlane(tid >> 6);
    PT0(0);
    if (b >= 224) {
        const int k = b - 224;
        if ((k & 7) == 0 && w < 2) {
            const int pg = (k >> 3) + 4 * w;
            if (pg < NPAGE && l == 0) {
                GAS unsigned* a = (GAS unsigned*)(p.ws + OFF_HOP + (size_t)pg * PAGE + 126 * CH);
                unsigned best[4] = {~0u, ~0u, ~0u, ~0u};
                for (int it = 0; it < 3; ++it) {
#pragma unroll
                    for (int q = 0; q < 4; ++q) {
                        const unsigned long long t0 = __builtin_amdgcn_s_memtime();
                        __hip_atomic_store(a + q * 1024, 0xFFFFFFFFu, __ATOMIC_RELAXED, __HIP_MEMORY_SCOPE_AGENT);
                        asm volatile("s_waitcnt vmcnt(0)" ::: "memory");
                        const unsigned dt = (unsigned)(__builtin_amdgcn_s_memtime() - t0);
                        best[q] = min(best[q], dt);
                    }
                }
                ((unsigned*)(p.ws + OFF_CTL))[pg] = ((best[0] + best[1] > best[2] + best[3]) ? 1u : 0u) ^ ((xcc_id() >> 2) & 1u);
            }
            PT(3);
            if (pg == 7) {
                if (l == 0) ((unsigned*)(p.ws + OFF_CTL))[8] = xcc_id() & 7u;
                for (int i = l; i < (int)(CTL_BYTES - 256) / 16; i += 64) stg16(p.ws + OFF_CTL + 256 + i * 16, v4u{0u, 0u, 0u, 0u});
            }
            return;
        }
        if ((k & 7) == 0) return;
        const int kk = k - (k >> 3) - 1;
        misc_wave<false>(p, kk * 8 + w, l);
        PT(3);
        return;
    }
    const int r = b >> 3, h = (b & 7) + 8 * (r & 1), q = r >> 1;
    if (q < 7) prep_heavy<0>(p, L, h, q, w); else prep_heavy<1>(p, L, h, q - 7, w);
}

constexpr int L_MST = 0, L_TIM = L_MST + 2 * 7168, L_RED = L_TIM + 8192, L_REC_END = 96 * 1024;
#define DLY_M 1024
#define DM0 18
#define DLY_P 0
#define DLY_PO 0
#define PPOSY 7
#define GAP_M 150
#define DLY_G 300
#define DLY_3 280
#define DLY_4 60
#define DLY_V 240
#define DLY_Y 0
#define PPOS 8
#define DLY_Q 200
#define SPA 0
#define SPB 2
#define SW_DLY 60
#define STAG 1
__device__ __forceinline__ void delay_var(int n) {
    asm volatile("s_cmp_lt_i32 %0, 1\n\ts_cbranch_scc1 2f\n1:\n\ts_nop 5\n\ts_sub_i32 %0, %0, 1\n\ts_cmp_gt_i32 %0, 0\n\ts_cbranch_scc1 1b\n2:" : "+s"(n) :: "scc", "memory");
}
template <int C> __device__ __forceinline__ void delay_cycles() {
    if constexpr (C >= 64) __builtin_amdgcn_s_sleep(C / 64 > 127 ? 127 : C / 64);
    if constexpr ((C & 63) >= 4) asm volatile("s_nop %0" :: "i"(((C & 63) >> 2) - 1));
}

__device__ __forceinline__ bool fresh(v4u x) {
    const unsigned m = max(max(x.x, x.y), max(x.z, x.w));
    return __all(m != 0xFFFFFFFFu);
}
#define BAR() do { asm volatile("s_waitcnt lgkmcnt(0)" ::: "memory"); __builtin_amdgcn_s_barrier(); asm volatile("" ::: "memory"); } while (0)
#define FENCE() do { asm volatile("" ::: "memory"); __builtin_amdgcn_sched_barrier(0); } while (0)

#define STAMP(k) do {} while (0)
#define PROF_DECL
#define PROF_ACC do {} while (0)
#define PROF_OUT(slot) do {} while (0)
typedef _Float16 f16x2 __attribute__((ext_vector_type(2)));
__device__ __forceinline__ unsigned packh(float a, float b) { return __builtin_bit_cast(unsigned, __builtin_amdgcn_cvt_pkrtz(a, b)); }
__device__ __forceinline__ float hlo(unsigned v) { const f16x2 h = __builtin_bit_cast(f16x2, v); return (float)h.x; }
__device__ __forceinline__ float hhi(unsigned v) { const f16x2 h = __builtin_bit_cast(f16x2, v); return (float)h.y; }
constexpr unsigned SPIN_CAP = 1u << 20;

__device__ __forceinline__ unsigned pkmaxh(unsigned a, unsigned b) { unsigned r; asm("v_pk_max_f16 %0, %1, %2" : "=v"(r) : "v"(a), "v"(b)); return r; }
__device__ __forceinline__ float vmax(float a, float b) { float r; asm("v_max_f32 %0, %1, %2" : "=v"(r) : "v"(a), "v"(b)); return r; }
__device__ __forceinline__ float xg_max_raw(float v) {
    const v2u a = __builtin_amdgcn_permlane16_swap(fbits(v), fbits(v), false, false);
    v = vmax(bitsf(a.x), bitsf(a.y));
    const v2u b = __builtin_amdgcn_permlane32_swap(fbits(v), fbits(v), false, false);
    return vmax(bitsf(b.x), bitsf(b.y));
}
__device__ __forceinline__ float addh_lo(unsigned a, unsigned b) { float r; asm("v_fma_mix_f32 %0, %1, 1.0, %2 op_sel_hi:[1,0,1]" : "=v"(r) : "v"(a), "v"(b)); return r; }
__device__ __forceinline__ float addh_hi(unsigned a, unsigned b) { float r; asm("v_fma_mix_f32 %0, %1, 1.0, %2 op_sel:[1,0,1] op_sel_hi:[1,0,1]" : "=v"(r) : "v"(a), "v"(b)); return r; }
__device__ __forceinline__ float subh_lo(unsigned a, float b) { float r; asm("v_fma_mix_f32 %0, %1, 1.0, -%2 op_sel_hi:[1,0,0]" : "=v"(r) : "v"(a), "v"(b)); return r; }
__device__ __forceinline__ float subh_hi(unsigned a, float b) { float r; asm("v_fma_mix_f32 %0, %1, 1.0, -%2 op_sel:[1,0,0] op_sel_hi:[1,0,0]" : "=v"(r) : "v"(a), "v"(b)); return r; }
__device__ __forceinline__ float add_ror8(float a) { float r; asm("s_nop 1\n\tv_add_f32_dpp %0, %1, %1 row_ror:8 row_mask:0xf bank_mask:0xf" : "=v"(r) : "v"(a)); return r; }
#define UNLIKELY(x) __builtin_expect(!!(x), 0)
#define CNT(x) do {} while (0)

#define TLS(k) do {} while (0)
#define TLS7(k) do {} while (0)
template <int NG, int STR>
__device__ __forceinline__ int wait_ready(const __amdgpu_buffer_rsrc_t rs, const int g0, const int need, unsigned& mspin) {
    const int l = threadIdx.x & 63;
    int ready;
    for (;;) {
        asm volatile("" ::: "memory");
        const int v = (int)__builtin_amdgcn_raw_buffer_load_b32(rs, CT_PROG + (g0 * 3 + (l < 3 * NG ? l : 0)) * 128, 0, 16);
        ready = 1 << 20;
#pragma unroll
        for (int k = 0; k < 3 * NG; ++k) if (k % 3 < STR) ready = min(ready, STR * __builtin_amdgcn_readlane(v, k) + (k % 3));
        if (ready > need || ++mspin >= SPIN_CAP) break;
        __builtin_amdgcn_s_sleep(2);
    }
    asm volatile("" ::: "memory");
    return ready;
}

template <int ROLE, int DLY1>
__device__ __forceinline__ void score_wave(const Params& p, LAS unsigned char* L, const int u, const int w) {
    constexpr bool SPECIAL = ROLE == 2;
    const int l = threadIdx.x & 63;
    const int n = l & 15, g = l >> 4, i8 = l & 7, hh = (l >> 3) & 1;
    const int half = u & 1;
    const __amdgpu_buffer_rsrc_t rs = __builtin_amdgcn_make_buffer_rsrc(p.ws, 0, (int)WS_END, 0x00020000);
    const int idx = hh ? 0xEEEE : 0x4444;
    const unsigned parmask = load_parmask(p.ws);
    v8u tw[SPECIAL ? 1 : 14];
    v4u gx[SPECIAL ? 14 : 1];
    const LAS unsigned char* mrd[SPECIAL ? 1 : 7];
    if constexpr (!SPECIAL) {
        const unsigned char* src = p.ws + OFF_TW + ((size_t)(u * 14 + 2 * w) * 7) * 2048 + l * 16;
#pragma unroll
        for (int k = 0; k < 7; ++k) {
            const int f = w + k < 7 ? w + k : w + k - 7;
            mrd[k] = L + L_MST + f * 1024 + l * 16;
#pragma unroll
            for (int t = 0; t < 2; ++t) {
                const v4u a = ldg16(src + (7 * t + f) * 2048), b = ldg16(src + (7 * t + f) * 2048 + 1024);
                tw[7 * t + k] = v8u{a.x, a.y, a.z, a.w, b.x, b.y, b.z, b.w};
            }
        }
    } else {
        *(LAS v4u*)(L + L_TIM + (half ? 0 : 14 * 512) + l * 16) = v4u{0u, 0u, 0u, 0u};
        __builtin_amdgcn_s_setprio(2);
    }
    const int moff = C_M + w * 1024 + hh * 512 + g * 128 + i8 * 16;
    const int sst_off = C_S + hh * 4096 + u * 128 + (4 * (g & 1) + 2 * (g >> 1) + (l & 1)) * 16 + (i8 >> 1) * 4;
    const unsigned ssel = (l & 1) ? 0x03020706u : 0x05040100u;
    const int pst_off = hh ? 0x7FFFFFF0 : C_P + u * 256 + i8 * 32 + g * 8;
    const int goff = ((u & ~1) + ((n >> 2) & 1)) * 14336 + (8 * half + 4 * (n >> 3) + (n & 3) + 16 * g) * 16;
    const int xoff = (int)OFF_XP + (w * 14 + 7 * half) * 1024 + l * 16;
    LAS unsigned char* mq = L + L_MST + 7168 + w * 1024 + (i8 + 8 * (g >> 1) + 32 * (g & 1)) * 16 + hh * 8;
    unsigned spins = 0, mspin = 0;
    int ready = 0;
    if constexpr (!SPECIAL) {
#pragma unroll
        for (int k = 0; k < 7; ++k) asm volatile("" : "+v"(mrd[k]));
    }
    v4u xq[7], x3 = v4u{0u, 0u, 0u, 0u};
    if constexpr (ROLE == 1) {
#pragma unroll
        for (int k = 0; k < 7; ++k) xq[k] = __builtin_amdgcn_raw_buffer_load_b128(rs, xoff + k * 1024, (NSEG - 1) * 28672, 0);
    }
    if constexpr (SPECIAL) {
        ready = wait_ready<2, 2>(rs, u & ~1, 0, mspin);
#pragma unroll
        for (int f = 0; f < 14; ++f) gx[f] = __builtin_amdgcn_raw_buffer_load_b128(rs, goff + f * 1024, (int)OFF_GF, 16);
    }
    PROF_DECL
    for (int s = 0; s < NSEG; ++s) {
        const int ho = hop_off(s, parmask);
        FENCE();
        STAMP(0);
        if constexpr (!SPECIAL) {
            if (s) { delay_cycles<DLY_M>(); delay_var(DM0); }
            FENCE();
            v4u xs = __builtin_amdgcn_raw_buffer_load_b128(rs, moff, ho, 16);
            FENCE(); delay_cycles<GAP_M>(); FENCE();
            const v4u x1 = __builtin_amdgcn_raw_buffer_load_b128(rs, moff, ho + CH, 16);
            FENCE(); delay_cycles<GAP_M>(); FENCE();
            const v4u x2 = __builtin_amdgcn_raw_buffer_load_b128(rs, moff, ho + 2 * CH, 16);
            FENCE();
#define MOV4(d_, s_) do { asm volatile("v_mov_b64 %0, %1" : "=v"(d_.lo) : "v"(s_.lo)); asm volatile("v_mov_b64 %0, %1" : "=v"(d_.hi) : "v"(s_.hi)); } while (0)
            if (UNLIKELY(!fresh(xs))) {
                FENCE();
                x3 = __builtin_amdgcn_raw_buffer_load_b128(rs, moff, ho + 3 * CH, 16);
                FENCE();
                CNT(1);
                MOV4(xs, x1);
                if (!fresh(xs)) {
                    CNT(1);
                    MOV4(xs, x2);
                    if (!fresh(xs)) {
                        CNT(1);
                        MOV4(xs, x3);
                        if (!fresh(xs)) {
                            for (;;) {
                                asm volatile("" ::: "memory");
                                xs = __builtin_amdgcn_raw_buffer_load_b128(rs, moff, ho + 3 * CH, 16);
                                CNT(1);
                                if (fresh(xs) || ++mspin >= SPIN_CAP) break;
                            }
                        }
                    }
                }
            }
            STAMP(1);
            *(LAS v4u*)mrd[0] = xs;
            *(LAS v2u*)mq = v2u{xs.x, xs.y};
            *(LAS v2u*)(mq + 256) = v2u{xs.z, xs.w};
            FENCE();
            f32x4 a0 = {0.f, 0.f, 0.f, 0.f}, a1 = a0;
            a0 = smfmac(xs, tw[0], a0, idx); a1 = smfmac(xs, tw[7], a1, idx);
            asm volatile("" : "+v"(a0), "+v"(a1));
            FENCE();
            BAR();
            STAMP(2);
            v4u mp[7];
            delay_cycles<DLY1>();
            FENCE();
#pragma unroll
            for (int k = 1; k < 7; ++k) mp[k] = *(const LAS v4u*)mrd[k];
            FENCE();
#pragma unroll
            for (int k = 1; k < 7; ++k) { a0 = smfmac(mp[k], tw[k], a0, idx); a1 = smfmac(mp[k], tw[7 + k], a1, idx); }
            *(LAS v2u*)(L + L_TIM + (2 * w + 2 * half) * 512 + n * 32 + g * 8) = v2u{pack2(a0[0], a0[1]), pack2(a0[2], a0[3])};
            *(LAS v2u*)(L + L_TIM + (2 * w + 2 * half + 1) * 512 + n * 32 + g * 8) = v2u{pack2(a1[0], a1[1]), pack2(a1[2], a1[3])};
            STAMP(3);
            BAR();
            STAMP(4);
            asm volatile("" : "+v"(x3) : "v"(x1), "v"(x2));
            TLS(8 + s);
            if (ROLE == 1 && s == NSEG - 1) {
                v4u ta[7];
#pragma unroll
                for (int k = 0; k < 7; ++k) {
                    const v2u t0 = tr_read(L + L_TIM + (2 * k + 2 * half) * 512 + l * 8), t1 = tr_read(L + L_TIM + (2 * k + 2 * half + 1) * 512 + l * 8);
                    ta[k] = v4u{t0.x, t0.y, t1.x, t1.y};
                }
                FENCE();
                f32x4 d0 = {0.f, 0.f, 0.f, 0.f};
#pragma unroll
                for (int k = 0; k < 7; ++k) d0 = mfma32(ta[k], xq[k], d0);
                __builtin_amdgcn_raw_buffer_store_b64(v2u{sane(fbits(fold32(d0[0], d0[2]))), sane(fbits(fold32(d0[1], d0[3])))}, rs, w * 512 + l * 8,
                                                      (int)OFF_SXM + (s * 32 + u) * 1024, 16);
                FENCE();
            }
        } else {
            BAR();
            STAMP(2);
            v4u mp[7];
#pragma unroll
            for (int f = 0; f < 7; ++f) mp[f] = *(const LAS v4u*)(L + L_MST + 7168 + f * 1024 + l * 16);
            FENCE();
            f32x4 ae = {0.f, 0.f, 0.f, 0.f}, ao = ae;
            v4u m2[4];
#pragma unroll
            for (int f = 0; f < 7; ++f) {
                ae = mfma32(gx[2 * f], mp[f], ae);
                ao = mfma32(gx[2 * f + 1], mp[f], ao);
                asm volatile("" : "+v"(ae), "+v"(ao));
            }
            FENCE();
#pragma unroll
            for (int b = 0; b < 4; ++b) m2[b] = *(const LAS v4u*)(L + L_MST + (3 * half + b) * 1024 + l * 16);
            FENCE();
            const v2u pv = v2u{sane(packh(ae[0] + ror8(ao[0]), ae[1] + ror8(ao[1]))), sane(packh(ae[2] + ror8(ao[2]), ae[3] + ror8(ao[3])))};
            __builtin_amdgcn_raw_buffer_store_b64(pv, rs, pst_off, ho, 16);
            __builtin_amdgcn_raw_buffer_store_b64(pv, rs, pst_off, ho + CH, 16);
            FENCE();
            STAMP(3);
            f32x4 sa = {0.f, 0.f, 0.f, 0.f};
            asm volatile("" : "+v"(sa));
            BAR();
            STAMP(4);
            v8u tb[4];
#pragma unroll
            for (int b = 0; b < 4; ++b) {
                const v2u t0 = tr_read(L + L_TIM + (4 * b) * 512 + l * 8), t1 = tr_read(L + L_TIM + (4 * b + 1) * 512 + l * 8),
                          t2 = tr_read(L + L_TIM + (4 * b + 2) * 512 + l * 8), t3 = tr_read(L + L_TIM + (4 * b + 3) * 512 + l * 8);
                tb[b] = v8u{t0.x, t0.y, t1.x, t1.y, t2.x, t2.y, t3.x, t3.y};
            }
            FENCE();
#pragma unroll
            for (int b = 0; b < 4; ++b) sa = smfmac(m2[b], tb[b], sa, idx);
            const unsigned pk = packh(add_ror8(fold32(sa[0], sa[2])), add_ror8(fold32(sa[1], sa[3])));
            const unsigned nb = __builtin_amdgcn_update_dpp(0u, pk, 0xB1, 0xf, 0xf, true);
            __builtin_amdgcn_raw_buffer_store_b32(sane(__builtin_amdgcn_perm(nb, pk, ssel)), rs, sst_off, ho, 16);
            FENCE();
            STAMP(5);
            delay_cycles<DLY_G>();
            if (UNLIKELY(ready <= s + 1 && s + 1 < NSEG)) ready = wait_ready<2, 2>(rs, u & ~1, s + 1, mspin);
            FENCE();
#pragma unroll
            for (int f = 0; f < 14; ++f) gx[f] = __builtin_amdgcn_raw_buffer_load_b128(rs, goff + f * 1024, (int)(OFF_GF + (s + 1 < NSEG ? s + 1 : s) * SEGB), 16);
            FENCE();
        }
        PROF_ACC;
    }
    PROF_OUT(u * 8 + w);
}

template <bool LAST, bool YOUNG>
__device__ __forceinline__ void value_wave(const Params& p, LAS unsigned char* L, const int v, const int w) {
    const int l = threadIdx.x & 63;
    const int g = l >> 4, i8 = l & 7, hh = (l >> 3) & 1;
    const __amdgpu_buffer_rsrc_t rs = __builtin_amdgcn_make_buffer_rsrc(p.ws, 0, (int)WS_END, 0x00020000);
    const int idx = hh ? 0xEEEE : 0x4444;
    const unsigned parmask = load_parmask(p.ws);
    v8u yw[14];
    const LAS unsigned char* mrd[7];
    {
        const unsigned char* src = p.ws + OFF_YW + (size_t)(v * 8 + w) * 14 * 2048 + l * 16;
#pragma unroll
        for (int k = 0; k < 7; ++k) {
            const int f = w + k < 7 ? w + k : w + k - 7;
            mrd[k] = L + L_MST + f * 1024 + l * 16;
#pragma unroll
            for (int t = 0; t < 2; ++t) {
                const v4u a = ldg16(src + (7 * t + f) * 2048), b = ldg16(src + (7 * t + f) * 2048 + 1024);
                yw[7 * t + k] = v8u{a.x, a.y, a.z, a.w, b.x, b.y, b.z, b.w};
            }
        }
    }
    if (LAST) __builtin_amdgcn_s_setprio(2);
    const int moff = C_M2 + w * 1024 + hh * 512 + g * 128 + i8 * 16;
    const int mst_off = C_M + v * 256 + ((l >> 2) & 1) * 128 + (l >> 3) * 16 + (l & 3) * 4;
    const int soff = C_S + ((2 * w + hh) * 2 + (g >> 1)) * 128 + i8 * 16 + (g & 1) * 8;
    const int poff = C_P + ((2 * w + hh) * 2 + (g >> 1)) * 256 + i8 * 32 + (g & 1) * 16;
    const int yst_off = (int)OFF_YM + (v * 8 + w) * 512 + l * 8;
    const int uoff = (v * 8 + w) * 2048 + l * 16;
    unsigned mlo = hh ? 0u : 0xFFFFFFFFu, mhi = ~mlo;
    v4u ones = v4u{0x3F803F80u, 0x3F803F80u, 0x3F803F80u, 0x3F803F80u};
    asm volatile("" : "+v"(mlo), "+v"(mhi), "+v"(ones));
#pragma unroll
    for (int k = 0; k < 7; ++k) asm volatile("" : "+v"(mrd[k]));
    unsigned spins = 0, mspin = 0;
    PROF_DECL
    int ho = hop_off(0, parmask);
    int ready = wait_ready<1, 3>(rs, 32 + v, 0, mspin);
    v4u ua = __builtin_amdgcn_raw_buffer_load_b128(rs, uoff, (int)OFF_UF, 16);
#define V_Y_PHASE(MIDA, MIDB) \
        f32x4 y0 = {0.f, 0.f, 0.f, 0.f}, y1 = y0; \
        if constexpr (!LAST) { \
            delay_cycles<DLY_V>(); \
            FENCE(); \
            v4u x = __builtin_amdgcn_raw_buffer_load_b128(rs, moff, ho, 16); \
            if (UNLIKELY(!fresh(x))) { \
                for (;;) { asm volatile("" ::: "memory"); x = __builtin_amdgcn_raw_buffer_load_b128(rs, moff, ho, 16); CNT(1); if (fresh(x) || ++mspin >= SPIN_CAP) break; } \
            } \
            STAMP(1); \
            *(LAS v4u*)mrd[0] = x; \
            FENCE(); \
            y0 = smfmac(x, yw[0], y0, idx); y1 = smfmac(x, yw[7], y1, idx); \
            asm volatile("" : "+v"(y0), "+v"(y1)); \
            FENCE(); \
        } \
        BAR(); \
        STAMP(2); \
        v4u mp[7]; \
        if constexpr (YOUNG) delay_cycles<DLY_Y>(); \
        FENCE(); \
        _Pragma("unroll") for (int k = LAST ? 0 : 1; k < 7; ++k) mp[k] = *(const LAS v4u*)mrd[k]; \
        FENCE(); \
        MIDA; \
        _Pragma("unroll") for (int k = LAST ? 0 : 1; k < 7; ++k) { \
            if (k == (YOUNG ? PPOSY : PPOS)) { MIDB; } \
            y0 = smfmac(mp[k], yw[k], y0, idx); y1 = smfmac(mp[k], yw[7 + k], y1, idx); \
        } \
        if ((YOUNG ? PPOSY : PPOS) == 7) { MIDB; } \
        v2u yop = v2u{pack2(fold32(y0[0], y0[2]), fold32(y0[1], y0[3])), pack2(fold32(y1[0], y1[2]), fold32(y1[1], y1[3]))}; \
        asm volatile("" : "+v"(yop)); \
        FENCE(); \
        STAMP(3)
    for (int s = 0; s < NSEG - 1; ++s) {
        const int hn = hop_off(s + 1, parmask);
        const int uo = (int)(OFF_UF + (s + 1) * SEGB);
        if (UNLIKELY(ready <= s + 1)) ready = wait_ready<1, 3>(rs, 32 + v, s + 1, mspin);
        if constexpr (LAST) {
            if (UNLIKELY((s + 2) % SPP == 0)) {
                const int sh = 4 * ((s + 2) / SPP - 1);
                for (;;) {
                    asm volatile("" ::: "memory");
                    const unsigned c = __builtin_amdgcn_readfirstlane(__builtin_amdgcn_raw_buffer_load_b32(rs, CT_PZ, 0, 16));
                    if (((c >> sh) & 15u) >= 6u || ++mspin >= SPIN_CAP) break;
                }
            }
        }
        int ho1 = ho + CH, ho2 = ho + 4096, hn1 = hn + CH, hn2 = hn + 2 * CH, hn3 = hn + 3 * CH, hnn = hn;
        asm volatile("" : "+s"(ho1), "+s"(ho2), "+s"(hn1), "+s"(hn2), "+s"(hn3), "+s"(hnn));
        FENCE();
        STAMP(0);
        v4u ub, pv;
        V_Y_PHASE(ub = __builtin_amdgcn_raw_buffer_load_b128(rs, uoff + 1024, uo - (int)SEGB, 16); FENCE(),
                  FENCE(); pv = __builtin_amdgcn_raw_buffer_load_b128(rs, poff, ho, 16); FENCE());
        if ((YOUNG ? PPOSY : PPOS) == 8) { delay_cycles<YOUNG ? DLY_P : DLY_P + DLY_PO>(); FENCE(); pv = __builtin_amdgcn_raw_buffer_load_b128(rs, poff, ho, 16); FENCE(); }
        delay_cycles<DLY_Q>();
        FENCE();
        const v4u pq = __builtin_amdgcn_raw_buffer_load_b128(rs, poff, ho1, 16);
        FENCE();
        if (UNLIKELY(!fresh(pv))) {
            CNT(1 << 20);
            asm volatile("v_mov_b64 %0, %1" : "=v"(pv.lo) : "v"(pq.lo)); asm volatile("v_mov_b64 %0, %1" : "=v"(pv.hi) : "v"(pq.hi));
            if (!fresh(pv)) {
                for (;;) {
                    asm volatile("" ::: "memory");
                    pv = __builtin_amdgcn_raw_buffer_load_b128(rs, poff, ho1, 16);
                    CNT(1 << 20);
                    if (fresh(pv) || ++mspin >= SPIN_CAP) break;
                }
            }
        }
        STAMP(7);
        v2u sv, sw2;
#define S_A() do { FENCE(); STAMP(6); sv = __builtin_amdgcn_raw_buffer_load_b64(rs, soff, ho, 16); FENCE(); } while (0)
#define S_B() do { FENCE(); sw2 = __builtin_amdgcn_raw_buffer_load_b64(rs, soff, ho2, 16); FENCE(); } while (0)
        if (SPA == 0) S_A();
        const unsigned hm = pkmaxh(pkmaxh(pv.x, pv.y), pkmaxh(pv.z, pv.w));
        const float mx = xg_max_raw(vmax(hlo(hm), hhi(hm)));
        if (SPA == 1) S_A();
        if (SPB == 1) S_B();
        const unsigned e0 = pack2(ex2(subh_lo(pv.x, mx)), ex2(subh_hi(pv.x, mx))), e1 = pack2(ex2(subh_lo(pv.y, mx)), ex2(subh_hi(pv.y, mx)));
        if (SPA == 2) S_A();
        if (SPB == 2) S_B();
        const unsigned e2 = pack2(ex2(subh_lo(pv.z, mx)), ex2(subh_hi(pv.z, mx))), e3 = pack2(ex2(subh_lo(pv.w, mx)), ex2(subh_hi(pv.w, mx)));
        const f32x4 z4 = {0.f, 0.f, 0.f, 0.f};
        const v4u eop = v4u{e0, e1, e2, e3};
        const f32x4 oa = mfma32(ua, eop, z4);
        const f32x4 ob = mfma32(ub, eop, z4);
        const f32x4 lx4 = mfma32(ones, eop, z4);
        if (SPB == 3) S_B();
        f32x4 ox = hh ? ob : oa;
        f32x4 lx = lx4;
        asm volatile("" : "+v"(ox), "+v"(lx));
        FENCE();
        STAMP(8);
        if (UNLIKELY(!__all(max(sv.x, sv.y) != 0xFFFFFFFFu))) {
            CNT(1 << 10);
            asm volatile("v_mov_b64 %0, %1" : "=v"(sv) : "v"(sw2));
            if (!__all(max(sv.x, sv.y) != 0xFFFFFFFFu)) {
                for (;;) {
                    asm volatile("" ::: "memory");
                    sv = __builtin_amdgcn_raw_buffer_load_b64(rs, soff, ho2, 16);
                    CNT(1 << 10);
                    if (__all(max(sv.x, sv.y) != 0xFFFFFFFFu) || ++mspin >= SPIN_CAP) break;
                }
            }
        }
        STAMP(9);
        const v2u sw = __builtin_amdgcn_permlane32_swap(sv.x, sv.y, false, false);
        const float t0 = addh_lo(sw.x, sw.y), t1 = addh_hi(sw.x, sw.y);
        float m;
        asm("v_max3_f32 %0, %1, %2, %3" : "=v"(m) : "v"(t0), "v"(t1), "v"(mx));
        m = xg_max_raw(m);
        const unsigned pk = pack2(ex2(t0 - m), ex2(t1 - m));
        const float beta = ex2(mx - m);
        const v2u bop = v2u{pk & mlo, pk & mhi};
        lx[0] *= beta;
        const f32x4 ls = mfma16(v2u{ones.x, ones.y}, bop, lx);
        f32x4 d = mfma16(yop, bop, ox * beta);
        const float r = __builtin_amdgcn_rcpf(ls[0]);
        d *= r;
        float r0, r1;
        asm("s_nop 1\n\tv_add_f32_dpp %0, %2, %2 row_ror:8 row_mask:0xf bank_mask:0x3\n\tv_add_f32_dpp %1, %3, %3 row_ror:8 row_mask:0xf bank_mask:0x3\n\t"
            "v_add_f32_dpp %0, %4, %4 row_ror:8 row_mask:0xf bank_mask:0xc\n\tv_add_f32_dpp %1, %5, %5 row_ror:8 row_mask:0xf bank_mask:0xc"
            : "=&v"(r0), "=&v"(r1) : "v"(d[0]), "v"(d[1]), "v"(d[2]), "v"(d[3]));
        *(LAS f32x2*)(L + L_RED + w * 512 + i8 * 64 + g * 16 + hh * 8) = f32x2{r0, r1};
        STAMP(10);
        FENCE();
        if constexpr (!LAST) ua = __builtin_amdgcn_raw_buffer_load_b128(rs, uoff, uo, 16);
        FENCE();
        BAR();
        STAMP(11);
        if constexpr (LAST) {
            f32x2 q[8];
#pragma unroll
            for (int k = 0; k < 8; ++k) q[k] = *(const LAS f32x2*)(L + L_RED + k * 512 + l * 8);
            FENCE();
            const f32x2 tot = ((q[0] + q[1]) + (q[2] + q[3])) + ((q[4] + q[5]) + (q[6] + q[7]));
            const unsigned mv = sane(pack2(tot[0], tot[1]));
            __builtin_amdgcn_raw_buffer_store_b32(mv, rs, mst_off, hn3, 16);
            __builtin_amdgcn_raw_buffer_store_b32(mv, rs, mst_off, hn2, 16);
            __builtin_amdgcn_raw_buffer_store_b32(mv, rs, mst_off, hn1, 16);
            __builtin_amdgcn_raw_buffer_store_b32(mv, rs, mst_off, hnn, 16);
            __builtin_amdgcn_raw_buffer_store_b32(mv, rs, mst_off, hn + C_M2, 16);
            __builtin_amdgcn_raw_buffer_store_b32(mv, rs, mst_off, hn + C_M3, 16);
            FENCE();
            asm volatile("" :: "v"(sw2), "v"(pq));
            ua = __builtin_amdgcn_raw_buffer_load_b128(rs, uoff, uo, 16);
        } else {
            delay_cycles<256>();
            asm volatile("" :: "v"(sw2), "v"(pq));
        }
        __builtin_amdgcn_raw_buffer_store_b64(v2u{sane(yop.x), sane(yop.y)}, rs, yst_off, s * 224 * 512, 16);
        ho = hn;
        if constexpr (LAST) PROF_ACC;
    }
    {
        const int s = NSEG - 1;
        V_Y_PHASE(, );
        __builtin_amdgcn_raw_buffer_store_b64(v2u{sane(yop.x), sane(yop.y)}, rs, yst_off, s * 224 * 512, 16);
    }
    PROF_OUT(256 + v * 8 + w);
}

__device__ __forceinline__ void glds16(const void* gsrc, unsigned lds_dst) {
    unsigned keep;
    asm volatile("s_mov_b32 %0, m0\n\ts_mov_b32 m0, %2\n\ts_nop 0\n\tglobal_load_lds_dwordx4 %1, off\n\ts_mov_b32 m0, %0"
                 : "=&s"(keep) : "v"(gsrc), "s"(lds_dst) : "memory");
}
__device__ __forceinline__ v4u ldc16(const __amdgpu_buffer_rsrc_t rs, int voff, int soff) { return __builtin_amdgcn_raw_buffer_load_b128(rs, voff, soff, 16); }
__device__ __forceinline__ v4u pack8(f32x4 a, f32x4 b) { return v4u{pack2(a[0], a[1]), pack2(a[2], a[3]), pack2(b[0], b[1]), pack2(b[2], b[3])}; }
constexpr int L_XB = 0, XBUF = 28672;

template <bool GTYPE>
__device__ __forceinline__ void sweep(const Params& p, LAS unsigned char* L, const __amdgpu_buffer_rsrc_t rs, const int grp, const int r, const int w) {
    constexpr int STR = GTYPE ? 2 : 3;
    const int l = threadIdx.x & 63;
    const unsigned lbase = (unsigned)(size_t)L;
    const unsigned char* wsrc;
    int obase, ostride, osec;
    float oscale = 1.0f;
    if (GTYPE) {
        const int ks = 7 * (grp & 1) + w;
        if (w < 7) { wsrc = p.ws + OFF_TW + (size_t)((grp >> 1) * 14 + ks) * 28672; obase = (int)OFF_GF + ((grp >> 1) * 28 + ks) * 1024; ostride = (int)SEGB; osec = 14336; }
        else { wsrc = p.ws + OFF_QW + (size_t)grp * 28672; obase = (int)OFF_QF + (grp >> 4) * (2048 * 1024) + (grp & 15) * 1024; ostride = 32768; osec = 16384;
               if (grp < 16) oscale = LOG2E * 0.18898223650461362f; }
    } else {
        osec = 1024;
        if (grp < 60) { wsrc = p.ws + OFF_YW + (size_t)((grp - 32) * 8 + w) * 28672; obase = (int)OFF_UF + ((grp - 32) * 8 + w) * 2048; ostride = (int)SEGB; }
        else { wsrc = p.ws + OFF_QW + (size_t)(32 + (grp - 60) * 8 + w) * 28672; obase = (int)OFF_VF + ((grp - 60) * 8 + w) * 2048; ostride = 32768; }
    }
    const int npc = w < 4 ? 4 : 3, pc0 = w < 4 ? 4 * w : 16 + 3 * (w - 4);
    const unsigned char* xsrc = p.ws + OFF_XP + pc0 * 1024 + l * 16;
    if (r > 0) __builtin_amdgcn_s_sleep(SW_DLY);
    for (int k = 0; k < npc; ++k) glds16(xsrc + (size_t)r * XBUF + k * 1024, lbase + L_XB + (pc0 + k) * 1024);
    v4u wt[2][14];
#pragma unroll
    for (int f = 0; f < 14; ++f) { wt[0][f] = ldg16(wsrc + f * 1024 + l * 16); wt[1][f] = ldg16(wsrc + (14 + f) * 1024 + l * 16); }
#pragma unroll
    for (int f = 0; f < 14; ++f) asm volatile("" : "+v"(wt[0][f]), "+v"(wt[1][f]));
    asm volatile("s_waitcnt vmcnt(0)" ::: "memory");
    BAR();
    TLS(1);
    const int nseg = (NSEG + STR - 1 - r) / STR;
    for (int i = 0; i < nseg; ++i) {
        const int seg = r + STR * i;
        if (i + 1 < nseg) for (int k = 0; k < npc; ++k) glds16(xsrc + (size_t)(seg + STR) * XBUF + k * 1024, lbase + L_XB + ((i + 1) & 1) * XBUF + (pc0 + k) * 1024);
        const LAS unsigned char* xb = L + L_XB + (i & 1) * XBUF + l * 16;
        f32x4 a[2][2];
#pragma unroll
        for (int t = 0; t < 4; ++t) a[t >> 1][t & 1] = f32x4{0.f, 0.f, 0.f, 0.f};
#pragma unroll
        for (int f = 0; f < 14; ++f) {
            const v4u x0 = *(const LAS v4u*)(xb + f * 1024), x1 = *(const LAS v4u*)(xb + (14 + f) * 1024);
            if (GTYPE) {
                a[0][0] = mfma32(wt[0][f], x0, a[0][0]); a[0][1] = mfma32(wt[1][f], x0, a[0][1]);
                a[1][0] = mfma32(wt[0][f], x1, a[1][0]); a[1][1] = mfma32(wt[1][f], x1, a[1][1]);
            } else {
                a[0][0] = mfma32(x0, wt[0][f], a[0][0]); a[0][1] = mfma32(x0, wt[1][f], a[0][1]);
                a[1][0] = mfma32(x1, wt[0][f], a[1][0]); a[1][1] = mfma32(x1, wt[1][f], a[1][1]);
            }
        }
        v4u o0 = GTYPE ? pack8(a[0][0] * oscale, a[0][1] * oscale) : pack8(a[0][0], a[1][0]);
        v4u o1 = GTYPE ? pack8(a[1][0] * oscale, a[1][1] * oscale) : pack8(a[0][1], a[1][1]);
        if (GTYPE && w < 7) { o0 = v4u{o0.x, o0.z, o0.y, o0.w}; o1 = v4u{o1.x, o1.z, o1.y, o1.w}; }
        __builtin_amdgcn_raw_buffer_store_b128(o0, rs, l * 16, obase + seg * ostride, 16);
        __builtin_amdgcn_raw_buffer_store_b128(o1, rs, l * 16, obase + seg * ostride + osec, 16);
        const bool drain = (i < 2 || i + 1 == nseg);
        if (drain) asm volatile("s_waitcnt vmcnt(0)" ::: "memory"); else asm volatile("s_waitcnt vmcnt(2)" ::: "memory");
        BAR();
        if (threadIdx.x == 0) __builtin_amdgcn_raw_buffer_store_b32((unsigned)(drain ? i + 1 : i), rs, CT_PROG + (grp * 3 + r) * 128, 0, 16);
        if (i == 0) TLS(2);
    }
    if (threadIdx.x == 0) __hip_atomic_fetch_add((GAS unsigned*)(p.ws + CT_DONE), 1u, __ATOMIC_RELAXED, __HIP_MEMORY_SCOPE_AGENT);
    TLS(3);
}

__device__ __forceinline__ void serve(const Params& p, LAS unsigned char* L, const __amdgpu_buffer_rsrc_t rs, const int grp, const int w) {
    const int l = threadIdx.x & 63, n = l & 15, g = l >> 4;
    const int ks = 7 * (grp & 1) + w;
    v4u wt[2][14];
    {
        const unsigned char* wsrc = p.ws + OFF_TW + (size_t)((grp >> 1) * 14 + (w < 7 ? ks : 0)) * 28672;
        __builtin_amdgcn_s_sleep(100);
#pragma unroll
        for (int f = 0; f < 14; ++f) { wt[0][f] = ldg16(wsrc + f * 1024 + l * 16); wt[1][f] = ldg16(wsrc + (14 + f) * 1024 + l * 16); }
#pragma unroll
        for (int f = 0; f < 14; ++f) asm volatile("" : "+v"(wt[0][f]), "+v"(wt[1][f]));
    }
    const unsigned parmask = load_parmask(p.ws);
    unsigned mspin = 0;
    const int mrow = 4 * ((n >> 2) & 1) + 2 * (n >> 3) + (n & 1);
    const LAS unsigned char* mrd = L + (g >> 1) * 128 + mrow * 16 + (g & 1) * 8;
    const unsigned char* xr = p.ws + OFF_XP + (w < 7 ? ks : 0) * 1024 + l * 16;
    v4u mx_ = ldc16(rs, (w < 7 ? w : 0) * 1024 + l * 16, hop_off(0, parmask) + C_M3);
    for (int s = 0; s < NSEG - 1; ++s) {
        v4u xa, xb2;
        if (w < 7) {
            xa = ldg16(xr + (size_t)s * 28672); xb2 = ldg16(xr + (size_t)s * 28672 + 14336);
            const int ho = hop_off(s, parmask) + C_M3;
            while (!fresh(mx_) && ++mspin < SPIN_CAP) {
                __builtin_amdgcn_s_sleep(2);
                asm volatile("" ::: "memory");
                mx_ = ldc16(rs, w * 1024 + l * 16, ho);
            }
            *(LAS v4u*)(L + w * 1024 + l * 16) = mx_;
            FENCE();
            mx_ = ldc16(rs, w * 1024 + l * 16, hop_off(s + 1 < NSEG ? s + 1 : s, parmask) + C_M3);
        }
        BAR();
        if (w < 7) {
            v2u lo[14], hi[14];
#pragma unroll
            for (int f = 0; f < 14; ++f) {
                lo[f] = *(const LAS v2u*)(mrd + (f >> 1) * 1024 + (f & 1) * 256); hi[f] = *(const LAS v2u*)(mrd + (f >> 1) * 1024 + (f & 1) * 256 + 512);
            }
            FENCE();
            f32x4 a0 = {0.f, 0.f, 0.f, 0.f}, a1 = a0;
#pragma unroll
            for (int f = 0; f < 14; ++f) {
                const v4u mf = v4u{lo[f].x, hi[f].x, lo[f].y, hi[f].y};
                a0 = mfma32(wt[0][f], mf, a0); a1 = mfma32(wt[1][f], mf, a1);
            }
            const v4u gm = pack8(a0, a1);
            const f32x4 z4 = {0.f, 0.f, 0.f, 0.f};
            const f32x4 d0 = mfma32(gm, xa, z4), d1 = mfma32(gm, xb2, z4);
            *(LAS f32x4*)(L + 8192 + w * 1024 + l * 16) = f32x4{d0[0], d0[1], d1[0], d1[1]};
        }
        BAR();
        TLS(8 + s);
        if (w == 7) {
            f32x4 q[7];
#pragma unroll
            for (int k = 0; k < 7; ++k) q[k] = *(const LAS f32x4*)(L + 8192 + k * 1024 + l * 16);
            const f32x4 t = ((q[0] + q[1]) + (q[2] + q[3])) + ((q[4] + q[5]) + q[6]);
            __builtin_amdgcn_raw_buffer_store_b64(v2u{sane(fbits(t[0])), sane(fbits(t[1]))}, rs, l * 8, (int)OFF_SXM + (s * 32 + grp) * 1024, 16);
            __builtin_amdgcn_raw_buffer_store_b64(v2u{sane(fbits(t[2])), sane(fbits(t[3]))}, rs, l * 8 + 512, (int)OFF_SXM + (s * 32 + grp) * 1024, 16);
        }
    }
}

__device__ __forceinline__ void attn_item(const Params& p, const __amdgpu_buffer_rsrc_t rs, const int item) {
    const int l = threadIdx.x & 63, n = l & 15, g = l >> 4;
    const int rt = item >> 4, h = item & 15;
    const int s = rt >> 1, q = rt * 16 + n;
    const v4u qf = ldc16(rs, l * 16, (int)OFF_QF + (rt * 16 + h) * 1024);
    f32x4 sc[18];
    const f32x4 z4 = {0.f, 0.f, 0.f, 0.f};
    const float NEG = -3.0e38f;
    v4u kf[18];
#pragma unroll
    for (int k = 0; k < 18; ++k) { const int kt = max(2 * (s - 8) + k, 0); kf[k] = ldc16(rs, l * 16, (int)OFF_KF + (kt * 16 + h) * 1024); }
#pragma unroll
    for (int k = 0; k < 18; ++k) {
        const int kt = 2 * (s - 8) + k;
        f32x4 a = mfma32(kf[k], qf, z4);
        if (k < 2 || k >= 16) {
#pragma unroll
            for (int r = 0; r < 4; ++r) {
                const int dq = q - (16 * kt + 4 * g + r);
                a[r] = (dq >= 0 && dq < 256) ? a[r] : NEG;
            }
        }
        sc[k] = kt >= 0 ? a : f32x4{NEG, NEG, NEG, NEG};
    }
    FENCE();
    v4u vf[18];
#pragma unroll
    for (int k = 0; k < 18; ++k) { const int b = max(s - 8 + (k >> 1), 0); vf[k] = ldc16(rs, l * 16, (int)OFF_VF + ((b * 16 + h) * 2 + (k & 1)) * 1024); }
    float m = NEG;
#pragma unroll
    for (int k = 0; k < 18; ++k) m = fmaxf(m, fmaxf(fmaxf(sc[k][0], sc[k][1]), fmaxf(sc[k][2], sc[k][3])));
    m = xg_max(m);
    float sum = 0.f;
    f32x4 o0 = z4, o1 = z4;
#pragma unroll
    for (int blk = 0; blk < 9; ++blk) {
        f32x4 e0, e1;
#pragma unroll
        for (int r = 0; r < 4; ++r) { e0[r] = ex2(sc[2 * blk][r] - m); e1[r] = ex2(sc[2 * blk + 1][r] - m); }
        sum += ((e0[0] + e0[1]) + (e0[2] + e0[3])) + ((e1[0] + e1[1]) + (e1[2] + e1[3]));
        const v4u pb = pack8(e0, e1);
        o0 = mfma32(vf[2 * blk], pb, o0);
        o1 = mfma32(vf[2 * blk + 1], pb, o1);
    }
    sum = xg_sum(sum);
    const float f = ((const float*)(p.ws + OFF_GATE))[q] / sum;
    __builtin_amdgcn_raw_buffer_store_b128(pack8(o0 * f, o1 * f), rs, l * 16, (int)OFF_OF + (rt * 16 + h) * 1024, 16);
    asm volatile("s_waitcnt vmcnt(0)" ::: "memory");
    if (l == 0) __hip_atomic_fetch_add((GAS unsigned*)(p.ws + CT_OFC + s * 128), 1u, __ATOMIC_RELAXED, __HIP_MEMORY_SCOPE_AGENT);
}

__device__ __forceinline__ void out_item(const Params& p, LAS unsigned char* L, const __amdgpu_buffer_rsrc_t rs, const int s, const int q4, const int w, unsigned& mspin) {
    const int l = threadIdx.x & 63, n = l & 15, g = l >> 4;
    const f32x4 z4 = {0.f, 0.f, 0.f, 0.f};
    const float NEG = -3.0e38f;
    const int v = 7 * q4 + (w < 7 ? w : 0);
    f32x4 sxx[2][3];
#pragma unroll
    for (int a = 0; a < 2; ++a) { sxx[a][0] = z4; sxx[a][1] = z4; sxx[a][2] = z4; }
    {
        const int gp = (int)OFF_GF + s * (int)SEGB + (2 * w) * 28 * 1024;
        const unsigned char* xr = p.ws + OFF_XP + (size_t)s * 28672 + l * 16;
#pragma unroll 1
        for (int kh = 0; kh < 2; ++kh) {
            v4u x0[7], x1[7], kk[4][7];
#pragma unroll
            for (int j = 0; j < 7; ++j) {
                const int ks = 7 * kh + j;
                x0[j] = ldg16(xr + ks * 1024); x1[j] = ldg16(xr + (14 + ks) * 1024);
#pragma unroll
                for (int t = 0; t < 4; ++t) kk[t][j] = ldc16(rs, l * 16, gp + (t * 14 + ks) * 1024);
            }
            FENCE();
#pragma unroll
            for (int j = 0; j < 7; ++j) {
                const v4u xa = v4u{x0[j].x, x0[j].z, x0[j].y, x0[j].w}, xb = v4u{x1[j].x, x1[j].z, x1[j].y, x1[j].w};
#pragma unroll
                for (int a = 0; a < 2; ++a) {
                    sxx[a][0] = mfma32(kk[2 * a][j], xa, sxx[a][0]);
                    sxx[a][1] = mfma32(kk[2 * a][j], xb, sxx[a][1]);
                    sxx[a][2] = mfma32(kk[2 * a + 1][j], xb, sxx[a][2]);
                }
            }
            FENCE();
        }
    }
    if (w == 0) {
        for (;;) {
            asm volatile("" ::: "memory");
            const unsigned c = __builtin_amdgcn_raw_buffer_load_b32(rs, CT_OFC + s * 128, 0, 16);
            if (__builtin_amdgcn_readfirstlane(c) >= 32u || ++mspin >= SPIN_CAP) break;
            __builtin_amdgcn_s_sleep(4);
        }
    }
    BAR();
    f32x4 o0 = z4, o1 = z4;
    {
        const unsigned char* wp = p.ws + OFF_WOF + (size_t)(v * 16) * 1024 + l * 16;
#pragma unroll 1
        for (int hb = 0; hb < 16; hb += 8) {
            v4u wf[8], oa[8], ob[8];
#pragma unroll
            for (int h = 0; h < 8; ++h) {
                wf[h] = ldg16(wp + (hb + h) * 1024);
                oa[h] = ldc16(rs, l * 16, (int)OFF_OF + (2 * s * 16 + hb + h) * 1024);
                ob[h] = ldc16(rs, l * 16, (int)OFF_OF + ((2 * s + 1) * 16 + hb + h) * 1024);
            }
            FENCE();
#pragma unroll
            for (int h = 0; h < 8; ++h) { o0 = mfma32(wf[h], oa[h], o0); o1 = mfma32(wf[h], ob[h], o1); }
            FENCE();
        }
    }
    v4u uf[16];
#pragma unroll
    for (int h = 0; h < 16; ++h) uf[h] = ldc16(rs, l * 16, (int)OFF_UF + s * (int)SEGB + (v * 16 + h) * 1024);
    const float gate0 = 1.0f - ((const float*)(p.ws + OFF_GATE))[32 * s + n], gate1 = 1.0f - ((const float*)(p.ws + OFF_GATE))[32 * s + 16 + n];
    float mxx[2][2];
#pragma unroll
    for (int a = 0; a < 2; ++a) {
#pragma unroll
        for (int r = 0; r < 4; ++r) { sxx[a][0][r] = (4 * g + r <= n) ? sxx[a][0][r] : NEG; sxx[a][2][r] = (4 * g + r <= n) ? sxx[a][2][r] : NEG; }
        mxx[a][0] = fmaxf(fmaxf(sxx[a][0][0], sxx[a][0][1]), fmaxf(sxx[a][0][2], sxx[a][0][3]));
        mxx[a][1] = fmaxf(fmaxf(fmaxf(sxx[a][1][0], sxx[a][1][1]), fmaxf(sxx[a][1][2], sxx[a][1][3])), fmaxf(fmaxf(sxx[a][2][0], sxx[a][2][1]), fmaxf(sxx[a][2][2], sxx[a][2][3])));
    }
    FENCE();
    TLS(s < 40 ? 8 : 10);
    v4u sm[4];
    for (;;) {
        asm volatile("" ::: "memory");
#pragma unroll
        for (int k = 0; k < 4; ++k) {
            const v2u q0 = __builtin_amdgcn_raw_buffer_load_b64(rs, l * 8, (int)OFF_SXM + (s * 32 + 4 * w + k) * 1024, 16),
                      q1 = __builtin_amdgcn_raw_buffer_load_b64(rs, l * 8 + 512, (int)OFF_SXM + (s * 32 + 4 * w + k) * 1024, 16);
            sm[k] = v4u{q0.x, q0.y, q1.x, q1.y};
        }
        if (fresh(__builtin_elementwise_max(__builtin_elementwise_max(sm[0], sm[1]), __builtin_elementwise_max(sm[2], sm[3]))) || ++mspin >= SPIN_CAP) break;
        __builtin_amdgcn_s_sleep(6);
    }
    TLS(s < 40 ? 9 : 11);
    if (s + 2 < NSEG) {
        for (;;) {
            asm volatile("" ::: "memory");
            const unsigned x = __builtin_amdgcn_raw_buffer_load_b32(rs, 0, (int)OFF_SXM + ((s + 2) * 32 + 4 * w) * 1024, 16);
            if (__builtin_amdgcn_readfirstlane(x) != 0xFFFFFFFFu || ++mspin >= SPIN_CAP) break;
            __builtin_amdgcn_s_sleep(6);
        }
        asm volatile("" ::: "memory");
    }
    v2u yf[8];
#pragma unroll
    for (int k = 0; k < 8; ++k) yf[k] = __builtin_amdgcn_raw_buffer_load_b64(rs, l * 8, (int)OFF_YM + (s * 224 + v * 8 + k) * 512, 16);
    unsigned px[2][2][4]; unsigned pm[2][2];
#pragma unroll
    for (int qt = 0; qt < 2; ++qt) {
#pragma unroll
        for (int a = 0; a < 2; ++a) {
            const float t0 = bitsf(sm[2 * a][2 * qt]) + bitsf(sm[2 * a + 1][2 * qt]), t1 = bitsf(sm[2 * a][2 * qt + 1]) + bitsf(sm[2 * a + 1][2 * qt + 1]);
            const float m = xg_max(fmaxf(mxx[a][qt], fmaxf(t0, t1)));
            const f32x4 dg = sxx[a][qt == 0 ? 0 : 2];
            float ed[4], ef[4];
            const float e0 = ex2(t0 - m), e1 = ex2(t1 - m);
            float sum = e0 + e1;
#pragma unroll
            for (int r = 0; r < 4; ++r) {
                ed[r] = ex2(dg[r] - m); sum += ed[r];
                if (qt == 1) { ef[r] = ex2(sxx[a][1][r] - m); sum += ef[r]; }
            }
            sum = xg_sum(sum);
            const float f = (qt == 0 ? gate0 : gate1) * __builtin_amdgcn_rcpf(sum);
            if (qt == 0) { px[a][0][0] = pack2(ed[0] * f, ed[1] * f); px[a][0][1] = pack2(ed[2] * f, ed[3] * f); }
            else {
                px[a][1][0] = pack2(ef[0] * f, ef[1] * f); px[a][1][1] = pack2(ef[2] * f, ef[3] * f);
                px[a][1][2] = pack2(ed[0] * f, ed[1] * f); px[a][1][3] = pack2(ed[2] * f, ed[3] * f);
            }
            pm[qt][a] = pack2(e0 * f, e1 * f);
        }
    }
    *(LAS v4u*)(L + w * 4096 + l * 16) = v4u{px[0][0][0], px[0][0][1], px[1][0][0], px[1][0][1]};
    *(LAS v4u*)(L + w * 4096 + 1024 + l * 16) = v4u{px[0][1][0], px[0][1][1], px[0][1][2], px[0][1][3]};
    *(LAS v4u*)(L + w * 4096 + 2048 + l * 16) = v4u{px[1][1][0], px[1][1][1], px[1][1][2], px[1][1][3]};
    *(LAS v4u*)(L + w * 4096 + 3072 + l * 16) = v4u{pm[0][0], pm[0][1], pm[1][0], pm[1][1]};
    for (;;) {
        unsigned mm = 0u;
#pragma unroll
        for (int k = 0; k < 8; ++k) mm = max(mm, max(yf[k].x, yf[k].y));
        if (__all(mm != 0xFFFFFFFFu) || ++mspin >= SPIN_CAP) break;
        asm volatile("" ::: "memory");
#pragma unroll
        for (int k = 0; k < 8; ++k) yf[k] = __builtin_amdgcn_raw_buffer_load_b64(rs, l * 8, (int)OFF_YM + (s * 224 + v * 8 + k) * 512, 16);
    }
    BAR();
    if (w < 7) {
#pragma unroll
        for (int pp = 0; pp < 4; ++pp) {
            v4u pmm[2];
#pragma unroll
            for (int z = 0; z < 2; ++z) {
                const int pr = 2 * pp + z;
                const v4u u0 = uf[2 * pr], u1 = uf[2 * pr + 1];
                const v4u pa = *(const LAS v4u*)(L + pr * 4096 + l * 16), pb = *(const LAS v4u*)(L + pr * 4096 + 1024 + l * 16),
                          pc = *(const LAS v4u*)(L + pr * 4096 + 2048 + l * 16);
                pmm[z] = *(const LAS v4u*)(L + pr * 4096 + 3072 + l * 16);
                o1 = mfma32(u0, pb, o1);
                o0 = mfma32(v4u{u0.x, u0.y, u1.x, u1.y}, pa, o0);
                o1 = mfma32(u1, pc, o1);
            }
            const v4u yy = v4u{yf[2 * pp].x, yf[2 * pp].y, yf[2 * pp + 1].x, yf[2 * pp + 1].y};
            o0 = mfma32(yy, v4u{pmm[0].x, pmm[0].y, pmm[1].x, pmm[1].y}, o0);
            o1 = mfma32(yy, v4u{pmm[0].z, pmm[0].w, pmm[1].z, pmm[1].w}, o1);
        }
        stg8(p.out + (size_t)(32 * s + n) * 448 + 16 * v + 4 * g, v2u{pack2(o0[0], o0[1]), pack2(o0[2], o0[3])});
        stg8(p.out + (size_t)(32 * s + 16 + n) * 448 + 16 * v + 4 * g, v2u{pack2(o1[0], o1[1]), pack2(o1[2], o1[3])});
    }
}

__global__ void __launch_bounds__(512, 2) k_main(Params p) {
    extern __shared__ __attribute__((aligned(16))) unsigned char lds_[];
    p.ws = align_ws(p.ws);
    const int rot = __builtin_amdgcn_readfirstlane(((const int*)(p.ws + OFF_CTL))[8]);
    const int grp = (blockIdx.x + rot) & 7, u = blockIdx.x >> 3;
    LAS unsigned char* L = (LAS unsigned char*)lds_;
    const int w = __builtin_amdgcn_readfirstlane(threadIdx.x >> 6);
    TLS(0);
    if (grp == 0) {
        if (w < 2) score_wave<1, 0>(p, L, u, w); else if (w == 2) score_wave<0, 0>(p, L, u, w); else if (w == 3) score_wave<0, DLY_3>(p, L, u, w);
        else if (w < 7) score_wave<0, DLY_4>(p, L, u, w); else score_wave<2, 0>(p, L, u, w);
        TLS(7);
        return;
    }
    if (grp == 1) {
        if (u < 28) { if (w < 3) value_wave<false, false>(p, L, u, w); else if (w < 7) value_wave<false, true>(p, L, u, w); else value_wave<true, false>(p, L, u, w); }
        TLS(7);
        return;
    }
    const __amdgpu_buffer_rsrc_t rs = __builtin_amdgcn_make_buffer_rsrc(p.ws, 0, (int)WS_END, 0x00020000);
    const int k = (grp - 2) * 32 + u;
    if (k < 96) {
        if (k % 3 == 0) { serve(p, L, rs, k / 3, w); TLS(7); return; }
        sweep<true>(p, L, rs, k / 3, k % 3 - 1, w);
    } else if (k < 186) sweep<false>(p, L, rs, k / 3, k % 3, w);
    else {
        const unsigned parmask = load_parmask(p.ws);
        const int l = threadIdx.x & 63, wi = (k - 186) * 8 + w;
        const v4u ff = v4u{0xFFFFFFFFu, 0xFFFFFFFFu, 0xFFFFFFFFu, 0xFFFFFFFFu};
        __builtin_amdgcn_s_sleep(127); __builtin_amdgcn_s_sleep(127);
        for (int pg = 1; pg < NPAGE; ++pg) {
            const int base = hop_off(pg * SPP, parmask);
            for (int i = wi; i < SPP * NCHUNK * 8; i += 48) __builtin_amdgcn_raw_buffer_store_b128(ff, rs, l * 16, base + (i >> 3) * CH + (i & 7) * 1024, 16);
            for (int i = wi; i < SPP * 32 && pg * SPP * 32 + i < NSEG * 32; i += 48) __builtin_amdgcn_raw_buffer_store_b128(ff, rs, l * 16, (int)OFF_SXM + (pg * SPP * 32 + i) * 1024, 16);
            asm volatile("s_waitcnt vmcnt(0)" ::: "memory");
            BAR();
            if (threadIdx.x == 0) __hip_atomic_fetch_add((GAS unsigned*)(p.ws + CT_PZ), 1u << (4 * (pg - 1)), __ATOMIC_RELAXED, __HIP_MEMORY_SCOPE_AGENT);
        }
        if (threadIdx.x == 0) __hip_atomic_fetch_add((GAS unsigned*)(p.ws + CT_DONE), 1u, __ATOMIC_RELAXED, __HIP_MEMORY_SCOPE_AGENT);
    }
    const int wk = k < 96 ? (k / 3) * 2 + (k % 3 - 1) : k - 32;
    unsigned mspin = 0;
    if (w == 0) {
        for (;;) {
            asm volatile("" ::: "memory");
            const unsigned c = __builtin_amdgcn_raw_buffer_load_b32(rs, CT_DONE, 0, 16);
            if (__builtin_amdgcn_readfirstlane(c) >= 160u || ++mspin >= SPIN_CAP) break;
            __builtin_amdgcn_s_sleep(16);
        }
    }
    BAR();
    TLS(4);
    for (int it = wk * 8 + w; it < 2048; it += 1280) attn_item(p, rs, it);
    TLS(5);
    for (int i = 0; i < (wk * STAG) >> 4; ++i) __builtin_amdgcn_s_sleep(64);
    for (int it = wk; it < 256; it += 160) { out_item(p, L, rs, it >> 2, it & 3, w, mspin); TLS(it < 160 ? 6 : 7); }
}

extern "C" void kernel_launch(void* const* d_in, const int* in_sizes, int n_in, void* d_out, int out_size, void* d_ws, size_t ws_size,
                              hipStream_t stream) {
    Params p;
    p.x = (const u16*)d_in[0]; p.wqkv = (const u16*)d_in[1]; p.wo = (const u16*)d_in[2]; p.mem0 = (const u16*)d_in[4];
    p.Wq = (const u16*)d_in[5]; p.Wk = (const u16*)d_in[6]; p.Wv = (const u16*)d_in[7]; p.Wo = (const u16*)d_in[8];
    p.gate_w = (const u16*)d_in[10];
    p.out = (u16*)d_out; p.ws = (unsigned char*)d_ws;
    (void)hipFuncSetAttribute((const void*)k_prep, hipFuncAttributeMaxDynamicSharedMemorySize, K1_LDS);
    (void)hipFuncSetAttribute((const void*)k_main, hipFuncAttributeMaxDynamicSharedMemorySize, L_REC_END);
    k_prep<<<256, 512, K1_LDS, stream>>>(p);
    k_main<<<256, 512, L_REC_END, stream>>>(p);
}
```

```cpp
#define TL
#define PREP_TIME
#include <cstdio>
#include <hip/hip_runtime.h>
#include <hip/hip_bf16.h>
#include <stdint.h>

#define GAS __attribute__((address_space(1)))
#define LAS __attribute__((address_space(3)))
typedef __bf16 bf16x2 __attribute__((ext_vector_type(2)));
typedef __bf16 bf16x4 __attribute__((ext_vector_type(4)));
typedef __bf16 bf16x8 __attribute__((ext_vector_type(8)));
typedef __bf16 bf16x16 __attribute__((ext_vector_type(16)));
typedef short s16x4 __attribute__((ext_vector_type(4)));
typedef float f32x2 __attribute__((ext_vector_type(2)));
typedef float f32x4 __attribute__((ext_vector_type(4)));
typedef unsigned v2u __attribute__((ext_vector_type(2)));
typedef unsigned v4u __attribute__((ext_vector_type(4)));
typedef unsigned v8u __attribute__((ext_vector_type(8)));
typedef unsigned short u16;

constexpr int S_ = 2048, D_ = 448, H_ = 16, NSEG = 64;
constexpr float LOG2E = 1.4426950408889634f;

constexpr size_t SZ_W = (size_t)16 * 28 * 14 * 1024;
constexpr size_t OFF_HOP = 0;
constexpr int PAGE = 2 << 20, CH = 16384, NCHUNK = 9, STEPB = NCHUNK * CH, NPAGE = 5, SPP = 14;
constexpr int C_M = 0, C_M2 = 4 * CH, C_M3 = 5 * CH, C_S = 6 * CH, C_P = 7 * CH;
constexpr size_t HOP_BYTES = (size_t)NPAGE * PAGE;
constexpr size_t OFF_CTL = OFF_HOP + HOP_BYTES;
constexpr size_t CTL_BYTES = 36864;
constexpr int CT_DONE = (int)OFF_CTL + 256, CT_PZ = (int)OFF_CTL + 384, CT_OFC = (int)OFF_CTL + 512, CT_PROG = CT_OFC + 64 * 128;
constexpr size_t OFF_TW = OFF_CTL + CTL_BYTES;
constexpr size_t OFF_YW = OFF_TW + SZ_W;
constexpr size_t OFF_QW = OFF_YW + SZ_W;
constexpr size_t OFF_VW = OFF_QW + (size_t)32 * 28 * 1024;
constexpr size_t OFF_WOF = OFF_VW + (size_t)16 * 28 * 1024;
constexpr size_t OFF_XP = OFF_WOF + (size_t)28 * 16 * 1024;
constexpr size_t OFF_GATE = OFF_XP + (size_t)128 * 14 * 1024;
constexpr size_t SEGB = (size_t)448 * 1024;
constexpr size_t OFF_GF = OFF_GATE + 8192;
constexpr size_t OFF_UF = OFF_GF + 64 * SEGB;
constexpr size_t OFF_QF = OFF_UF + 64 * SEGB;
constexpr size_t OFF_KF = OFF_QF + (size_t)2048 * 1024;
constexpr size_t OFF_VF = OFF_KF + (size_t)2048 * 1024;
constexpr size_t OFF_OF = OFF_VF + (size_t)2048 * 1024;
constexpr size_t OFF_SXM = OFF_OF + (size_t)2048 * 1024;
constexpr size_t OFF_YM = OFF_SXM + (size_t)64 * 32 * 1024;
constexpr size_t WS_END = OFF_YM + (size_t)64 * 224 * 512;

struct Params {
    const u16* x; const u16* wqkv; const u16* wo; const u16* mem0;
    const u16* Wq; const u16* Wk; const u16* Wv; const u16* Wo; const u16* gate_w;
    u16* out; unsigned char* ws;
};
__device__ __forceinline__ unsigned char* align_ws(unsigned char* w) {
    return w + ((0 - (size_t)w) & (size_t)(PAGE - 1));
}
__device__ __forceinline__ unsigned xcc_id() { return (unsigned)__builtin_amdgcn_s_getreg(20 | (3 << 11)) & 0xfu; }
__device__ __forceinline__ unsigned load_parmask(const unsigned char* ws) {
    const unsigned* c = (const unsigned*)(ws + OFF_CTL);
    unsigned m = 0;
#pragma unroll
    for (int k = 0; k < NPAGE; ++k) m |= (c[k] & 1u) << k;
    return __builtin_amdgcn_readfirstlane(m);
}
__device__ __forceinline__ int hop_off(int s, unsigned parmask) {
    const int pg = s / SPP, sl = s - pg * SPP;
    return pg * PAGE + sl * STEPB + (int)((parmask >> pg) & 1u) * 8192;
}

__device__ __forceinline__ unsigned pack2(float a, float b) {
    return __builtin_bit_cast(unsigned, __builtin_convertvector(f32x2{a, b}, bf16x2));
}
__device__ __forceinline__ float bf2f(u16 v) { return __builtin_bit_cast(float, (unsigned)v << 16); }
__device__ __forceinline__ f32x4 mfma32(v4u a, v4u b, f32x4 c) {
    return __builtin_amdgcn_mfma_f32_16x16x32_bf16(__builtin_bit_cast(bf16x8, a), __builtin_bit_cast(bf16x8, b), c, 0, 0, 0);
}
__device__ __forceinline__ f32x4 mfma16(v2u a, v2u b, f32x4 c) {
    return __builtin_amdgcn_mfma_f32_16x16x16bf16_1k(__builtin_bit_cast(s16x4, a), __builtin_bit_cast(s16x4, b), c, 0, 0, 0);
}
__device__ __forceinline__ f32x4 smfmac(v4u a, v8u b, f32x4 c, int idx) {
    return __builtin_amdgcn_smfmac_f32_16x16x64_bf16(__builtin_bit_cast(bf16x8, a), __builtin_bit_cast(bf16x16, b), c, idx, 0, 0);
}
__device__ __forceinline__ v4u ldg16(const void* p) { return *(const GAS v4u*)p; }
__device__ __forceinline__ v2u ldg8(const void* p) { return *(const GAS v2u*)p; }
__device__ __forceinline__ void stg16(void* p, v4u v) { *(GAS v4u*)p = v; }
__device__ __forceinline__ void stg8(void* p, v2u v) { *(GAS v2u*)p = v; }
__device__ __forceinline__ v2u tr_read(const LAS void* p) {
    return __builtin_bit_cast(v2u, __builtin_amdgcn_ds_read_tr16_b64_v4i16((LAS s16x4*)p));
}
__device__ __forceinline__ float ror8(float v) {
    return __builtin_bit_cast(float, __builtin_amdgcn_update_dpp(0, __builtin_bit_cast(int, v), 0x128, 0xf, 0xf, false));
}
__device__ __forceinline__ unsigned fbits(float v) { return __builtin_bit_cast(unsigned, v); }
__device__ __forceinline__ float bitsf(unsigned v) { return __builtin_bit_cast(float, v); }
__device__ __forceinline__ float xg_max(float v) {
    const v2u a = __builtin_amdgcn_permlane16_swap(fbits(v), fbits(v), false, false);
    v = fmaxf(bitsf(a.x), bitsf(a.y));
    const v2u b = __builtin_amdgcn_permlane32_swap(fbits(v), fbits(v), false, false);
    return fmaxf(bitsf(b.x), bitsf(b.y));
}
__device__ __forceinline__ float xg_sum(float v) {
    const v2u a = __builtin_amdgcn_permlane16_swap(fbits(v), fbits(v), false, false);
    v = bitsf(a.x) + bitsf(a.y);
    const v2u b = __builtin_amdgcn_permlane32_swap(fbits(v), fbits(v), false, false);
    return bitsf(b.x) + bitsf(b.y);
}
__device__ __forceinline__ float fold32(float a, float b) {
    const v2u r = __builtin_amdgcn_permlane32_swap(fbits(a), fbits(b), false, false);
    return bitsf(r.x) + bitsf(r.y);
}
__device__ __forceinline__ float ex2(float v) { return __builtin_amdgcn_exp2f(v); }
__device__ __forceinline__ unsigned sane(unsigned v) { return v < 0xFFFFFFFEu ? v : 0xFFFFFFFEu; }

__device__ __forceinline__ int porder(int f, int g, int e) {
    return 64 * (f >> 1) + 16 * (f & 1) + 4 * g + 32 * ((e >> 1) & 1) + 2 * (e >> 2) + (e & 1);
}
constexpr int NU_HOP = SPP * NCHUNK * 16;
template <bool MIDBAR>
__device__ __forceinline__ void misc_wave(const Params& p, const int mw, const int l) {
    const int n = l & 15, g = l >> 4;
    v2u xl[4], xh[4];
#pragma unroll
    for (int k = 0; k < 4; ++k) {
        const int i = mw * 4 + k, rt = i / 14, f = i % 14;
        const u16* row = p.x + (size_t)(rt * 16 + n) * 448 + 64 * (f >> 1) + 16 * (f & 1) + 4 * g;
        xl[k] = ldg8(row); xh[k] = ldg8(row + 32);
    }
    unsigned qv[3][8];
    bool qok[3];
#pragma unroll
    for (int k = 0; k < 3; ++k) {
        const int i = mw * 3 + k;
        const bool isv = i >= 896;
        const int fr = isv ? i - 896 : i;
        const int unit = fr / 28, t = (fr % 28) / 14, f = fr % 14;
        const int d = isv ? 16 * t + n : 8 * (n >> 2) + 4 * t + (n & 3);
        const int col = (isv ? 896 + unit * 28 : (unit >> 4) * 448 + (unit & 15) * 28) + (d < 28 ? d : 0);
        qok[k] = d < 28;
#pragma unroll
        for (int e = 0; e < 8; ++e) qv[k][e] = p.wqkv[(size_t)porder(f, g, e) * 1344 + col];
    }
    unsigned wv[8];
#pragma unroll
    for (int e = 0; e < 8; ++e) {
        const int d = (e < 4) ? 4 * g + e : 16 + 4 * g + e - 4;
        wv[e] = p.wo[(size_t)((mw & 15) * 28 + (d < 28 ? d : 0)) * 448 + 16 * (mw >> 4) + n];
        if (d >= 28) wv[e] = 0u;
    }
    v4u gxv[7], ggv[7];
    const int row = (mw & 255) * 8 + (l >> 3), c0 = (l & 7) * 8;
#pragma unroll
    for (int k = 0; k < 7; ++k) { gxv[k] = ldg16(p.x + (size_t)row * 448 + k * 64 + c0); ggv[k] = ldg16(p.gate_w + k * 64 + c0); }
    for (int i = mw; i < NU_HOP; i += 448) {
        const int st = i / (NCHUNK * 16), ck = (i >> 4) % NCHUNK, kb = i & 15;
        v4u val = v4u{0xFFFFFFFFu, 0xFFFFFFFFu, 0xFFFFFFFFu, 0xFFFFFFFFu};
        if (st == 0 && ck < 6 && (kb & 7) < 7) {
            const int im = (kb & 7) * 64 + l;
            const v4u m = ldg16(p.mem0 + (size_t)(im & 7) * 448 + (im >> 3) * 8);
            val = v4u{sane(m.x), sane(m.y), sane(m.z), sane(m.w)};
        }
        stg16(p.ws + OFF_HOP + (size_t)hop_off(st, 0u) + ck * CH + kb * 1024 + l * 16, val);
    }
    if (MIDBAR) { __builtin_amdgcn_sched_barrier(0); __builtin_amdgcn_s_barrier(); __builtin_amdgcn_sched_barrier(0); }
    static_assert(SPP * 32 <= 448, "one KiB of the first page's S_xm slots per wave");
    if (mw < SPP * 32) stg16(p.ws + OFF_SXM + (size_t)mw * 1024 + l * 16, v4u{0xFFFFFFFFu, 0xFFFFFFFFu, 0xFFFFFFFFu, 0xFFFFFFFFu});
    if (mw < 224) stg16(p.ws + OFF_YM + (size_t)62 * 224 * 512 + (size_t)mw * 1024 + l * 16, v4u{0xFFFFFFFFu, 0xFFFFFFFFu, 0xFFFFFFFFu, 0xFFFFFFFFu});
#pragma unroll
    for (int k = 0; k < 4; ++k) stg16(p.ws + OFF_XP + (size_t)(mw * 4 + k) * 1024 + l * 16, v4u{xl[k].x, xh[k].x, xl[k].y, xh[k].y});
#pragma unroll
    for (int k = 0; k < 3; ++k) {
        const v4u o = v4u{qv[k][0] | (qv[k][1] << 16), qv[k][2] | (qv[k][3] << 16), qv[k][4] | (qv[k][5] << 16), qv[k][6] | (qv[k][7] << 16)};
        stg16(p.ws + OFF_QW + (size_t)(mw * 3 + k) * 1024 + l * 16, qok[k] ? o : v4u{0u, 0u, 0u, 0u});
    }
    stg16(p.ws + OFF_WOF + (size_t)mw * 1024 + l * 16, v4u{wv[0] | (wv[1] << 16), wv[2] | (wv[3] << 16), wv[4] | (wv[5] << 16), wv[6] | (wv[7] << 16)});
    float s = 0.f;
#pragma unroll
    for (int k = 0; k < 7; ++k)
#pragma unroll
        for (int j = 0; j < 4; ++j) {
            s += bitsf(gxv[k][j] << 16) * bitsf(ggv[k][j] << 16);
            s += bitsf(gxv[k][j] & 0xFFFF0000u) * bitsf(ggv[k][j] & 0xFFFF0000u);
        }
    s += __shfl_xor(s, 1); s += __shfl_xor(s, 2); s += __shfl_xor(s, 4);
    if ((l & 7) == 0 && mw < 256) ((float*)(p.ws + OFF_GATE))[row] = 1.0f / (1.0f + __expf(-s));
}

constexpr int K1_RS = 528, K1_OS = 144, K1_IMG = 36864, K1_IS = 136, K1_LDS = K1_IMG + 7 * 64 * K1_IS;
#define PT(k) do { asm volatile("s_waitcnt vmcnt(0)" ::: "memory"); if (l == 0) ((unsigned*)(p.ws + WS_END + (1 << 20)))[(b * 8 + w) * 4 + (k)] = (unsigned)__builtin_amdgcn_s_memrealtime(); } while (0)
#define PT0(k) do { if (l == 0) ((unsigned*)(p.ws + WS_END + (1 << 20)))[(b * 8 + w) * 4 + (k)] = (unsigned)__builtin_amdgcn_s_memrealtime(); } while (0)
template <int mat>
__device__ __forceinline__ void prep_heavy(const Params& p, LAS unsigned char* L, const int h, const int blk, const int w) {
    const int b = blockIdx.x, tid = threadIdx.x, l = tid & 63, n = l & 15, g = l >> 4;
    v4u t[4];
    if (mat == 0) {
#pragma unroll
        for (int k = 0; k < 4; ++k) { const int u = tid + 512 * k; t[k] = ldg16(p.Wq + ((size_t)(64 * blk + (u >> 5)) * 16 + h) * 256 + (u & 31) * 8); }
    } else {
#pragma unroll
        for (int k = 0; k < 4; ++k) { const int u = tid + 512 * k; t[k] = ldg16(p.Wo + ((size_t)h * 256 + (u >> 3)) * 448 + 64 * blk + (u & 7) * 8); }
    }
    __builtin_amdgcn_sched_barrier(0);
    const int rs_ = mat == 0 ? K1_RS : K1_OS, sh = mat == 0 ? 5 : 3;
    if (w == 7) {
#pragma unroll
        for (int k = 0; k < 4; ++k) { const int u = tid + 512 * k; *(LAS v4u*)(L + (u >> sh) * rs_ + (u & ((1 << sh) - 1)) * 16) = t[k]; }
        asm volatile("s_waitcnt lgkmcnt(0)" ::: "memory");
        misc_wave<true>(p, 224 + b, l);
        PT(3);
        return;
    }
    v4u cr[4][8];
    LAS unsigned char* img = L + K1_IMG + w * 64 * K1_IS;
    {
        const __amdgpu_buffer_rsrc_t wr = __builtin_amdgcn_make_buffer_rsrc((void*)(mat == 0 ? p.Wk : p.Wv), 0, 448 * 4096 * 2, 0x00020000);
        const int vo = ((64 * w + (l >> 5)) * 16 + h) * 512 + (l & 31) * 16;
#define K1_LOAD(tt) do { _Pragma("unroll") for (int j = 0; j < 8; ++j) \
            cr[tt][j] = __builtin_amdgcn_raw_buffer_load_b128(wr, vo, (16 * ((tt) >> 1) + 2 * ((tt) & 1) + 4 * (j >> 1) + 32 * (j & 1)) * 8192, 0); \
            __builtin_amdgcn_sched_barrier(0); } while (0)
#define K1_TURN(tt) do { \
        _Pragma("unroll") for (int j = 0; j < 8; ++j) *(LAS v4u*)(img + (2 * j + (l >> 5)) * K1_RS + (l & 31) * 16) = cr[tt][j]; \
        asm volatile("" ::: "memory"); \
        _Pragma("unroll") for (int es = 0; es < 8; ++es) cr[tt][es] = *(const LAS v4u*)(img + n * K1_RS + es * 64 + g * 16); \
        asm volatile("" ::: "memory"); __builtin_amdgcn_sched_barrier(0); } while (0)
        K1_LOAD(0); K1_LOAD(1); K1_LOAD(2);
#pragma unroll
        for (int k = 0; k < 4; ++k) { const int u = tid + 512 * k; *(LAS v4u*)(L + (u >> sh) * rs_ + (u & ((1 << sh) - 1)) * 16) = t[k]; }
        __builtin_amdgcn_sched_barrier(0);
        K1_TURN(0);
        K1_LOAD(3);
        K1_TURN(1); K1_TURN(2); K1_TURN(3);
    }
    asm volatile("s_waitcnt lgkmcnt(0)" ::: "memory");
    __builtin_amdgcn_s_barrier();
    asm volatile("" ::: "memory");
    PT0(1);
    f32x4 acc[4][4];
#pragma unroll
    for (int x = 0; x < 4; ++x)
#pragma unroll
        for (int y = 0; y < 4; ++y) acc[x][y] = f32x4{0.f, 0.f, 0.f, 0.f};
#pragma unroll
    for (int es = 0; es < 8; ++es) {
        v4u bfr[4];
#pragma unroll
        for (int y = 0; y < 4; ++y) {
            if constexpr (mat == 0) {
                bfr[y] = *(const LAS v4u*)(L + (16 * (y >> 1) + 2 * (y & 1) + 4 * (n >> 2) + 32 * ((n >> 1) & 1) + (n & 1)) * K1_RS + es * 64 + g * 16);
            } else {
                const LAS unsigned char* rp = L + (32 * es + 8 * g + (n >> 2)) * K1_OS + y * 32 + (l & 3) * 8;
                const v2u lo = tr_read(rp), hi = tr_read(rp + 4 * K1_OS);
                bfr[y] = v4u{lo.x, lo.y, hi.x, hi.y};
            }
        }
#pragma unroll
        for (int x = 0; x < 4; ++x)
#pragma unroll
            for (int y = 0; y < 4; ++y) acc[x][y] = mfma32(cr[x][es], bfr[y], acc[x][y]);
    }
    __builtin_amdgcn_sched_barrier(0); PT0(2); __builtin_amdgcn_sched_barrier(0);
    constexpr float SC = mat == 0 ? LOG2E / 16.0f : 1.0f;
#pragma unroll
    for (int y = 0; y < 4; ++y) {
        unsigned char* d = mat == 0 ? p.ws + OFF_TW + ((size_t)(h * 28 + 4 * blk + y) * 7 + w) * 2048 + l * 16
                                    : p.ws + OFF_YW + ((size_t)((4 * blk + y) * 16 + h) * 7 + w) * 2048 + l * 16;
#pragma unroll
        for (int a = 0; a < 2; ++a) {
            const f32x4 lo = acc[2 * a][y] * SC, hi = acc[2 * a + 1][y] * SC;
            stg16(d + a * 1024, v4u{pack2(lo[0], lo[1]), pack2(lo[2], lo[3]), pack2(hi[0], hi[1]), pack2(hi[2], hi[3])});
        }
    }
    PT(3);
}

__global__ void __launch_bounds__(512, 2) k_prep(Params p) {
    extern __shared__ __attribute__((aligned(16))) unsigned char lds_[];
    LAS unsigned char* L = (LAS unsigned char*)lds_;
    p.ws = align_ws(p.ws);
    const int b = blockIdx.x, tid = threadIdx.x;
    const int l = tid & 63, w = __builtin_amdgcn_readfirstlane(tid >> 6);
    PT0(0);
    if (threadIdx.x == 0) ((unsigned*)(p.ws + WS_END + (4 << 20)))[blockIdx.x * 128 + 126] = (unsigned)__builtin_amdgcn_s_getreg(20 | (3 << 11)) & 0xfu;
    if (b >= 224) {
        const int k = b - 224;
        if ((k & 7) == 0 && w < 2) {
            const int pg = (k >> 3) + 4 * w;
            if (pg < NPAGE && l == 0) {
                GAS unsigned* a = (GAS unsigned*)(p.ws + OFF_HOP + (size_t)pg * PAGE + 126 * CH);
                unsigned best[4] = {~0u, ~0u, ~0u, ~0u};
                for (int it = 0; it < 3; ++it) {
#pragma unroll
                    for (int q = 0; q < 4; ++q) {
                        const unsigned long long t0 = __builtin_amdgcn_s_memtime();
                        __hip_atomic_store(a + q * 1024, 0xFFFFFFFFu, __ATOMIC_RELAXED, __HIP_MEMORY_SCOPE_AGENT);
                        asm volatile("s_waitcnt vmcnt(0)" ::: "memory");
                        const unsigned dt = (unsigned)(__builtin_amdgcn_s_memtime() - t0);
                        best[q] = min(best[q], dt);
                    }
                }
                ((unsigned*)(p.ws + OFF_CTL))[pg] = ((best[0] + best[1] > best[2] + best[3]) ? 1u : 0u) ^ ((xcc_id() >> 2) & 1u);
            }
            PT(3);
            if (pg == 7) {
                if (l == 0) ((unsigned*)(p.ws + OFF_CTL))[8] = xcc_id() & 7u;
                for (int i = l; i < (int)(CTL_BYTES - 256) / 16; i += 64) stg16(p.ws + OFF_CTL + 256 + i * 16, v4u{0u, 0u, 0u, 0u});
            }
            return;
        }
        if ((k & 7) == 0) return;
        const int kk = k - (k >> 3) - 1;
        misc_wave<false>(p, kk * 8 + w, l);
        PT(3);
        return;
    }
    const int r = b >> 3, h = (b & 7) + 8 * (r & 1), q = r >> 1;
    if (q < 7) prep_heavy<0>(p, L, h, q, w); else prep_heavy<1>(p, L, h, q - 7, w);
}

constexpr int L_MST = 0, L_TIM = L_MST + 2 * 7168, L_RED = L_TIM + 8192, L_REC_END = 96 * 1024;
#define DLY_M 1024
#define DM0 18
#define DLY_P 0
#define DLY_PO 0
#define PPOSY 7
#define GAP_M 150
#define DLY_G 300
#define DLY_3 280
#define DLY_4 60
#define DLY_V 240
#define DLY_Y 0
#define PPOS 8
#define DLY_Q 200
#define SPA 0
#define SPB 2
#define SW_DLY 60
#define STAG 1
__device__ __forceinline__ void delay_var(int n) {
    asm volatile("s_cmp_lt_i32 %0, 1\n\ts_cbranch_scc1 2f\n1:\n\ts_nop 5\n\ts_sub_i32 %0, %0, 1\n\ts_cmp_gt_i32 %0, 0\n\ts_cbranch_scc1 1b\n2:" : "+s"(n) :: "scc", "memory");
}
template <int C> __device__ __forceinline__ void delay_cycles() {
    if constexpr (C >= 64) __builtin_amdgcn_s_sleep(C / 64 > 127 ? 127 : C / 64);
    if constexpr ((C & 63) >= 4) asm volatile("s_nop %0" :: "i"(((C & 63) >> 2) - 1));
}

__device__ __forceinline__ bool fresh(v4u x) {
    const unsigned m = max(max(x.x, x.y), max(x.z, x.w));
    return __all(m != 0xFFFFFFFFu);
}
#define BAR() do { asm volatile("s_waitcnt lgkmcnt(0)" ::: "memory"); __builtin_amdgcn_s_barrier(); asm volatile("" ::: "memory"); } while (0)
#define FENCE() do { asm volatile("" ::: "memory"); __builtin_amdgcn_sched_barrier(0); } while (0)

#define STAMP(k) do {} while (0)
#define PROF_DECL
#define PROF_ACC do {} while (0)
#define PROF_OUT(slot) do {} while (0)
typedef _Float16 f16x2 __attribute__((ext_vector_type(2)));
__device__ __forceinline__ unsigned packh(float a, float b) { return __builtin_bit_cast(unsigned, __builtin_amdgcn_cvt_pkrtz(a, b)); }
__device__ __forceinline__ float hlo(unsigned v) { const f16x2 h = __builtin_bit_cast(f16x2, v); return (float)h.x; }
__device__ __forceinline__ float hhi(unsigned v) { const f16x2 h = __builtin_bit_cast(f16x2, v); return (float)h.y; }
constexpr unsigned SPIN_CAP = 1u << 20;

__device__ __forceinline__ unsigned pkmaxh(unsigned a, unsigned b) { unsigned r; asm("v_pk_max_f16 %0, %1, %2" : "=v"(r) : "v"(a), "v"(b)); return r; }
__device__ __forceinline__ float vmax(float a, float b) { float r; asm("v_max_f32 %0, %1, %2" : "=v"(r) : "v"(a), "v"(b)); return r; }
__device__ __forceinline__ float xg_max_raw(float v) {
    const v2u a = __builtin_amdgcn_permlane16_swap(fbits(v), fbits(v), false, false);
    v = vmax(bitsf(a.x), bitsf(a.y));
    const v2u b = __builtin_amdgcn_permlane32_swap(fbits(v), fbits(v), false, false);
    return vmax(bitsf(b.x), bitsf(b.y));
}
__device__ __forceinline__ float addh_lo(unsigned a, unsigned b) { float r; asm("v_fma_mix_f32 %0, %1, 1.0, %2 op_sel_hi:[1,0,1]" : "=v"(r) : "v"(a), "v"(b)); return r; }
__device__ __forceinline__ float addh_hi(unsigned a, unsigned b) { float r; asm("v_fma_mix_f32 %0, %1, 1.0, %2 op_sel:[1,0,1] op_sel_hi:[1,0,1]" : "=v"(r) : "v"(a), "v"(b)); return r; }
__device__ __forceinline__ float subh_lo(unsigned a, float b) { float r; asm("v_fma_mix_f32 %0, %1, 1.0, -%2 op_sel_hi:[1,0,0]" : "=v"(r) : "v"(a), "v"(b)); return r; }
__device__ __forceinline__ float subh_hi(unsigned a, float b) { float r; asm("v_fma_mix_f32 %0, %1, 1.0, -%2 op_sel:[1,0,0] op_sel_hi:[1,0,0]" : "=v"(r) : "v"(a), "v"(b)); return r; }
__device__ __forceinline__ float add_ror8(float a) { float r; asm("s_nop 1\n\tv_add_f32_dpp %0, %1, %1 row_ror:8 row_mask:0xf bank_mask:0xf" : "=v"(r) : "v"(a)); return r; }
#define UNLIKELY(x) __builtin_expect(!!(x), 0)
#define CNT(x) do {} while (0)

#define TLS(k) do { if (threadIdx.x == 0) ((unsigned*)(p.ws + WS_END + (4 << 20)))[blockIdx.x * 128 + (k)] = (unsigned)__builtin_amdgcn_s_memrealtime(); } while (0)
#define TLS7(k) do {} while (0)
template <int NG, int STR>
__device__ __forceinline__ int wait_ready(const __amdgpu_buffer_rsrc_t rs, const int g0, const int need, unsigned& mspin) {
    const int l = threadIdx.x & 63;
    int ready;
    for (;;) {
        asm volatile("" ::: "memory");
        const int v = (int)__builtin_amdgcn_raw_buffer_load_b32(rs, CT_PROG + (g0 * 3 + (l < 3 * NG ? l : 0)) * 128, 0, 16);
        ready = 1 << 20;
#pragma unroll
        for (int k = 0; k < 3 * NG; ++k) if (k % 3 < STR) ready = min(ready, STR * __builtin_amdgcn_readlane(v, k) + (k % 3));
        if (ready > need || ++mspin >= SPIN_CAP) break;
        __builtin_amdgcn_s_sleep(2);
    }
    asm volatile("" ::: "memory");
    return ready;
}

template <int ROLE, int DLY1>
__device__ __forceinline__ void score_wave(const Params& p, LAS unsigned char* L, const int u, const int w) {
    constexpr bool SPECIAL = ROLE == 2;
    const int l = threadIdx.x & 63;
    const int n = l & 15, g = l >> 4, i8 = l & 7, hh = (l >> 3) & 1;
    const int half = u & 1;
    const __amdgpu_buffer_rsrc_t rs = __builtin_amdgcn_make_buffer_rsrc(p.ws, 0, (int)WS_END, 0x00020000);
    const int idx = hh ? 0xEEEE : 0x4444;
    const unsigned parmask = load_parmask(p.ws);
    v8u tw[SPECIAL ? 1 : 14];
    v4u gx[SPECIAL ? 14 : 1];
    const LAS unsigned char* mrd[SPECIAL ? 1 : 7];
    if constexpr (!SPECIAL) {
        const unsigned char* src = p.ws + OFF_TW + ((size_t)(u * 14 + 2 * w) * 7) * 2048 + l * 16;
#pragma unroll
        for (int k = 0; k < 7; ++k) {
            const int f = w + k < 7 ? w + k : w + k - 7;
            mrd[k] = L + L_MST + f * 1024 + l * 16;
#pragma unroll
            for (int t = 0; t < 2; ++t) {
                const v4u a = ldg16(src + (7 * t + f) * 2048), b = ldg16(src + (7 * t + f) * 2048 + 1024);
                tw[7 * t + k] = v8u{a.x, a.y, a.z, a.w, b.x, b.y, b.z, b.w};
            }
        }
    } else {
        *(LAS v4u*)(L + L_TIM + (half ? 0 : 14 * 512) + l * 16) = v4u{0u, 0u, 0u, 0u};
        __builtin_amdgcn_s_setprio(2);
    }
    const int moff = C_M + w * 1024 + hh * 512 + g * 128 + i8 * 16;
    const int sst_off = C_S + hh * 4096 + u * 128 + (4 * (g & 1) + 2 * (g >> 1) + (l & 1)) * 16 + (i8 >> 1) * 4;
    const unsigned ssel = (l & 1) ? 0x03020706u : 0x05040100u;
    const int pst_off = hh ? 0x7FFFFFF0 : C_P + u * 256 + i8 * 32 + g * 8;
    const int goff = ((u & ~1) + ((n >> 2) & 1)) * 14336 + (8 * half + 4 * (n >> 3) + (n & 3) + 16 * g) * 16;
    const int xoff = (int)OFF_XP + (w * 14 + 7 * half) * 1024 + l * 16;
    LAS unsigned char* mq = L + L_MST + 7168 + w * 1024 + (i8 + 8 * (g >> 1) + 32 * (g & 1)) * 16 + hh * 8;
    unsigned spins = 0, mspin = 0;
    int ready = 0;
    if constexpr (!SPECIAL) {
#pragma unroll
        for (int k = 0; k < 7; ++k) asm volatile("" : "+v"(mrd[k]));
    }
    v4u xq[7], x3 = v4u{0u, 0u, 0u, 0u};
    if constexpr (ROLE == 1) {
#pragma unroll
        for (int k = 0; k < 7; ++k) xq[k] = __builtin_amdgcn_raw_buffer_load_b128(rs, xoff + k * 1024, (NSEG - 1) * 28672, 0);
    }
    if constexpr (SPECIAL) {
        ready = wait_ready<2, 2>(rs, u & ~1, 0, mspin);
#pragma unroll
        for (int f = 0; f < 14; ++f) gx[f] = __builtin_amdgcn_raw_buffer_load_b128(rs, goff + f * 1024, (int)OFF_GF, 16);
    }
    PROF_DECL
    for (int s = 0; s < NSEG; ++s) {
        const int ho = hop_off(s, parmask);
        FENCE();
        STAMP(0);
        if constexpr (!SPECIAL) {
            if (s) { delay_cycles<DLY_M>(); delay_var(DM0); }
            FENCE();
            v4u xs = __builtin_amdgcn_raw_buffer_load_b128(rs, moff, ho, 16);
            FENCE(); delay_cycles<GAP_M>(); FENCE();
            const v4u x1 = __builtin_amdgcn_raw_buffer_load_b128(rs, moff, ho + CH, 16);
            FENCE(); delay_cycles<GAP_M>(); FENCE();
            const v4u x2 = __builtin_amdgcn_raw_buffer_load_b128(rs, moff, ho + 2 * CH, 16);
            FENCE();
#define MOV4(d_, s_) do { asm volatile("v_mov_b64 %0, %1" : "=v"(d_.lo) : "v"(s_.lo)); asm volatile("v_mov_b64 %0, %1" : "=v"(d_.hi) : "v"(s_.hi)); } while (0)
            if (UNLIKELY(!fresh(xs))) {
                FENCE();
                x3 = __builtin_amdgcn_raw_buffer_load_b128(rs, moff, ho + 3 * CH, 16);
                FENCE();
                CNT(1);
                MOV4(xs, x1);
                if (!fresh(xs)) {
                    CNT(1);
                    MOV4(xs, x2);
                    if (!fresh(xs)) {
                        CNT(1);
                        MOV4(xs, x3);
                        if (!fresh(xs)) {
                            for (;;) {
                                asm volatile("" ::: "memory");
                                xs = __builtin_amdgcn_raw_buffer_load_b128(rs, moff, ho + 3 * CH, 16);
                                CNT(1);
                                if (fresh(xs) || ++mspin >= SPIN_CAP) break;
                            }
                        }
                    }
                }
            }
            STAMP(1);
            *(LAS v4u*)mrd[0] = xs;
            *(LAS v2u*)mq = v2u{xs.x, xs.y};
            *(LAS v2u*)(mq + 256) = v2u{xs.z, xs.w};
            FENCE();
            f32x4 a0 = {0.f, 0.f, 0.f, 0.f}, a1 = a0;
            a0 = smfmac(xs, tw[0], a0, idx); a1 = smfmac(xs, tw[7], a1, idx);
            asm volatile("" : "+v"(a0), "+v"(a1));
            FENCE();
            BAR();
            STAMP(2);
            v4u mp[7];
            delay_cycles<DLY1>();
            FENCE();
#pragma unroll
            for (int k = 1; k < 7; ++k) mp[k] = *(const LAS v4u*)mrd[k];
            FENCE();
#pragma unroll
            for (int k = 1; k < 7; ++k) { a0 = smfmac(mp[k], tw[k], a0, idx); a1 = smfmac(mp[k], tw[7 + k], a1, idx); }
            *(LAS v2u*)(L + L_TIM + (2 * w + 2 * half) * 512 + n * 32 + g * 8) = v2u{pack2(a0[0], a0[1]), pack2(a0[2], a0[3])};
            *(LAS v2u*)(L + L_TIM + (2 * w + 2 * half + 1) * 512 + n * 32 + g * 8) = v2u{pack2(a1[0], a1[1]), pack2(a1[2], a1[3])};
            STAMP(3);
            BAR();
            STAMP(4);
            asm volatile("" : "+v"(x3) : "v"(x1), "v"(x2));
            TLS(8 + s);
            if (ROLE == 1 && s == NSEG - 1) {
                v4u ta[7];
#pragma unroll
                for (int k = 0; k < 7; ++k) {
                    const v2u t0 = tr_read(L + L_TIM + (2 * k + 2 * half) * 512 + l * 8), t1 = tr_read(L + L_TIM + (2 * k + 2 * half + 1) * 512 + l * 8);
                    ta[k] = v4u{t0.x, t0.y, t1.x, t1.y};
                }
                FENCE();
                f32x4 d0 = {0.f, 0.f, 0.f, 0.f};
#pragma unroll
                for (int k = 0; k < 7; ++k) d0 = mfma32(ta[k], xq[k], d0);
                __builtin_amdgcn_raw_buffer_store_b64(v2u{sane(fbits(fold32(d0[0], d0[2]))), sane(fbits(fold32(d0[1], d0[3])))}, rs, w * 512 + l * 8,
                                                      (int)OFF_SXM + (s * 32 + u) * 1024, 16);
                FENCE();
            }
        } else {
            BAR();
            STAMP(2);
            v4u mp[7];
#pragma unroll
            for (int f = 0; f < 7; ++f) mp[f] = *(const LAS v4u*)(L + L_MST + 7168 + f * 1024 + l * 16);
            FENCE();
            f32x4 ae = {0.f, 0.f, 0.f, 0.f}, ao = ae;
            v4u m2[4];
#pragma unroll
            for (int f = 0; f < 7; ++f) {
                ae = mfma32(gx[2 * f], mp[f], ae);
                ao = mfma32(gx[2 * f + 1], mp[f], ao);
                asm volatile("" : "+v"(ae), "+v"(ao));
            }
            FENCE();
#pragma unroll
            for (int b = 0; b < 4; ++b) m2[b] = *(const LAS v4u*)(L + L_MST + (3 * half + b) * 1024 + l * 16);
            FENCE();
            const v2u pv = v2u{sane(packh(ae[0] + ror8(ao[0]), ae[1] + ror8(ao[1]))), sane(packh(ae[2] + ror8(ao[2]), ae[3] + ror8(ao[3])))};
            __builtin_amdgcn_raw_buffer_store_b64(pv, rs, pst_off, ho, 16);
            __builtin_amdgcn_raw_buffer_store_b64(pv, rs, pst_off, ho + CH, 16);
            FENCE();
            STAMP(3);
            f32x4 sa = {0.f, 0.f, 0.f, 0.f};
            asm volatile("" : "+v"(sa));
            BAR();
            STAMP(4);
            v8u tb[4];
#pragma unroll
            for (int b = 0; b < 4; ++b) {
                const v2u t0 = tr_read(L + L_TIM + (4 * b) * 512 + l * 8), t1 = tr_read(L + L_TIM + (4 * b + 1) * 512 + l * 8),
                          t2 = tr_read(L + L_TIM + (4 * b + 2) * 512 + l * 8), t3 = tr_read(L + L_TIM + (4 * b + 3) * 512 + l * 8);
                tb[b] = v8u{t0.x, t0.y, t1.x, t1.y, t2.x, t2.y, t3.x, t3.y};
            }
            FENCE();
#pragma unroll
            for (int b = 0; b < 4; ++b) sa = smfmac(m2[b], tb[b], sa, idx);
            const unsigned pk = packh(add_ror8(fold32(sa[0], sa[2])), add_ror8(fold32(sa[1], sa[3])));
            const unsigned nb = __builtin_amdgcn_update_dpp(0u, pk, 0xB1, 0xf, 0xf, true);
            __builtin_amdgcn_raw_buffer_store_b32(sane(__builtin_amdgcn_perm(nb, pk, ssel)), rs, sst_off, ho, 16);
            FENCE();
            STAMP(5);
            delay_cycles<DLY_G>();
            if (UNLIKELY(ready <= s + 1 && s + 1 < NSEG)) ready = wait_ready<2, 2>(rs, u & ~1, s + 1, mspin);
            FENCE();
#pragma unroll
            for (int f = 0; f < 14; ++f) gx[f] = __builtin_amdgcn_raw_buffer_load_b128(rs, goff + f * 1024, (int)(OFF_GF + (s + 1 < NSEG ? s + 1 : s) * SEGB), 16);
            FENCE();
        }
        PROF_ACC;
    }
    PROF_OUT(u * 8 + w);
}

template <bool LAST, bool YOUNG>
__device__ __forceinline__ void value_wave(const Params& p, LAS unsigned char* L, const int v, const int w) {
    const int l = threadIdx.x & 63;
    const int g = l >> 4, i8 = l & 7, hh = (l >> 3) & 1;
    const __amdgpu_buffer_rsrc_t rs = __builtin_amdgcn_make_buffer_rsrc(p.ws, 0, (int)WS_END, 0x00020000);
    const int idx = hh ? 0xEEEE : 0x4444;
    const unsigned parmask = load_parmask(p.ws);
    v8u yw[14];
    const LAS unsigned char* mrd[7];
    {
        const unsigned char* src = p.ws + OFF_YW + (size_t)(v * 8 + w) * 14 * 2048 + l * 16;
#pragma unroll
        for (int k = 0; k < 7; ++k) {
            const int f = w + k < 7 ? w + k : w + k - 7;
            mrd[k] = L + L_MST + f * 1024 + l * 16;
#pragma unroll
            for (int t = 0; t < 2; ++t) {
                const v4u a = ldg16(src + (7 * t + f) * 2048), b = ldg16(src + (7 * t + f) * 2048 + 1024);
                yw[7 * t + k] = v8u{a.x, a.y, a.z, a.w, b.x, b.y, b.z, b.w};
            }
        }
    }
    if (LAST) __builtin_amdgcn_s_setprio(2);
    const int moff = C_M2 + w * 1024 + hh * 512 + g * 128 + i8 * 16;
    const int mst_off = C_M + v * 256 + ((l >> 2) & 1) * 128 + (l >> 3) * 16 + (l & 3) * 4;
    const int soff = C_S + ((2 * w + hh) * 2 + (g >> 1)) * 128 + i8 * 16 + (g & 1) * 8;
    const int poff = C_P + ((2 * w + hh) * 2 + (g >> 1)) * 256 + i8 * 32 + (g & 1) * 16;
    const int yst_off = (int)OFF_YM + (v * 8 + w) * 512 + l * 8;
    const int uoff = (v * 8 + w) * 2048 + l * 16;
    unsigned mlo = hh ? 0u : 0xFFFFFFFFu, mhi = ~mlo;
    v4u ones = v4u{0x3F803F80u, 0x3F803F80u, 0x3F803F80u, 0x3F803F80u};
    asm volatile("" : "+v"(mlo), "+v"(mhi), "+v"(ones));
#pragma unroll
    for (int k = 0; k < 7; ++k) asm volatile("" : "+v"(mrd[k]));
    unsigned spins = 0, mspin = 0;
    PROF_DECL
    int ho = hop_off(0, parmask);
    int ready = wait_ready<1, 3>(rs, 32 + v, 0, mspin);
    v4u ua = __builtin_amdgcn_raw_buffer_load_b128(rs, uoff, (int)OFF_UF, 16);
#define V_Y_PHASE(MIDA, MIDB) \
        f32x4 y0 = {0.f, 0.f, 0.f, 0.f}, y1 = y0; \
        if constexpr (!LAST) { \
            delay_cycles<DLY_V>(); \
            FENCE(); \
            v4u x = __builtin_amdgcn_raw_buffer_load_b128(rs, moff, ho, 16); \
            if (UNLIKELY(!fresh(x))) { \
                for (;;) { asm volatile("" ::: "memory"); x = __builtin_amdgcn_raw_buffer_load_b128(rs, moff, ho, 16); CNT(1); if (fresh(x) || ++mspin >= SPIN_CAP) break; } \
            } \
            STAMP(1); \
            *(LAS v4u*)mrd[0] = x; \
            FENCE(); \
            y0 = smfmac(x, yw[0], y0, idx); y1 = smfmac(x, yw[7], y1, idx); \
            asm volatile("" : "+v"(y0), "+v"(y1)); \
            FENCE(); \
        } \
        BAR(); \
        STAMP(2); \
        v4u mp[7]; \
        if constexpr (YOUNG) delay_cycles<DLY_Y>(); \
        FENCE(); \
        _Pragma("unroll") for (int k = LAST ? 0 : 1; k < 7; ++k) mp[k] = *(const LAS v4u*)mrd[k]; \
        FENCE(); \
        MIDA; \
        _Pragma("unroll") for (int k = LAST ? 0 : 1; k < 7; ++k) { \
            if (k == (YOUNG ? PPOSY : PPOS)) { MIDB; } \
            y0 = smfmac(mp[k], yw[k], y0, idx); y1 = smfmac(mp[k], yw[7 + k], y1, idx); \
        } \
        if ((YOUNG ? PPOSY : PPOS) == 7) { MIDB; } \
        v2u yop = v2u{pack2(fold32(y0[0], y0[2]), fold32(y0[1], y0[3])), pack2(fold32(y1[0], y1[2]), fold32(y1[1], y1[3]))}; \
        asm volatile("" : "+v"(yop)); \
        FENCE(); \
        STAMP(3)
    for (int s = 0; s < NSEG - 1; ++s) {
        const int hn = hop_off(s + 1, parmask);
        const int uo = (int)(OFF_UF + (s + 1) * SEGB);
        if (UNLIKELY(ready <= s + 1)) ready = wait_ready<1, 3>(rs, 32 + v, s + 1, mspin);
        if constexpr (LAST) {
            if (UNLIKELY((s + 2) % SPP == 0)) {
                const int sh = 4 * ((s + 2) / SPP - 1);
                for (;;) {
                    asm volatile("" ::: "memory");
                    const unsigned c = __builtin_amdgcn_readfirstlane(__builtin_amdgcn_raw_buffer_load_b32(rs, CT_PZ, 0, 16));
                    if (((c >> sh) & 15u) >= 6u || ++mspin >= SPIN_CAP) break;
                }
            }
        }
        int ho1 = ho + CH, ho2 = ho + 4096, hn1 = hn + CH, hn2 = hn + 2 * CH, hn3 = hn + 3 * CH, hnn = hn;
        asm volatile("" : "+s"(ho1), "+s"(ho2), "+s"(hn1), "+s"(hn2), "+s"(hn3), "+s"(hnn));
        FENCE();
        STAMP(0);
        v4u ub, pv;
        V_Y_PHASE(ub = __builtin_amdgcn_raw_buffer_load_b128(rs, uoff + 1024, uo - (int)SEGB, 16); FENCE(),
                  FENCE(); pv = __builtin_amdgcn_raw_buffer_load_b128(rs, poff, ho, 16); FENCE());
        if ((YOUNG ? PPOSY : PPOS) == 8) { delay_cycles<YOUNG ? DLY_P : DLY_P + DLY_PO>(); FENCE(); pv = __builtin_amdgcn_raw_buffer_load_b128(rs, poff, ho, 16); FENCE(); }
        delay_cycles<DLY_Q>();
        FENCE();
        const v4u pq = __builtin_amdgcn_raw_buffer_load_b128(rs, poff, ho1, 16);
        FENCE();
        if (UNLIKELY(!fresh(pv))) {
            CNT(1 << 20);
            asm volatile("v_mov_b64 %0, %1" : "=v"(pv.lo) : "v"(pq.lo)); asm volatile("v_mov_b64 %0, %1" : "=v"(pv.hi) : "v"(pq.hi));
            if (!fresh(pv)) {
                for (;;) {
                    asm volatile("" ::: "memory");
                    pv = __builtin_amdgcn_raw_buffer_load_b128(rs, poff, ho1, 16);
                    CNT(1 << 20);
                    if (fresh(pv) || ++mspin >= SPIN_CAP) break;
                }
            }
        }
        STAMP(7);
        v2u sv, sw2;
#define S_A() do { FENCE(); STAMP(6); sv = __builtin_amdgcn_raw_buffer_load_b64(rs, soff, ho, 16); FENCE(); } while (0)
#define S_B() do { FENCE(); sw2 = __builtin_amdgcn_raw_buffer_load_b64(rs, soff, ho2, 16); FENCE(); } while (0)
        if (SPA == 0) S_A();
        const unsigned hm = pkmaxh(pkmaxh(pv.x, pv.y), pkmaxh(pv.z, pv.w));
        const float mx = xg_max_raw(vmax(hlo(hm), hhi(hm)));
        if (SPA == 1) S_A();
        if (SPB == 1) S_B();
        const unsigned e0 = pack2(ex2(subh_lo(pv.x, mx)), ex2(subh_hi(pv.x, mx))), e1 = pack2(ex2(subh_lo(pv.y, mx)), ex2(subh_hi(pv.y, mx)));
        if (SPA == 2) S_A();
        if (SPB == 2) S_B();
        const unsigned e2 = pack2(ex2(subh_lo(pv.z, mx)), ex2(subh_hi(pv.z, mx))), e3 = pack2(ex2(subh_lo(pv.w, mx)), ex2(subh_hi(pv.w, mx)));
        const f32x4 z4 = {0.f, 0.f, 0.f, 0.f};
        const v4u eop = v4u{e0, e1, e2, e3};
        const f32x4 oa = mfma32(ua, eop, z4);
        const f32x4 ob = mfma32(ub, eop, z4);
        const f32x4 lx4 = mfma32(ones, eop, z4);
        if (SPB == 3) S_B();
        f32x4 ox = hh ? ob : oa;
        f32x4 lx = lx4;
        asm volatile("" : "+v"(ox), "+v"(lx));
        FENCE();
        STAMP(8);
        if (UNLIKELY(!__all(max(sv.x, sv.y) != 0xFFFFFFFFu))) {
            CNT(1 << 10);
            asm volatile("v_mov_b64 %0, %1" : "=v"(sv) : "v"(sw2));
            if (!__all(max(sv.x, sv.y) != 0xFFFFFFFFu)) {
                for (;;) {
                    asm volatile("" ::: "memory");
                    sv = __builtin_amdgcn_raw_buffer_load_b64(rs, soff, ho2, 16);
                    CNT(1 << 10);
                    if (__all(max(sv.x, sv.y) != 0xFFFFFFFFu) || ++mspin >= SPIN_CAP) break;
                }
            }
        }
        STAMP(9);
        const v2u sw = __builtin_amdgcn_permlane32_swap(sv.x, sv.y, false, false);
        const float t0 = addh_lo(sw.x, sw.y), t1 = addh_hi(sw.x, sw.y);
        float m;
        asm("v_max3_f32 %0, %1, %2, %3" : "=v"(m) : "v"(t0), "v"(t1), "v"(mx));
        m = xg_max_raw(m);
        const unsigned pk = pack2(ex2(t0 - m), ex2(t1 - m));
        const float beta = ex2(mx - m);
        const v2u bop = v2u{pk & mlo, pk & mhi};
        lx[0] *= beta;
        const f32x4 ls = mfma16(v2u{ones.x, ones.y}, bop, lx);
        f32x4 d = mfma16(yop, bop, ox * beta);
        const float r = __builtin_amdgcn_rcpf(ls[0]);
        d *= r;
        float r0, r1;
        asm("s_nop 1\n\tv_add_f32_dpp %0, %2, %2 row_ror:8 row_mask:0xf bank_mask:0x3\n\tv_add_f32_dpp %1, %3, %3 row_ror:8 row_mask:0xf bank_mask:0x3\n\t"
            "v_add_f32_dpp %0, %4, %4 row_ror:8 row_mask:0xf bank_mask:0xc\n\tv_add_f32_dpp %1, %5, %5 row_ror:8 row_mask:0xf bank_mask:0xc"
            : "=&v"(r0), "=&v"(r1) : "v"(d[0]), "v"(d[1]), "v"(d[2]), "v"(d[3]));
        *(LAS f32x2*)(L + L_RED + w * 512 + i8 * 64 + g * 16 + hh * 8) = f32x2{r0, r1};
        STAMP(10);
        FENCE();
        if constexpr (!LAST) ua = __builtin_amdgcn_raw_buffer_load_b128(rs, uoff, uo, 16);
        FENCE();
        BAR();
        STAMP(11);
        if constexpr (LAST) {
            f32x2 q[8];
#pragma unroll
            for (int k = 0; k < 8; ++k) q[k] = *(const LAS f32x2*)(L + L_RED + k * 512 + l * 8);
            FENCE();
            const f32x2 tot = ((q[0] + q[1]) + (q[2] + q[3])) + ((q[4] + q[5]) + (q[6] + q[7]));
            const unsigned mv = sane(pack2(tot[0], tot[1]));
            __builtin_amdgcn_raw_buffer_store_b32(mv, rs, mst_off, hn3, 16);
            __builtin_amdgcn_raw_buffer_store_b32(mv, rs, mst_off, hn2, 16);
            __builtin_amdgcn_raw_buffer_store_b32(mv, rs, mst_off, hn1, 16);
            __builtin_amdgcn_raw_buffer_store_b32(mv, rs, mst_off, hnn, 16);
            __builtin_amdgcn_raw_buffer_store_b32(mv, rs, mst_off, hn + C_M2, 16);
            __builtin_amdgcn_raw_buffer_store_b32(mv, rs, mst_off, hn + C_M3, 16);
            FENCE();
            asm volatile("" :: "v"(sw2), "v"(pq));
            ua = __builtin_amdgcn_raw_buffer_load_b128(rs, uoff, uo, 16);
        } else {
            delay_cycles<256>();
            asm volatile("" :: "v"(sw2), "v"(pq));
        }
        __builtin_amdgcn_raw_buffer_store_b64(v2u{sane(yop.x), sane(yop.y)}, rs, yst_off, s * 224 * 512, 16);
        ho = hn;
        if constexpr (LAST) PROF_ACC;
    }
    {
        const int s = NSEG - 1;
        V_Y_PHASE(, );
        __builtin_amdgcn_raw_buffer_store_b64(v2u{sane(yop.x), sane(yop.y)}, rs, yst_off, s * 224 * 512, 16);
    }
    PROF_OUT(256 + v * 8 + w);
}

__device__ __forceinline__ void glds16(const void* gsrc, unsigned lds_dst) {
    unsigned keep;
    asm volatile("s_mov_b32 %0, m0\n\ts_mov_b32 m0, %2\n\ts_nop 0\n\tglobal_load_lds_dwordx4 %1, off\n\ts_mov_b32 m0, %0"
                 : "=&s"(keep) : "v"(gsrc), "s"(lds_dst) : "memory");
}
__device__ __forceinline__ v4u ldc16(const __amdgpu_buffer_rsrc_t rs, int voff, int soff) { return __builtin_amdgcn_raw_buffer_load_b128(rs, voff, soff, 16); }
__device__ __forceinline__ v4u pack8(f32x4 a, f32x4 b) { return v4u{pack2(a[0], a[1]), pack2(a[2], a[3]), pack2(b[0], b[1]), pack2(b[2], b[3])}; }
constexpr int L_XB = 0, XBUF = 28672;

template <bool GTYPE>
__device__ __forceinline__ void sweep(const Params& p, LAS unsigned char* L, const __amdgpu_buffer_rsrc_t rs, const int grp, const int r, const int w) {
    constexpr int STR = GTYPE ? 2 : 3;
    const int l = threadIdx.x & 63;
    const unsigned lbase = (unsigned)(size_t)L;
    const unsigned char* wsrc;
    int obase, ostride, osec;
    float oscale = 1.0f;
    if (GTYPE) {
        const int ks = 7 * (grp & 1) + w;
        if (w < 7) { wsrc = p.ws + OFF_TW + (size_t)((grp >> 1) * 14 + ks) * 28672; obase = (int)OFF_GF + ((grp >> 1) * 28 + ks) * 1024; ostride = (int)SEGB; osec = 14336; }
        else { wsrc = p.ws + OFF_QW + (size_t)grp * 28672; obase = (int)OFF_QF + (grp >> 4) * (2048 * 1024) + (grp & 15) * 1024; ostride = 32768; osec = 16384;
               if (grp < 16) oscale = LOG2E * 0.18898223650461362f; }
    } else {
        osec = 1024;
        if (grp < 60) { wsrc = p.ws + OFF_YW + (size_t)((grp - 32) * 8 + w) * 28672; obase = (int)OFF_UF + ((grp - 32) * 8 + w) * 2048; ostride = (int)SEGB; }
        else { wsrc = p.ws + OFF_QW + (size_t)(32 + (grp - 60) * 8 + w) * 28672; obase = (int)OFF_VF + ((grp - 60) * 8 + w) * 2048; ostride = 32768; }
    }
    const int npc = w < 4 ? 4 : 3, pc0 = w < 4 ? 4 * w : 16 + 3 * (w - 4);
    const unsigned char* xsrc = p.ws + OFF_XP + pc0 * 1024 + l * 16;
    if (r > 0) __builtin_amdgcn_s_sleep(SW_DLY);
    for (int k = 0; k < npc; ++k) glds16(xsrc + (size_t)r * XBUF + k * 1024, lbase + L_XB + (pc0 + k) * 1024);
    v4u wt[2][14];
#pragma unroll
    for (int f = 0; f < 14; ++f) { wt[0][f] = ldg16(wsrc + f * 1024 + l * 16); wt[1][f] = ldg16(wsrc + (14 + f) * 1024 + l * 16); }
#pragma unroll
    for (int f = 0; f < 14; ++f) asm volatile("" : "+v"(wt[0][f]), "+v"(wt[1][f]));
    asm volatile("s_waitcnt vmcnt(0)" ::: "memory");
    BAR();
    TLS(1);
    const int nseg = (NSEG + STR - 1 - r) / STR;
    for (int i = 0; i < nseg; ++i) {
        const int seg = r + STR * i;
        if (i + 1 < nseg) for (int k = 0; k < npc; ++k) glds16(xsrc + (size_t)(seg + STR) * XBUF + k * 1024, lbase + L_XB + ((i + 1) & 1) * XBUF + (pc0 + k) * 1024);
        const LAS unsigned char* xb = L + L_XB + (i & 1) * XBUF + l * 16;
        f32x4 a[2][2];
#pragma unroll
        for (int t = 0; t < 4; ++t) a[t >> 1][t & 1] = f32x4{0.f, 0.f, 0.f, 0.f};
#pragma unroll
        for (int f = 0; f < 14; ++f) {
            const v4u x0 = *(const LAS v4u*)(xb + f * 1024), x1 = *(const LAS v4u*)(xb + (14 + f) * 1024);
            if (GTYPE) {
                a[0][0] = mfma32(wt[0][f], x0, a[0][0]); a[0][1] = mfma32(wt[1][f], x0, a[0][1]);
                a[1][0] = mfma32(wt[0][f], x1, a[1][0]); a[1][1] = mfma32(wt[1][f], x1, a[1][1]);
            } else {
                a[0][0] = mfma32(x0, wt[0][f], a[0][0]); a[0][1] = mfma32(x0, wt[1][f], a[0][1]);
                a[1][0] = mfma32(x1, wt[0][f], a[1][0]); a[1][1] = mfma32(x1, wt[1][f], a[1][1]);
            }
        }
        v4u o0 = GTYPE ? pack8(a[0][0] * oscale, a[0][1] * oscale) : pack8(a[0][0], a[1][0]);
        v4u o1 = GTYPE ? pack8(a[1][0] * oscale, a[1][1] * oscale) : pack8(a[0][1], a[1][1]);
        if (GTYPE && w < 7) { o0 = v4u{o0.x, o0.z, o0.y, o0.w}; o1 = v4u{o1.x, o1.z, o1.y, o1.w}; }
        __builtin_amdgcn_raw_buffer_store_b128(o0, rs, l * 16, obase + seg * ostride, 16);
        __builtin_amdgcn_raw_buffer_store_b128(o1, rs, l * 16, obase + seg * ostride + osec, 16);
        const bool drain = (i < 2 || i + 1 == nseg);
        if (drain) asm volatile("s_waitcnt vmcnt(0)" ::: "memory"); else asm volatile("s_waitcnt vmcnt(2)" ::: "memory");
        BAR();
        if (threadIdx.x == 0) __builtin_amdgcn_raw_buffer_store_b32((unsigned)(drain ? i + 1 : i), rs, CT_PROG + (grp * 3 + r) * 128, 0, 16);
        if (i == 0) TLS(2);
    }
    if (threadIdx.x == 0) __hip_atomic_fetch_add((GAS unsigned*)(p.ws + CT_DONE), 1u, __ATOMIC_RELAXED, __HIP_MEMORY_SCOPE_AGENT);
    TLS(3);
}

__device__ __forceinline__ void serve(const Params& p, LAS unsigned char* L, const __amdgpu_buffer_rsrc_t rs, const int grp, const int w) {
    const int l = threadIdx.x & 63, n = l & 15, g = l >> 4;
    const int ks = 7 * (grp & 1) + w;
    v4u wt[2][14];
    {
        const unsigned char* wsrc = p.ws + OFF_TW + (size_t)((grp >> 1) * 14 + (w < 7 ? ks : 0)) * 28672;
        __builtin_amdgcn_s_sleep(100);
#pragma unroll
        for (int f = 0; f < 14; ++f) { wt[0][f] = ldg16(wsrc + f * 1024 + l * 16); wt[1][f] = ldg16(wsrc + (14 + f) * 1024 + l * 16); }
#pragma unroll
        for (int f = 0; f < 14; ++f) asm volatile("" : "+v"(wt[0][f]), "+v"(wt[1][f]));
    }
    const unsigned parmask = load_parmask(p.ws);
    unsigned mspin = 0;
    const int mrow = 4 * ((n >> 2) & 1) + 2 * (n >> 3) + (n & 1);
    const LAS unsigned char* mrd = L + (g >> 1) * 128 + mrow * 16 + (g & 1) * 8;
    const unsigned char* xr = p.ws + OFF_XP + (w < 7 ? ks : 0) * 1024 + l * 16;
    v4u mx_ = ldc16(rs, (w < 7 ? w : 0) * 1024 + l * 16, hop_off(0, parmask) + C_M3);
    for (int s = 0; s < NSEG - 1; ++s) {
        v4u xa, xb2;
        if (w < 7) {
            xa = ldg16(xr + (size_t)s * 28672); xb2 = ldg16(xr + (size_t)s * 28672 + 14336);
            const int ho = hop_off(s, parmask) + C_M3;
            while (!fresh(mx_) && ++mspin < SPIN_CAP) {
                __builtin_amdgcn_s_sleep(2);
                asm volatile("" ::: "memory");
                mx_ = ldc16(rs, w * 1024 + l * 16, ho);
            }
            *(LAS v4u*)(L + w * 1024 + l * 16) = mx_;
            FENCE();
            mx_ = ldc16(rs, w * 1024 + l * 16, hop_off(s + 1 < NSEG ? s + 1 : s, parmask) + C_M3);
        }
        BAR();
        if (w < 7) {
            v2u lo[14], hi[14];
#pragma unroll
            for (int f = 0; f < 14; ++f) {
                lo[f] = *(const LAS v2u*)(mrd + (f >> 1) * 1024 + (f & 1) * 256); hi[f] = *(const LAS v2u*)(mrd + (f >> 1) * 1024 + (f & 1) * 256 + 512);
            }
            FENCE();
            f32x4 a0 = {0.f, 0.f, 0.f, 0.f}, a1 = a0;
#pragma unroll
            for (int f = 0; f < 14; ++f) {
                const v4u mf = v4u{lo[f].x, hi[f].x, lo[f].y, hi[f].y};
                a0 = mfma32(wt[0][f], mf, a0); a1 = mfma32(wt[1][f], mf, a1);
            }
            const v4u gm = pack8(a0, a1);
            const f32x4 z4 = {0.f, 0.f, 0.f, 0.f};
            const f32x4 d0 = mfma32(gm, xa, z4), d1 = mfma32(gm, xb2, z4);
            *(LAS f32x4*)(L + 8192 + w * 1024 + l * 16) = f32x4{d0[0], d0[1], d1[0], d1[1]};
        }
        BAR();
        TLS(8 + s);
        if (w == 7) {
            f32x4 q[7];
#pragma unroll
            for (int k = 0; k < 7; ++k) q[k] = *(const LAS f32x4*)(L + 8192 + k * 1024 + l * 16);
            const f32x4 t = ((q[0] + q[1]) + (q[2] + q[3])) + ((q[4] + q[5]) + q[6]);
            __builtin_amdgcn_raw_buffer_store_b64(v2u{sane(fbits(t[0])), sane(fbits(t[1]))}, rs, l * 8, (int)OFF_SXM + (s * 32 + grp) * 1024, 16);
            __builtin_amdgcn_raw_buffer_store_b64(v2u{sane(fbits(t[2])), sane(fbits(t[3]))}, rs, l * 8 + 512, (int)OFF_SXM + (s * 32 + grp) * 1024, 16);
        }
    }
}

__device__ __forceinline__ void attn_item(const Params& p, const __amdgpu_buffer_rsrc_t rs, const int item) {
    const int l = threadIdx.x & 63, n = l & 15, g = l >> 4;
    const int rt = item >> 4, h = item & 15;
    const int s = rt >> 1, q = rt * 16 + n;
    const v4u qf = ldc16(rs, l * 16, (int)OFF_QF + (rt * 16 + h) * 1024);
    f32x4 sc[18];
    const f32x4 z4 = {0.f, 0.f, 0.f, 0.f};
    const float NEG = -3.0e38f;
    v4u kf[18];
#pragma unroll
    for (int k = 0; k < 18; ++k) { const int kt = max(2 * (s - 8) + k, 0); kf[k] = ldc16(rs, l * 16, (int)OFF_KF + (kt * 16 + h) * 1024); }
#pragma unroll
    for (int k = 0; k < 18; ++k) {
        const int kt = 2 * (s - 8) + k;
        f32x4 a = mfma32(kf[k], qf, z4);
        if (k < 2 || k >= 16) {
#pragma unroll
            for (int r = 0; r < 4; ++r) {
                const int dq = q - (16 * kt + 4 * g + r);
                a[r] = (dq >= 0 && dq < 256) ? a[r] : NEG;
            }
        }
        sc[k] = kt >= 0 ? a : f32x4{NEG, NEG, NEG, NEG};
    }
    FENCE();
    v4u vf[18];
#pragma unroll
    for (int k = 0; k < 18; ++k) { const int b = max(s - 8 + (k >> 1), 0); vf[k] = ldc16(rs, l * 16, (int)OFF_VF + ((b * 16 + h) * 2 + (k & 1)) * 1024); }
    float m = NEG;
#pragma unroll
    for (int k = 0; k < 18; ++k) m = fmaxf(m, fmaxf(fmaxf(sc[k][0], sc[k][1]), fmaxf(sc[k][2], sc[k][3])));
    m = xg_max(m);
    float sum = 0.f;
    f32x4 o0 = z4, o1 = z4;
#pragma unroll
    for (int blk = 0; blk < 9; ++blk) {
        f32x4 e0, e1;
#pragma unroll
        for (int r = 0; r < 4; ++r) { e0[r] = ex2(sc[2 * blk][r] - m); e1[r] = ex2(sc[2 * blk + 1][r] - m); }
        sum += ((e0[0] + e0[1]) + (e0[2] + e0[3])) + ((e1[0] + e1[1]) + (e1[2] + e1[3]));
        const v4u pb = pack8(e0, e1);
        o0 = mfma32(vf[2 * blk], pb, o0);
        o1 = mfma32(vf[2 * blk + 1], pb, o1);
    }
    sum = xg_sum(sum);
    const float f = ((const float*)(p.ws + OFF_GATE))[q] / sum;
    __builtin_amdgcn_raw_buffer_store_b128(pack8(o0 * f, o1 * f), rs, l * 16, (int)OFF_OF + (rt * 16 + h) * 1024, 16);
    asm volatile("s_waitcnt vmcnt(0)" ::: "memory");
    if (l == 0) __hip_atomic_fetch_add((GAS unsigned*)(p.ws + CT_OFC + s * 128), 1u, __ATOMIC_RELAXED, __HIP_MEMORY_SCOPE_AGENT);
}

__device__ __forceinline__ void out_item(const Params& p, LAS unsigned char* L, const __amdgpu_buffer_rsrc_t rs, const int s, const int q4, const int w, unsigned& mspin) {
    const int l = threadIdx.x & 63, n = l & 15, g = l >> 4;
    const f32x4 z4 = {0.f, 0.f, 0.f, 0.f};
    const float NEG = -3.0e38f;
    const int v = 7 * q4 + (w < 7 ? w : 0);
    f32x4 sxx[2][3];
#pragma unroll
    for (int a = 0; a < 2; ++a) { sxx[a][0] = z4; sxx[a][1] = z4; sxx[a][2] = z4; }
    {
        const int gp = (int)OFF_GF + s * (int)SEGB + (2 * w) * 28 * 1024;
        const unsigned char* xr = p.ws + OFF_XP + (size_t)s * 28672 + l * 16;
#pragma unroll 1
        for (int kh = 0; kh < 2; ++kh) {
            v4u x0[7], x1[7], kk[4][7];
#pragma unroll
            for (int j = 0; j < 7; ++j) {
                const int ks = 7 * kh + j;
                x0[j] = ldg16(xr + ks * 1024); x1[j] = ldg16(xr + (14 + ks) * 1024);
#pragma unroll
                for (int t = 0; t < 4; ++t) kk[t][j] = ldc16(rs, l * 16, gp + (t * 14 + ks) * 1024);
            }
            FENCE();
#pragma unroll
            for (int j = 0; j < 7; ++j) {
                const v4u xa = v4u{x0[j].x, x0[j].z, x0[j].y, x0[j].w}, xb = v4u{x1[j].x, x1[j].z, x1[j].y, x1[j].w};
#pragma unroll
                for (int a = 0; a < 2; ++a) {
                    sxx[a][0] = mfma32(kk[2 * a][j], xa, sxx[a][0]);
                    sxx[a][1] = mfma32(kk[2 * a][j], xb, sxx[a][1]);
                    sxx[a][2] = mfma32(kk[2 * a + 1][j], xb, sxx[a][2]);
                }
            }
            FENCE();
        }
    }
    if (w == 0) {
        for (;;) {
            asm volatile("" ::: "memory");
            const unsigned c = __builtin_amdgcn_raw_buffer_load_b32(rs, CT_OFC + s * 128, 0, 16);
            if (__builtin_amdgcn_readfirstlane(c) >= 32u || ++mspin >= SPIN_CAP) break;
            __builtin_amdgcn_s_sleep(4);
        }
    }
    BAR();
    f32x4 o0 = z4, o1 = z4;
    {
        const unsigned char* wp = p.ws + OFF_WOF + (size_t)(v * 16) * 1024 + l * 16;
#pragma unroll 1
        for (int hb = 0; hb < 16; hb += 8) {
            v4u wf[8], oa[8], ob[8];
#pragma unroll
            for (int h = 0; h < 8; ++h) {
                wf[h] = ldg16(wp + (hb + h) * 1024);
                oa[h] = ldc16(rs, l * 16, (int)OFF_OF + (2 * s * 16 + hb + h) * 1024);
                ob[h] = ldc16(rs, l * 16, (int)OFF_OF + ((2 * s + 1) * 16 + hb + h) * 1024);
            }
            FENCE();
#pragma unroll
            for (int h = 0; h < 8; ++h) { o0 = mfma32(wf[h], oa[h], o0); o1 = mfma32(wf[h], ob[h], o1); }
            FENCE();
        }
    }
    v4u uf[16];
#pragma unroll
    for (int h = 0; h < 16; ++h) uf[h] = ldc16(rs, l * 16, (int)OFF_UF + s * (int)SEGB + (v * 16 + h) * 1024);
    const float gate0 = 1.0f - ((const float*)(p.ws + OFF_GATE))[32 * s + n], gate1 = 1.0f - ((const float*)(p.ws + OFF_GATE))[32 * s + 16 + n];
    float mxx[2][2];
#pragma unroll
    for (int a = 0; a < 2; ++a) {
#pragma unroll
        for (int r = 0; r < 4; ++r) { sxx[a][0][r] = (4 * g + r <= n) ? sxx[a][0][r] : NEG; sxx[a][2][r] = (4 * g + r <= n) ? sxx[a][2][r] : NEG; }
        mxx[a][0] = fmaxf(fmaxf(sxx[a][0][0], sxx[a][0][1]), fmaxf(sxx[a][0][2], sxx[a][0][3]));
        mxx[a][1] = fmaxf(fmaxf(fmaxf(sxx[a][1][0], sxx[a][1][1]), fmaxf(sxx[a][1][2], sxx[a][1][3])), fmaxf(fmaxf(sxx[a][2][0], sxx[a][2][1]), fmaxf(sxx[a][2][2], sxx[a][2][3])));
    }
    FENCE();
    TLS(s < 40 ? 8 : 10);
    v4u sm[4];
    for (;;) {
        asm volatile("" ::: "memory");
#pragma unroll
        for (int k = 0; k < 4; ++k) {
            const v2u q0 = __builtin_amdgcn_raw_buffer_load_b64(rs, l * 8, (int)OFF_SXM + (s * 32 + 4 * w + k) * 1024, 16),
                      q1 = __builtin_amdgcn_raw_buffer_load_b64(rs, l * 8 + 512, (int)OFF_SXM + (s * 32 + 4 * w + k) * 1024, 16);
            sm[k] = v4u{q0.x, q0.y, q1.x, q1.y};
        }
        if (fresh(__builtin_elementwise_max(__builtin_elementwise_max(sm[0], sm[1]), __builtin_elementwise_max(sm[2], sm[3]))) || ++mspin >= SPIN_CAP) break;
        __builtin_amdgcn_s_sleep(6);
    }
    TLS(s < 40 ? 9 : 11);
    if (s + 2 < NSEG) {
        for (;;) {
            asm volatile("" ::: "memory");
            const unsigned x = __builtin_amdgcn_raw_buffer_load_b32(rs, 0, (int)OFF_SXM + ((s + 2) * 32 + 4 * w) * 1024, 16);
            if (__builtin_amdgcn_readfirstlane(x) != 0xFFFFFFFFu || ++mspin >= SPIN_CAP) break;
            __builtin_amdgcn_s_sleep(6);
        }
        asm volatile("" ::: "memory");
    }
    v2u yf[8];
#pragma unroll
    for (int k = 0; k < 8; ++k) yf[k] = __builtin_amdgcn_raw_buffer_load_b64(rs, l * 8, (int)OFF_YM + (s * 224 + v * 8 + k) * 512, 16);
    unsigned px[2][2][4]; unsigned pm[2][2];
#pragma unroll
    for (int qt = 0; qt < 2; ++qt) {
#pragma unroll
        for (int a = 0; a < 2; ++a) {
            const float t0 = bitsf(sm[2 * a][2 * qt]) + bitsf(sm[2 * a + 1][2 * qt]), t1 = bitsf(sm[2 * a][2 * qt + 1]) + bitsf(sm[2 * a + 1][2 * qt + 1]);
            const float m = xg_max(fmaxf(mxx[a][qt], fmaxf(t0, t1)));
            const f32x4 dg = sxx[a][qt == 0 ? 0 : 2];
            float ed[4], ef[4];
            const float e0 = ex2(t0 - m), e1 = ex2(t1 - m);
            float sum = e0 + e1;
#pragma unroll
            for (int r = 0; r < 4; ++r) {
                ed[r] = ex2(dg[r] - m); sum += ed[r];
                if (qt == 1) { ef[r] = ex2(sxx[a][1][r] - m); sum += ef[r]; }
            }
            sum = xg_sum(sum);
            const float f = (qt == 0 ? gate0 : gate1) * __builtin_amdgcn_rcpf(sum);
            if (qt == 0) { px[a][0][0] = pack2(ed[0] * f, ed[1] * f); px[a][0][1] = pack2(ed[2] * f, ed[3] * f); }
            else {
                px[a][1][0] = pack2(ef[0] * f, ef[1] * f); px[a][1][1] = pack2(ef[2] * f, ef[3] * f);
                px[a][1][2] = pack2(ed[0] * f, ed[1] * f); px[a][1][3] = pack2(ed[2] * f, ed[3] * f);
            }
            pm[qt][a] = pack2(e0 * f, e1 * f);
        }
    }
    *(LAS v4u*)(L + w * 4096 + l * 16) = v4u{px[0][0][0], px[0][0][1], px[1][0][0], px[1][0][1]};
    *(LAS v4u*)(L + w * 4096 + 1024 + l * 16) = v4u{px[0][1][0], px[0][1][1], px[0][1][2], px[0][1][3]};
    *(LAS v4u*)(L + w * 4096 + 2048 + l * 16) = v4u{px[1][1][0], px[1][1][1], px[1][1][2], px[1][1][3]};
    *(LAS v4u*)(L + w * 4096 + 3072 + l * 16) = v4u{pm[0][0], pm[0][1], pm[1][0], pm[1][1]};
    for (;;) {
        unsigned mm = 0u;
#pragma unroll
        for (int k = 0; k < 8; ++k) mm = max(mm, max(yf[k].x, yf[k].y));
        if (__all(mm != 0xFFFFFFFFu) || ++mspin >= SPIN_CAP) break;
        asm volatile("" ::: "memory");
#pragma unroll
        for (int k = 0; k < 8; ++k) yf[k] = __builtin_amdgcn_raw_buffer_load_b64(rs, l * 8, (int)OFF_YM + (s * 224 + v * 8 + k) * 512, 16);
    }
    BAR();
    if (w < 7) {
#pragma unroll
        for (int pp = 0; pp < 4; ++pp) {
            v4u pmm[2];
#pragma unroll
            for (int z = 0; z < 2; ++z) {
                const int pr = 2 * pp + z;
                const v4u u0 = uf[2 * pr], u1 = uf[2 * pr + 1];
                const v4u pa = *(const LAS v4u*)(L + pr * 4096 + l * 16), pb = *(const LAS v4u*)(L + pr * 4096 + 1024 + l * 16),
                          pc = *(const LAS v4u*)(L + pr * 4096 + 2048 + l * 16);
                pmm[z] = *(const LAS v4u*)(L + pr * 4096 + 3072 + l * 16);
                o1 = mfma32(u0, pb, o1);
                o0 = mfma32(v4u{u0.x, u0.y, u1.x, u1.y}, pa, o0);
                o1 = mfma32(u1, pc, o1);
            }
            const v4u yy = v4u{yf[2 * pp].x, yf[2 * pp].y, yf[2 * pp + 1].x, yf[2 * pp + 1].y};
            o0 = mfma32(yy, v4u{pmm[0].x, pmm[0].y, pmm[1].x, pmm[1].y}, o0);
            o1 = mfma32(yy, v4u{pmm[0].z, pmm[0].w, pmm[1].z, pmm[1].w}, o1);
        }
        stg8(p.out + (size_t)(32 * s + n) * 448 + 16 * v + 4 * g, v2u{pack2(o0[0], o0[1]), pack2(o0[2], o0[3])});
        stg8(p.out + (size_t)(32 * s + 16 + n) * 448 + 16 * v + 4 * g, v2u{pack2(o1[0], o1[1]), pack2(o1[2], o1[3])});
    }
}

__global__ void __launch_bounds__(512, 2) k_main(Params p) {
    extern __shared__ __attribute__((aligned(16))) unsigned char lds_[];
    p.ws = align_ws(p.ws);
    const int rot = __builtin_amdgcn_readfirstlane(((const int*)(p.ws + OFF_CTL))[8]);
    const int grp = (blockIdx.x + rot) & 7, u = blockIdx.x >> 3;
    LAS unsigned char* L = (LAS unsigned char*)lds_;
    const int w = __builtin_amdgcn_readfirstlane(threadIdx.x >> 6);
    TLS(0);
    if (threadIdx.x == 0) ((unsigned*)(p.ws + WS_END + (4 << 20)))[blockIdx.x * 128 + 127] = (unsigned)__builtin_amdgcn_s_getreg(20 | (3 << 11)) & 0xfu;
    if (grp == 0) {
        if (w < 2) score_wave<1, 0>(p, L, u, w); else if (w == 2) score_wave<0, 0>(p, L, u, w); else if (w == 3) score_wave<0, DLY_3>(p, L, u, w);
        else if (w < 7) score_wave<0, DLY_4>(p, L, u, w); else score_wave<2, 0>(p, L, u, w);
        TLS(7);
        return;
    }
    if (grp == 1) {
        if (u < 28) { if (w < 3) value_wave<false, false>(p, L, u, w); else if (w < 7) value_wave<false, true>(p, L, u, w); else value_wave<true, false>(p, L, u, w); }
        TLS(7);
        return;
    }
    const __amdgpu_buffer_rsrc_t rs = __builtin_amdgcn_make_buffer_rsrc(p.ws, 0, (int)WS_END, 0x00020000);
    const int k = (grp - 2) * 32 + u;
    if (k < 96) {
        if (k % 3 == 0) { serve(p, L, rs, k / 3, w); TLS(7); return; }
        sweep<true>(p, L, rs, k / 3, k % 3 - 1, w);
    } else if (k < 186) sweep<false>(p, L, rs, k / 3, k % 3, w);
    else {
        const unsigned parmask = load_parmask(p.ws);
        const int l = threadIdx.x & 63, wi = (k - 186) * 8 + w;
        const v4u ff = v4u{0xFFFFFFFFu, 0xFFFFFFFFu, 0xFFFFFFFFu, 0xFFFFFFFFu};
        __builtin_amdgcn_s_sleep(127); __builtin_amdgcn_s_sleep(127);
        for (int pg = 1; pg < NPAGE; ++pg) {
            const int base = hop_off(pg * SPP, parmask);
            for (int i = wi; i < SPP * NCHUNK * 8; i += 48) __builtin_amdgcn_raw_buffer_store_b128(ff, rs, l * 16, base + (i >> 3) * CH + (i & 7) * 1024, 16);
            for (int i = wi; i < SPP * 32 && pg * SPP * 32 + i < NSEG * 32; i += 48) __builtin_amdgcn_raw_buffer_store_b128(ff, rs, l * 16, (int)OFF_SXM + (pg * SPP * 32 + i) * 1024, 16);
            asm volatile("s_waitcnt vmcnt(0)" ::: "memory");
            BAR();
            if (threadIdx.x == 0) __hip_atomic_fetch_add((GAS unsigned*)(p.ws + CT_PZ), 1u << (4 * (pg - 1)), __ATOMIC_RELAXED, __HIP_MEMORY_SCOPE_AGENT);
        }
        if (threadIdx.x == 0) __hip_atomic_fetch_add((GAS unsigned*)(p.ws + CT_DONE), 1u, __ATOMIC_RELAXED, __HIP_MEMORY_SCOPE_AGENT);
    }
    const int wk = k < 96 ? (k / 3) * 2 + (k % 3 - 1) : k - 32;
    unsigned mspin = 0;
    if (w == 0) {
        for (;;) {
            asm volatile("" ::: "memory");
            const unsigned c = __builtin_amdgcn_raw_buffer_load_b32(rs, CT_DONE, 0, 16);
            if (__builtin_amdgcn_readfirstlane(c) >= 160u || ++mspin >= SPIN_CAP) break;
            __builtin_amdgcn_s_sleep(16);
        }
    }
    BAR();
    TLS(4);
    for (int it = wk * 8 + w; it < 2048; it += 1280) attn_item(p, rs, it);
    TLS(5);
    for (int i = 0; i < (wk * STAG) >> 4; ++i) __builtin_amdgcn_s_sleep(64);
    for (int it = wk; it < 256; it += 160) { out_item(p, L, rs, it >> 2, it & 3, w, mspin); TLS(it < 160 ? 6 : 7); }
}

__global__ void k_dump(Params p) {
    p.ws = align_ws(p.ws);
    const unsigned* d = (const unsigned*)(p.ws + WS_END + (4 << 20));
    const unsigned* q = (const unsigned*)(p.ws + WS_END + (1 << 20));
    const int rot = ((const int*)(p.ws + OFF_CTL))[8];
    unsigned t0 = ~0u, p0 = ~0u, p1 = 0, pb = 0, e = 0, w1 = 0, f1 = 0;
    for (int b = 0; b < 256; ++b) { t0 = min(t0, d[b * 128]); p0 = min(p0, q[b * 32]); if (b < 224) { for (int w = 0; w < 8; ++w) p1 = max(p1, q[(b * 8 + w) * 4 + 3]); pb = max(pb, q[b * 32 + 1]); } }
    for (int b = 0; b < 256; ++b) { int grp = (b + rot) & 7, k = (grp - 2) * 32 + (b >> 3); if (grp >= 2) { int wk = k < 96 ? (k / 3) * 2 + (k % 3 - 1) : k - 32; if (!(k < 96 && k % 3 == 0) && wk < 96) e = max(e, d[b * 128 + 7]); if (k < 186 && !(k < 96 && k % 3 == 0)) { w1 = max(w1, d[b * 128 + 1]); f1 = max(f1, d[b * 128 + 2]); } } }
    const unsigned* sc = d + ((8 - rot) & 7) * 128 + 8;
    printf("rot %d par %u%u%u%u%u prep start %d barrier %d end %d | main: wload %d first %d B2_0 %d B2_63 %d end %d (x10ns)\n", rot, ((const unsigned*)(p.ws + OFF_CTL))[0], ((const unsigned*)(p.ws + OFF_CTL))[1], ((const unsigned*)(p.ws + OFF_CTL))[2], ((const unsigned*)(p.ws + OFF_CTL))[3], ((const unsigned*)(p.ws + OFF_CTL))[4],
           (int)(p0 - t0), (int)(pb - t0), (int)(p1 - t0), (int)(w1 - t0), (int)(f1 - t0), (int)(sc[0] - t0), (int)(sc[63] - t0), (int)(e - t0));
    for (int s = 1; s < 64; ++s) printf("%d ", (int)(sc[s] - sc[s - 1]));
    printf("\nws %p\n", p.ws);
}
extern "C" void kernel_launch(void* const* d_in, const int* in_sizes, int n_in, void* d_out, int out_size, void* d_ws, size_t ws_size,
                              hipStream_t stream) {
    Params p;
    p.x = (const u16*)d_in[0]; p.wqkv = (const u16*)d_in[1]; p.wo = (const u16*)d_in[2]; p.mem0 = (const u16*)d_in[4];
    p.Wq = (const u16*)d_in[5]; p.Wk = (const u16*)d_in[6]; p.Wv = (const u16*)d_in[7]; p.Wo = (const u16*)d_in[8];
    p.gate_w = (const u16*)d_in[10];
    p.out = (u16*)d_out; p.ws = (unsigned char*)d_ws;
    (void)hipFuncSetAttribute((const void*)k_prep, hipFuncAttributeMaxDynamicSharedMemorySize, K1_LDS);
    (void)hipFuncSetAttribute((const void*)k_main, hipFuncAttributeMaxDynamicSharedMemorySize, L_REC_END);
    k_prep<<<256, 512, K1_LDS, stream>>>(p);
    k_main<<<256, 512, L_REC_END, stream>>>(p);
    k_dump<<<1, 1, 0, stream>>>(p);
}
extern "C" size_t ws_end() { return WS_END; }
```

```cpp
#include <hip/hip_runtime.h>
#include <hip/hip_bf16.h>
#include <stdint.h>

#define GAS __attribute__((address_space(1)))
#define LAS __attribute__((address_space(3)))
typedef __bf16 bf16x2 __attribute__((ext_vector_type(2)));
typedef __bf16 bf16x4 __attribute__((ext_vector_type(4)));
typedef __bf16 bf16x8 __attribute__((ext_vector_type(8)));
typedef __bf16 bf16x16 __attribute__((ext_vector_type(16)));
typedef short s16x4 __attribute__((ext_vector_type(4)));
typedef float f32x2 __attribute__((ext_vector_type(2)));
typedef float f32x4 __attribute__((ext_vector_type(4)));
typedef unsigned v2u __attribute__((ext_vector_type(2)));
typedef unsigned v4u __attribute__((ext_vector_type(4)));
typedef unsigned v8u __attribute__((ext_vector_type(8)));
typedef unsigned short u16;

constexpr int S_ = 2048, D_ = 448, H_ = 16, NSEG = 64;
constexpr float LOG2E = 1.4426950408889634f;

constexpr size_t SZ_W = (size_t)16 * 28 * 14 * 1024;
constexpr size_t OFF_HOP = 0;
constexpr int PAGE = 2 << 20, CH = 16384, NCHUNK = 9, STEPB = NCHUNK * CH, NPAGE = 5, SPP = 14;
constexpr int C_M = 0, C_M2 = 4 * CH, C_M3 = 5 * CH, C_S = 6 * CH, C_P = 7 * CH;
constexpr size_t HOP_BYTES = (size_t)NPAGE * PAGE;
constexpr size_t OFF_CTL = OFF_HOP + HOP_BYTES;
constexpr size_t CTL_BYTES = 36864;
constexpr int CT_DONE = (int)OFF_CTL + 256, CT_PZ = (int)OFF_CTL + 384, CT_OFC = (int)OFF_CTL + 512, CT_PROG = CT_OFC + 64 * 128;
constexpr size_t OFF_TW = OFF_CTL + CTL_BYTES;
constexpr size_t OFF_YW = OFF_TW + SZ_W;
constexpr size_t OFF_QW = OFF_YW + SZ_W;
constexpr size_t OFF_VW = OFF_QW + (size_t)32 * 28 * 1024;
constexpr size_t OFF_WOF = OFF_VW + (size_t)16 * 28 * 1024;
constexpr size_t OFF_XP = OFF_WOF + (size_t)28 * 16 * 1024;
constexpr size_t OFF_GATE = OFF_XP + (size_t)128 * 14 * 1024;
constexpr size_t SEGB = (size_t)448 * 1024;
constexpr size_t OFF_GF = OFF_GATE + 8192;
constexpr size_t OFF_UF = OFF_GF + 64 * SEGB;
constexpr size_t OFF_QF = OFF_UF + 64 * SEGB;
constexpr size_t OFF_KF = OFF_QF + (size_t)2048 * 1024;
constexpr size_t OFF_VF = OFF_KF + (size_t)2048 * 1024;
constexpr size_t OFF_OF = OFF_VF + (size_t)2048 * 1024;
constexpr size_t OFF_SXM = OFF_OF + (size_t)2048 * 1024;
constexpr size_t OFF_YM = OFF_SXM + (size_t)64 * 32 * 1024;
constexpr size_t WS_END = OFF_YM + (size_t)64 * 224 * 512;

struct Params {
    const u16* x; const u16* wqkv; const u16* wo; const u16* mem0;
    const u16* Wq; const u16* Wk; const u16* Wv; const u16* Wo; const u16* gate_w;
    u16* out; unsigned char* ws;
};
__device__ __forceinline__ unsigned char* align_ws(unsigned char* w) {
    return w + ((0 - (size_t)w) & (size_t)(PAGE - 1));
}
__device__ __forceinline__ unsigned xcc_id() { return (unsigned)__builtin_amdgcn_s_getreg(20 | (3 << 11)) & 0xfu; }
__device__ __forceinline__ unsigned load_parmask(const unsigned char* ws) {
    const unsigned* c = (const unsigned*)(ws + OFF_CTL);
    unsigned m = 0;
#pragma unroll
    for (int k = 0; k < NPAGE; ++k) m |= (c[k] & 1u) << k;
    return __builtin_amdgcn_readfirstlane(m);
}
__device__ __forceinline__ int hop_off(int s, unsigned parmask) {
    const int pg = s / SPP, sl = s - pg * SPP;
    return pg * PAGE + sl * STEPB + (int)((parmask >> pg) & 1u) * 8192;
}

__device__ __forceinline__ unsigned pack2(float a, float b) {
    return __builtin_bit_cast(unsigned, __builtin_convertvector(f32x2{a, b}, bf16x2));
}
__device__ __forceinline__ float bf2f(u16 v) { return __builtin_bit_cast(float, (unsigned)v << 16); }
__device__ __forceinline__ f32x4 mfma32(v4u a, v4u b, f32x4 c) {
    return __builtin_amdgcn_mfma_f32_16x16x32_bf16(__builtin_bit_cast(bf16x8, a), __builtin_bit_cast(bf16x8, b), c, 0, 0, 0);
}
__device__ __forceinline__ f32x4 mfma16(v2u a, v2u b, f32x4 c) {
    return __builtin_amdgcn_mfma_f32_16x16x16bf16_1k(__builtin_bit_cast(s16x4, a), __builtin_bit_cast(s16x4, b), c, 0, 0, 0);
}
__device__ __forceinline__ f32x4 smfmac(v4u a, v8u b, f32x4 c, int idx) {
    return __builtin_amdgcn_smfmac_f32_16x16x64_bf16(__builtin_bit_cast(bf16x8, a), __builtin_bit_cast(bf16x16, b), c, idx, 0, 0);
}
__device__ __forceinline__ v4u ldg16(const void* p) { return *(const GAS v4u*)p; }
__device__ __forceinline__ v2u ldg8(const void* p) { return *(const GAS v2u*)p; }
__device__ __forceinline__ void stg16(void* p, v4u v) { *(GAS v4u*)p = v; }
__device__ __forceinline__ void stg8(void* p, v2u v) { *(GAS v2u*)p = v; }
__device__ __forceinline__ v2u tr_read(const LAS void* p) {
    return __builtin_bit_cast(v2u, __builtin_amdgcn_ds_read_tr16_b64_v4i16((LAS s16x4*)p));
}
__device__ __forceinline__ float ror8(float v) {
    return __builtin_bit_cast(float, __builtin_amdgcn_update_dpp(0, __builtin_bit_cast(int, v), 0x128, 0xf, 0xf, false));
}
__device__ __forceinline__ unsigned fbits(float v) { return __builtin_bit_cast(unsigned, v); }
__device__ __forceinline__ float bitsf(unsigned v) { return __builtin_bit_cast(float, v); }
__device__ __forceinline__ float xg_max(float v) {
    const v2u a = __builtin_amdgcn_permlane16_swap(fbits(v), fbits(v), false, false);
    v = fmaxf(bitsf(a.x), bitsf(a.y));
    const v2u b = __builtin_amdgcn_permlane32_swap(fbits(v), fbits(v), false, false);
    return fmaxf(bitsf(b.x), bitsf(b.y));
}
__device__ __forceinline__ float xg_sum(float v) {
    const v2u a = __builtin_amdgcn_permlane16_swap(fbits(v), fbits(v), false, false);
    v = bitsf(a.x) + bitsf(a.y);
    const v2u b = __builtin_amdgcn_permlane32_swap(fbits(v), fbits(v), false, false);
    return bitsf(b.x) + bitsf(b.y);
}
__device__ __forceinline__ float fold32(float a, float b) {
    const v2u r = __builtin_amdgcn_permlane32_swap(fbits(a), fbits(b), false, false);
    return bitsf(r.x) + bitsf(r.y);
}
__device__ __forceinline__ float ex2(float v) { return __builtin_amdgcn_exp2f(v); }
__device__ __forceinline__ unsigned sane(unsigned v) { return v < 0xFFFFFFFEu ? v : 0xFFFFFFFEu; }

__device__ __forceinline__ int porder(int f, int g, int e) {
    return 64 * (f >> 1) + 16 * (f & 1) + 4 * g + 32 * ((e >> 1) & 1) + 2 * (e >> 2) + (e & 1);
}
constexpr int NU_HOP = SPP * NCHUNK * 16;
template <bool MIDBAR>
__device__ __forceinline__ void misc_wave(const Params& p, const int mw, const int l) {
    const int n = l & 15, g = l >> 4;
    v2u xl[4], xh[4];
#pragma unroll
    for (int k = 0; k < 4; ++k) {
        const int i = mw * 4 + k, rt = i / 14, f = i % 14;
        const u16* row = p.x + (size_t)(rt * 16 + n) * 448 + 64 * (f >> 1) + 16 * (f & 1) + 4 * g;
        xl[k] = ldg8(row); xh[k] = ldg8(row + 32);
    }
    unsigned qv[3][8];
    bool qok[3];
#pragma unroll
    for (int k = 0; k < 3; ++k) {
        const int i = mw * 3 + k;
        const bool isv = i >= 896;
        const int fr = isv ? i - 896 : i;
        const int unit = fr / 28, t = (fr % 28) / 14, f = fr % 14;
        const int d = isv ? 16 * t + n : 8 * (n >> 2) + 4 * t + (n & 3);
        const int col = (isv ? 896 + unit * 28 : (unit >> 4) * 448 + (unit & 15) * 28) + (d < 28 ? d : 0);
        qok[k] = d < 28;
#pragma unroll
        for (int e = 0; e < 8; ++e) qv[k][e] = p.wqkv[(size_t)porder(f, g, e) * 1344 + col];
    }
    unsigned wv[8];
#pragma unroll
    for (int e = 0; e < 8; ++e) {
        const int d = (e < 4) ? 4 * g + e : 16 + 4 * g + e - 4;
        wv[e] = p.wo[(size_t)((mw & 15) * 28 + (d < 28 ? d : 0)) * 448 + 16 * (mw >> 4) + n];
        if (d >= 28) wv[e] = 0u;
    }
    v4u gxv[7], ggv[7];
    const int row = (mw & 255) * 8 + (l >> 3), c0 = (l & 7) * 8;
#pragma unroll
    for (int k = 0; k < 7; ++k) { gxv[k] = ldg16(p.x + (size_t)row * 448 + k * 64 + c0); ggv[k] = ldg16(p.gate_w + k * 64 + c0); }
    for (int i = mw; i < NU_HOP; i += 448) {
        const int st = i / (NCHUNK * 16), ck = (i >> 4) % NCHUNK, kb = i & 15;
        v4u val = v4u{0xFFFFFFFFu, 0xFFFFFFFFu, 0xFFFFFFFFu, 0xFFFFFFFFu};
        if (st == 0 && ck < 6 && (kb & 7) < 7) {
            const int im = (kb & 7) * 64 + l;
            const v4u m = ldg16(p.mem0 + (size_t)(im & 7) * 448 + (im >> 3) * 8);
            val = v4u{sane(m.x), sane(m.y), sane(m.z), sane(m.w)};
        }
        stg16(p.ws + OFF_HOP + (size_t)hop_off(st, 0u) + ck * CH + kb * 1024 + l * 16, val);
    }
    if (MIDBAR) { __builtin_amdgcn_sched_barrier(0); __builtin_amdgcn_s_barrier(); __builtin_amdgcn_sched_barrier(0); }
    static_assert(SPP * 32 <= 448, "one KiB of the first page's S_xm slots per wave");
    if (mw < SPP * 32) stg16(p.ws + OFF_SXM + (size_t)mw * 1024 + l * 16, v4u{0xFFFFFFFFu, 0xFFFFFFFFu, 0xFFFFFFFFu, 0xFFFFFFFFu});
    if (mw < 224) stg16(p.ws + OFF_YM + (size_t)62 * 224 * 512 + (size_t)mw * 1024 + l * 16, v4u{0xFFFFFFFFu, 0xFFFFFFFFu, 0xFFFFFFFFu, 0xFFFFFFFFu});
#pragma unroll
    for (int k = 0; k < 4; ++k) stg16(p.ws + OFF_XP + (size_t)(mw * 4 + k) * 1024 + l * 16, v4u{xl[k].x, xh[k].x, xl[k].y, xh[k].y});
#pragma unroll
    for (int k = 0; k < 3; ++k) {
        const v4u o = v4u{qv[k][0] | (qv[k][1] << 16), qv[k][2] | (qv[k][3] << 16), qv[k][4] | (qv[k][5] << 16), qv[k][6] | (qv[k][7] << 16)};
        stg16(p.ws + OFF_QW + (size_t)(mw * 3 + k) * 1024 + l * 16, qok[k] ? o : v4u{0u, 0u, 0u, 0u});
    }
    stg16(p.ws + OFF_WOF + (size_t)mw * 1024 + l * 16, v4u{wv[0] | (wv[1] << 16), wv[2] | (wv[3] << 16), wv[4] | (wv[5] << 16), wv[6] | (wv[7] << 16)});
    float s = 0.f;
#pragma unroll
    for (int k = 0; k < 7; ++k)
#pragma unroll
        for (int j = 0; j < 4; ++j) {
            s += bitsf(gxv[k][j] << 16) * bitsf(ggv[k][j] << 16);
            s += bitsf(gxv[k][j] & 0xFFFF0000u) * bitsf(ggv[k][j] & 0xFFFF0000u);
        }
    s += __shfl_xor(s, 1); s += __shfl_xor(s, 2); s += __shfl_xor(s, 4);
    if ((l & 7) == 0 && mw < 256) ((float*)(p.ws + OFF_GATE))[row] = 1.0f / (1.0f + __expf(-s));
}

constexpr int K1_RS = 528, K1_OS = 144, K1_IMG = 36864, K1_IS = 136, K1_LDS = K1_IMG + 7 * 64 * K1_IS;
#define PT(k) do {} while (0)
#define PT0(k) do {} while (0)
template <int mat>
__device__ __forceinline__ void prep_heavy(const Params& p, LAS unsigned char* L, const int h, const int blk, const int w) {
    const int b = blockIdx.x, tid = threadIdx.x, l = tid & 63, n = l & 15, g = l >> 4;
    v4u t[4];
    if (mat == 0) {
#pragma unroll
        for (int k = 0; k < 4; ++k) { const int u = tid + 512 * k; t[k] = ldg16(p.Wq + ((size_t)(64 * blk + (u >> 5)) * 16 + h) * 256 + (u & 31) * 8); }
    } else {
#pragma unroll
        for (int k = 0; k < 4; ++k) { const int u = tid + 512 * k; t[k] = ldg16(p.Wo + ((size_t)h * 256 + (u >> 3)) * 448 + 64 * blk + (u & 7) * 8); }
    }
    __builtin_amdgcn_sched_barrier(0);
    const int rs_ = mat == 0 ? K1_RS : K1_OS, sh = mat == 0 ? 5 : 3;
    if (w == 7) {
#pragma unroll
        for (int k = 0; k < 4; ++k) { const int u = tid + 512 * k; *(LAS v4u*)(L + (u >> sh) * rs_ + (u & ((1 << sh) - 1)) * 16) = t[k]; }
        asm volatile("s_waitcnt lgkmcnt(0)" ::: "memory");
        misc_wave<true>(p, 224 + b, l);
        PT(3);
        return;
    }
    v4u cr[4][8];
    LAS unsigned char* img = L + K1_IMG + w * 64 * K1_IS;
    {
        const __amdgpu_buffer_rsrc_t wr = __builtin_amdgcn_make_buffer_rsrc((void*)(mat == 0 ? p.Wk : p.Wv), 0, 448 * 4096 * 2, 0x00020000);
        const int vo = ((64 * w + (l >> 5)) * 16 + h) * 512 + (l & 31) * 16;
#define K1_LOAD(tt) do { _Pragma("unroll") for (int j = 0; j < 8; ++j) \
            cr[tt][j] = __builtin_amdgcn_raw_buffer_load_b128(wr, vo, (16 * ((tt) >> 1) + 2 * ((tt) & 1) + 4 * (j >> 1) + 32 * (j & 1)) * 8192, 0); \
            __builtin_amdgcn_sched_barrier(0); } while (0)
#define K1_TURN(tt) do { \
        _Pragma("unroll") for (int j = 0; j < 8; ++j) *(LAS v4u*)(img + (2 * j + (l >> 5)) * K1_RS + (l & 31) * 16) = cr[tt][j]; \
        asm volatile("" ::: "memory"); \
        _Pragma("unroll") for (int es = 0; es < 8; ++es) cr[tt][es] = *(const LAS v4u*)(img + n * K1_RS + es * 64 + g * 16); \
        asm volatile("" ::: "memory"); __builtin_amdgcn_sched_barrier(0); } while (0)
        K1_LOAD(0); K1_LOAD(1); K1_LOAD(2);
#pragma unroll
        for (int k = 0; k < 4; ++k) { const int u = tid + 512 * k; *(LAS v4u*)(L + (u >> sh) * rs_ + (u & ((1 << sh) - 1)) * 16) = t[k]; }
        __builtin_amdgcn_sched_barrier(0);
        K1_TURN(0);
        K1_LOAD(3);
        K1_TURN(1); K1_TURN(2); K1_TURN(3);
    }
    asm volatile("s_waitcnt lgkmcnt(0)" ::: "memory");
    __builtin_amdgcn_s_barrier();
    asm volatile("" ::: "memory");
    PT0(1);
    f32x4 acc[4][4];
#pragma unroll
    for (int x = 0; x < 4; ++x)
#pragma unroll
        for (int y = 0; y < 4; ++y) acc[x][y] = f32x4{0.f, 0.f, 0.f, 0.f};
#pragma unroll
    for (int es = 0; es < 8; ++es) {
        v4u bfr[4];
#pragma unroll
        for (int y = 0; y < 4; ++y) {
            if constexpr (mat == 0) {
                bfr[y] = *(const LAS v4u*)(L + (16 * (y >> 1) + 2 * (y & 1) + 4 * (n >> 2) + 32 * ((n >> 1) & 1) + (n & 1)) * K1_RS + es * 64 + g * 16);
            } else {
                const LAS unsigned char* rp = L + (32 * es + 8 * g + (n >> 2)) * K1_OS + y * 32 + (l & 3) * 8;
                const v2u lo = tr_read(rp), hi = tr_read(rp + 4 * K1_OS);
                bfr[y] = v4u{lo.x, lo.y, hi.x, hi.y};
            }
        }
#pragma unroll
        for (int x = 0; x < 4; ++x)
#pragma unroll
            for (int y = 0; y < 4; ++y) acc[x][y] = mfma32(cr[x][es], bfr[y], acc[x][y]);
    }
    __builtin_amdgcn_sched_barrier(0); PT0(2); __builtin_amdgcn_sched_barrier(0);
    constexpr float SC = mat == 0 ? LOG2E / 16.0f : 1.0f;
#pragma unroll
    for (int y = 0; y < 4; ++y) {
        unsigned char* d = mat == 0 ? p.ws + OFF_TW + ((size_t)(h * 28 + 4 * blk + y) * 7 + w) * 2048 + l * 16
                                    : p.ws + OFF_YW + ((size_t)((4 * blk + y) * 16 + h) * 7 + w) * 2048 + l * 16;
#pragma unroll
        for (int a = 0; a < 2; ++a) {
            const f32x4 lo = acc[2 * a][y] * SC, hi = acc[2 * a + 1][y] * SC;
            stg16(d + a * 1024, v4u{pack2(lo[0], lo[1]), pack2(lo[2], lo[3]), pack2(hi[0], hi[1]), pack2(hi[2], hi[3])});
        }
    }
    PT(3);
}

__global__ void __launch_bounds__(512, 2) k_prep(Params p) {
    extern __shared__ __attribute__((aligned(16))) unsigned char lds_[];
    LAS unsigned char* L = (LAS unsigned char*)lds_;
    p.ws = align_ws(p.ws);
    const int b = blockIdx.x, tid = threadIdx.x;
    const int l = tid & 63, w = __builtin_amdgcn_readfirstlane(tid >> 6);
    PT0(0);
    if (b >= 224) {
        const int k = b - 224;
        if ((k & 7) == 0 && w < 2) {
            const int pg = (k >> 3) + 4 * w;
            if (pg < NPAGE && l == 0) {
                GAS unsigned* a = (GAS unsigned*)(p.ws + OFF_HOP + (size_t)pg * PAGE + 126 * CH);
                unsigned best[4] = {~0u, ~0u, ~0u, ~0u};
                for (int it = 0; it < 3; ++it) {
#pragma unroll
                    for (int q = 0; q < 4; ++q) {
                        const unsigned long long t0 = __builtin_amdgcn_s_memtime();
                        __hip_atomic_store(a + q * 1024, 0xFFFFFFFFu, __ATOMIC_RELAXED, __HIP_MEMORY_SCOPE_AGENT);
                        asm volatile("s_waitcnt vmcnt(0)" ::: "memory");
                        const unsigned dt = (unsigned)(__builtin_amdgcn_s_memtime() - t0);
                        best[q] = min(best[q], dt);
                    }
                }
                ((unsigned*)(p.ws + OFF_CTL))[pg] = ((best[0] + best[1] > best[2] + best[3]) ? 1u : 0u) ^ ((xcc_id() >> 2) & 1u);
            }
            PT(3);
            if (pg == 7) {
                if (l == 0) ((unsigned*)(p.ws + OFF_CTL))[8] = xcc_id() & 7u;
                for (int i = l; i < (int)(CTL_BYTES - 256) / 16; i += 64) stg16(p.ws + OFF_CTL + 256 + i * 16, v4u{0u, 0u, 0u, 0u});
            }
            return;
        }
        if ((k & 7) == 0) return;
        const int kk = k - (k >> 3) - 1;
        misc_wave<false>(p, kk * 8 + w, l);
        PT(3);
        return;
    }
    const int r = b >> 3, h = (b & 7) + 8 * (r & 1), q = r >> 1;
    if (q < 7) prep_heavy<0>(p, L, h, q, w); else prep_heavy<1>(p, L, h, q - 7, w);
}

constexpr int L_MST = 0, L_TIM = L_MST + 2 * 7168, L_RED = L_TIM + 8192, L_REC_END = 96 * 1024;
#define DLY_M 1024
#define DM0 17
#define DLY_P 0
#define DLY_PO 0
#define PPOSY 8
#define PRIO_X 1
#define PRIO_L 1
#define DLY_S 100
#define DLY_SO 40
#define GAP_M 150
#define DLY_G 300
#define DLY_3 130
#define DLY_4 60
#define DLY_V 240
#define DLY_Y 0
#define PPOS 8
#define DLY_Q 200
#define SPA 0
#define SPB 2
#define SW_DLY 60
#define STAG 1
__device__ __forceinline__ void delay_var(int n) {
    asm volatile("s_cmp_lt_i32 %0, 1\n\ts_cbranch_scc1 2f\n1:\n\ts_nop 5\n\ts_sub_i32 %0, %0, 1\n\ts_cmp_gt_i32 %0, 0\n\ts_cbranch_scc1 1b\n2:" : "+s"(n) :: "scc", "memory");
}
template <int C> __device__ __forceinline__ void delay_cycles() {
    if constexpr (C >= 64) __builtin_amdgcn_s_sleep(C / 64 > 127 ? 127 : C / 64);
    if constexpr ((C & 63) >= 4) asm volatile("s_nop %0" :: "i"(((C & 63) >> 2) - 1));
}

__device__ __forceinline__ bool fresh(v4u x) {
    const unsigned m = max(max(x.x, x.y), max(x.z, x.w));
    return __all(m != 0xFFFFFFFFu);
}
#define BAR() do { asm volatile("s_waitcnt lgkmcnt(0)" ::: "memory"); __builtin_amdgcn_s_barrier(); asm volatile("" ::: "memory"); } while (0)
#define FENCE() do { asm volatile("" ::: "memory"); __builtin_amdgcn_sched_barrier(0); } while (0)

#define STAMP(k) do {} while (0)
#define PROF_DECL
#define PROF_ACC do {} while (0)
#define PROF_OUT(slot) do {} while (0)
typedef _Float16 f16x2 __attribute__((ext_vector_type(2)));
__device__ __forceinline__ unsigned packh(float a, float b) { return __builtin_bit_cast(unsigned, __builtin_amdgcn_cvt_pkrtz(a, b)); }
__device__ __forceinline__ float hlo(unsigned v) { const f16x2 h = __builtin_bit_cast(f16x2, v); return (float)h.x; }
__device__ __forceinline__ float hhi(unsigned v) { const f16x2 h = __builtin_bit_cast(f16x2, v); return (float)h.y; }
constexpr unsigned SPIN_CAP = 1u << 20;

__device__ __forceinline__ unsigned pkmaxh(unsigned a, unsigned b) { unsigned r; asm("v_pk_max_f16 %0, %1, %2" : "=v"(r) : "v"(a), "v"(b)); return r; }
__device__ __forceinline__ float vmax(float a, float b) { float r; asm("v_max_f32 %0, %1, %2" : "=v"(r) : "v"(a), "v"(b)); return r; }
__device__ __forceinline__ float xg_max_raw(float v) {
    const v2u a = __builtin_amdgcn_permlane16_swap(fbits(v), fbits(v), false, false);
    v = vmax(bitsf(a.x), bitsf(a.y));
    const v2u b = __builtin_amdgcn_permlane32_swap(fbits(v), fbits(v), false, false);
    return vmax(bitsf(b.x), bitsf(b.y));
}
__device__ __forceinline__ float addh_lo(unsigned a, unsigned b) { float r; asm("v_fma_mix_f32 %0, %1, 1.0, %2 op_sel_hi:[1,0,1]" : "=v"(r) : "v"(a), "v"(b)); return r; }
__device__ __forceinline__ float addh_hi(unsigned a, unsigned b) { float r; asm("v_fma_mix_f32 %0, %1, 1.0, %2 op_sel:[1,0,1] op_sel_hi:[1,0,1]" : "=v"(r) : "v"(a), "v"(b)); return r; }
__device__ __forceinline__ float subh_lo(unsigned a, float b) { float r; asm("v_fma_mix_f32 %0, %1, 1.0, -%2 op_sel_hi:[1,0,0]" : "=v"(r) : "v"(a), "v"(b)); return r; }
__device__ __forceinline__ float subh_hi(unsigned a, float b) { float r; asm("v_fma_mix_f32 %0, %1, 1.0, -%2 op_sel:[1,0,0] op_sel_hi:[1,0,0]" : "=v"(r) : "v"(a), "v"(b)); return r; }
__device__ __forceinline__ float add_ror8(float a) { float r; asm("s_nop 1\n\tv_add_f32_dpp %0, %1, %1 row_ror:8 row_mask:0xf bank_mask:0xf" : "=v"(r) : "v"(a)); return r; }
#define UNLIKELY(x) __builtin_expect(!!(x), 0)
#define CNT(x) do {} while (0)

#define TLS(k) do {} while (0)
#define TLW(k) do {} while (0)
template <int NG, int STR>
__device__ __forceinline__ int wait_ready(const __amdgpu_buffer_rsrc_t rs, const int g0, const int need, unsigned& mspin) {
    const int l = threadIdx.x & 63;
    int ready;
    for (;;) {
        asm volatile("" ::: "memory");
        const int v = (int)__builtin_amdgcn_raw_buffer_load_b32(rs, CT_PROG + (g0 * 3 + (l < 3 * NG ? l : 0)) * 128, 0, 16);
        ready = 1 << 20;
#pragma unroll
        for (int k = 0; k < 3 * NG; ++k) if (k % 3 < STR) ready = min(ready, STR * __builtin_amdgcn_readlane(v, k) + (k % 3));
        if (ready > need || ++mspin >= SPIN_CAP) break;
        __builtin_amdgcn_s_sleep(2);
    }
    asm volatile("" ::: "memory");
    return ready;
}

template <int ROLE, int DLY1>
__device__ __forceinline__ void score_wave(const Params& p, LAS unsigned char* L, const int u, const int w) {
    constexpr bool SPECIAL = ROLE == 2;
    const int l = threadIdx.x & 63;
    const int n = l & 15, g = l >> 4, i8 = l & 7, hh = (l >> 3) & 1;
    const int half = u & 1;
    const __amdgpu_buffer_rsrc_t rs = __builtin_amdgcn_make_buffer_rsrc(p.ws, 0, (int)WS_END, 0x00020000);
    const int idx = hh ? 0xEEEE : 0x4444;
    const unsigned parmask = load_parmask(p.ws);
    v8u tw[SPECIAL ? 1 : 14];
    v4u gx[SPECIAL ? 14 : 1];
    const LAS unsigned char* mrd[SPECIAL ? 1 : 7];
    if constexpr (!SPECIAL) {
        const unsigned char* src = p.ws + OFF_TW + ((size_t)(u * 14 + 2 * w) * 7) * 2048 + l * 16;
#pragma unroll
        for (int k = 0; k < 7; ++k) {
            const int f = w + k < 7 ? w + k : w + k - 7;
            mrd[k] = L + L_MST + f * 1024 + l * 16;
#pragma unroll
            for (int t = 0; t < 2; ++t) {
                const v4u a = ldg16(src + (7 * t + f) * 2048), b = ldg16(src + (7 * t + f) * 2048 + 1024);
                tw[7 * t + k] = v8u{a.x, a.y, a.z, a.w, b.x, b.y, b.z, b.w};
            }
        }
    } else {
        *(LAS v4u*)(L + L_TIM + (half ? 0 : 14 * 512) + l * 16) = v4u{0u, 0u, 0u, 0u};
        __builtin_amdgcn_s_setprio(2);
    }
    const int moff = C_M + w * 1024 + hh * 512 + g * 128 + i8 * 16;
    const int sst_off = C_S + hh * 4096 + u * 128 + (4 * (g & 1) + 2 * (g >> 1) + (l & 1)) * 16 + (i8 >> 1) * 4;
    const unsigned ssel = (l & 1) ? 0x03020706u : 0x05040100u;
    const int pst_off = C_P + u * 256 + (4 * (g & 1) + 2 * (g >> 1) + (l & 1)) * 32 + (n >> 1) * 4;
    const int goff = ((u & ~1) + ((n >> 2) & 1)) * 14336 + (8 * half + 4 * (n >> 3) + (n & 3) + 16 * g) * 16;
    const int xoff = (int)OFF_XP + (w * 14 + 7 * half) * 1024 + l * 16;
    unsigned spins = 0, mspin = 0;
    int ready = 0;
    if constexpr (!SPECIAL) {
#pragma unroll
        for (int k = 0; k < 7; ++k) asm volatile("" : "+v"(mrd[k]));
    }
    v4u xq[7], x3 = v4u{0u, 0u, 0u, 0u};
    if constexpr (ROLE == 1) {
#pragma unroll
        for (int k = 0; k < 7; ++k) xq[k] = __builtin_amdgcn_raw_buffer_load_b128(rs, xoff + k * 1024, (NSEG - 1) * 28672, 0);
    }
    if constexpr (SPECIAL) {
        ready = wait_ready<2, 2>(rs, u & ~1, 0, mspin);
        TLW(1);
#pragma unroll
        for (int f = 0; f < 14; ++f) gx[f] = __builtin_amdgcn_raw_buffer_load_b128(rs, goff + f * 1024, (int)OFF_GF, 16);
    }
    PROF_DECL
    for (int s = 0; s < NSEG; ++s) {
        const int ho = hop_off(s, parmask);
        FENCE();
        STAMP(0);
        if constexpr (!SPECIAL) {
            if (s) { delay_cycles<DLY_M>(); delay_var(DM0); }
            FENCE();
            v4u xs = __builtin_amdgcn_raw_buffer_load_b128(rs, moff, ho, 16);
            FENCE(); delay_cycles<GAP_M>(); FENCE();
            const v4u x1 = __builtin_amdgcn_raw_buffer_load_b128(rs, moff, ho + CH, 16);
            FENCE(); delay_cycles<GAP_M>(); FENCE();
            const v4u x2 = __builtin_amdgcn_raw_buffer_load_b128(rs, moff, ho + 2 * CH, 16);
            FENCE();
#define MOV4(d_, s_) do { asm volatile("v_mov_b64 %0, %1" : "=v"(d_.lo) : "v"(s_.lo)); asm volatile("v_mov_b64 %0, %1" : "=v"(d_.hi) : "v"(s_.hi)); } while (0)
            if (UNLIKELY(!fresh(xs))) {
                FENCE();
                x3 = __builtin_amdgcn_raw_buffer_load_b128(rs, moff, ho + 3 * CH, 16);
                FENCE();
                CNT(1);
                MOV4(xs, x1);
                if (!fresh(xs)) {
                    CNT(1);
                    MOV4(xs, x2);
                    if (!fresh(xs)) {
                        CNT(1);
                        MOV4(xs, x3);
                        if (!fresh(xs)) {
                            for (;;) {
                                asm volatile("" ::: "memory");
                                xs = __builtin_amdgcn_raw_buffer_load_b128(rs, moff, ho + 3 * CH, 16);
                                CNT(1);
                                if (fresh(xs) || ++mspin >= SPIN_CAP) break;
                            }
                        }
                    }
                }
            }
            STAMP(1);
            *(LAS v4u*)mrd[0] = xs;
            FENCE();
            f32x4 a0 = {0.f, 0.f, 0.f, 0.f}, a1 = a0;
            a0 = smfmac(xs, tw[0], a0, idx); a1 = smfmac(xs, tw[7], a1, idx);
            asm volatile("" : "+v"(a0), "+v"(a1));
            FENCE();
            BAR();
            STAMP(2);
            v4u mp[7];
            delay_cycles<DLY1>();
            FENCE();
#pragma unroll
            for (int k = 1; k < 7; ++k) mp[k] = *(const LAS v4u*)mrd[k];
            FENCE();
#pragma unroll
            for (int k = 1; k < 7; ++k) { a0 = smfmac(mp[k], tw[k], a0, idx); a1 = smfmac(mp[k], tw[7 + k], a1, idx); }
            *(LAS v2u*)(L + L_TIM + (2 * w + 2 * half) * 512 + n * 32 + (g ^ (n >> 2)) * 8) = v2u{pack2(a0[0], a0[1]), pack2(a0[2], a0[3])};
            *(LAS v2u*)(L + L_TIM + (2 * w + 2 * half + 1) * 512 + n * 32 + (g ^ (n >> 2)) * 8) = v2u{pack2(a1[0], a1[1]), pack2(a1[2], a1[3])};
            STAMP(3);
            BAR();
            STAMP(4);
            asm volatile("" : "+v"(x3) : "v"(x1), "v"(x2));
            TLS(8 + s);
            if (ROLE == 1 && s == NSEG - 1) {
                v4u ta[7];
#pragma unroll
                for (int k = 0; k < 7; ++k) {
                    const v2u t0 = tr_read(L + L_TIM + (2 * k + 2 * half) * 512 + (l ^ g) * 8), t1 = tr_read(L + L_TIM + (2 * k + 2 * half + 1) * 512 + (l ^ g) * 8);
                    ta[k] = v4u{t0.x, t0.y, t1.x, t1.y};
                }
                FENCE();
                f32x4 d0 = {0.f, 0.f, 0.f, 0.f};
#pragma unroll
                for (int k = 0; k < 7; ++k) d0 = mfma32(ta[k], xq[k], d0);
                __builtin_amdgcn_raw_buffer_store_b64(v2u{sane(fbits(fold32(d0[0], d0[2]))), sane(fbits(fold32(d0[1], d0[3])))}, rs, w * 512 + l * 8,
                                                      (int)OFF_SXM + (s * 32 + u) * 1024, 16);
                FENCE();
            }
        } else {
            f32x4 ae = {0.f, 0.f, 0.f, 0.f};
            asm volatile("" : "+v"(ae));
            BAR();
            STAMP(2);
            v4u mp[7];
#pragma unroll
            for (int f = 0; f < 7; ++f) mp[f] = *(const LAS v4u*)(L + L_MST + f * 1024 + l * 16);
            FENCE();
            v4u m2[4];
#pragma unroll
            for (int f = 0; f < 7; ++f) {
                const v4u ga = gx[2 * f], gb = gx[2 * f + 1];
                ae = smfmac(mp[f], v8u{ga.x, ga.y, ga.z, ga.w, gb.x, gb.y, gb.z, gb.w}, ae, idx);
            }
            const unsigned px = packh(fold32(ae[0], ae[2]), fold32(ae[1], ae[3]));
            const unsigned pv = sane(__builtin_amdgcn_perm(__builtin_amdgcn_update_dpp(0u, px, 0xB1, 0xf, 0xf, true), px, ssel));
            __builtin_amdgcn_raw_buffer_store_b32(pv, rs, pst_off, ho, 16);
            __builtin_amdgcn_raw_buffer_store_b32(pv, rs, pst_off, ho + CH, 16);
            FENCE();
#pragma unroll
            for (int b = 0; b < 4; ++b) m2[b] = *(const LAS v4u*)(L + L_MST + (3 * half + b) * 1024 + l * 16);
            FENCE();
            STAMP(3);
            f32x4 sa = {0.f, 0.f, 0.f, 0.f};
            asm volatile("" : "+v"(sa));
            BAR();
            STAMP(4);
            v8u tb[4];
#pragma unroll
            for (int b = 0; b < 4; ++b) {
                const v2u t0 = tr_read(L + L_TIM + (4 * b) * 512 + (l ^ g) * 8), t1 = tr_read(L + L_TIM + (4 * b + 1) * 512 + (l ^ g) * 8),
                          t2 = tr_read(L + L_TIM + (4 * b + 2) * 512 + (l ^ g) * 8), t3 = tr_read(L + L_TIM + (4 * b + 3) * 512 + (l ^ g) * 8);
                tb[b] = v8u{t0.x, t0.y, t1.x, t1.y, t2.x, t2.y, t3.x, t3.y};
            }
            FENCE();
#pragma unroll
            for (int b = 0; b < 4; ++b) sa = smfmac(m2[b], tb[b], sa, idx);
            const unsigned pk = packh(add_ror8(fold32(sa[0], sa[2])), add_ror8(fold32(sa[1], sa[3])));
            const unsigned nb = __builtin_amdgcn_update_dpp(0u, pk, 0xB1, 0xf, 0xf, true);
            __builtin_amdgcn_raw_buffer_store_b32(sane(__builtin_amdgcn_perm(nb, pk, ssel)), rs, sst_off, ho, 16);
            FENCE();
            STAMP(5);
            delay_cycles<DLY_G>();
            if (UNLIKELY(ready <= s + 1 && s + 1 < NSEG)) ready = wait_ready<2, 2>(rs, u & ~1, s + 1, mspin);
            FENCE();
#pragma unroll
            for (int f = 0; f < 14; ++f) gx[f] = __builtin_amdgcn_raw_buffer_load_b128(rs, goff + f * 1024, (int)(OFF_GF + (s + 1 < NSEG ? s + 1 : s) * SEGB), 16);
            FENCE();
        }
        PROF_ACC;
    }
    PROF_OUT(u * 8 + w);
}

template <bool LAST, bool YOUNG>
__device__ __forceinline__ void value_wave(const Params& p, LAS unsigned char* L, const int v, const int w) {
    const int l = threadIdx.x & 63;
    const int g = l >> 4, i8 = l & 7, hh = (l >> 3) & 1;
    const __amdgpu_buffer_rsrc_t rs = __builtin_amdgcn_make_buffer_rsrc(p.ws, 0, (int)WS_END, 0x00020000);
    const int idx = hh ? 0xEEEE : 0x4444;
    const unsigned parmask = load_parmask(p.ws);
    v8u yw[14];
    const LAS unsigned char* mrd[7];
    {
        const unsigned char* src = p.ws + OFF_YW + (size_t)(v * 8 + w) * 14 * 2048 + l * 16;
#pragma unroll
        for (int k = 0; k < 7; ++k) {
            const int f = w + k < 7 ? w + k : w + k - 7;
            mrd[k] = L + L_MST + f * 1024 + l * 16;
#pragma unroll
            for (int t = 0; t < 2; ++t) {
                const v4u a = ldg16(src + (7 * t + f) * 2048), b = ldg16(src + (7 * t + f) * 2048 + 1024);
                yw[7 * t + k] = v8u{a.x, a.y, a.z, a.w, b.x, b.y, b.z, b.w};
            }
        }
    }
    if (LAST && PRIO_L) __builtin_amdgcn_s_setprio(2);
    if (YOUNG && PRIO_X == 3) __builtin_amdgcn_s_setprio(3);
    const int moff = C_M2 + w * 1024 + hh * 512 + g * 128 + i8 * 16;
    const int mst_off = C_M + v * 256 + ((l >> 2) & 1) * 128 + (l >> 3) * 16 + (l & 3) * 4;
    const int soff = C_S + ((2 * w + hh) * 2 + (g >> 1)) * 128 + i8 * 16 + (g & 1) * 8;
    const int poff = C_P + ((2 * w + hh) * 2 + (g >> 1)) * 256 + i8 * 32 + (g & 1) * 16;
    const int yst_off = (int)OFF_YM + (v * 8 + w) * 512 + l * 8;
    const int uoff = (v * 8 + w) * 2048 + l * 16;
    unsigned mlo = hh ? 0u : 0xFFFFFFFFu, mhi = ~mlo;
    v4u ones = v4u{0x3F803F80u, 0x3F803F80u, 0x3F803F80u, 0x3F803F80u};
    asm volatile("" : "+v"(mlo), "+v"(mhi), "+v"(ones));
#pragma unroll
    for (int k = 0; k < 7; ++k) asm volatile("" : "+v"(mrd[k]));
    unsigned spins = 0, mspin = 0;
    PROF_DECL
    int ho = hop_off(0, parmask);
    int ready = wait_ready<1, 3>(rs, 32 + v, 0, mspin);
    TLS(1);
    v4u ua = __builtin_amdgcn_raw_buffer_load_b128(rs, uoff, (int)OFF_UF, 16);
#define V_Y_PHASE(MIDA, MIDB) \
        f32x4 y0 = {0.f, 0.f, 0.f, 0.f}, y1 = y0; \
        if constexpr (!LAST) { \
            delay_cycles<DLY_V>(); \
            FENCE(); \
            v4u x = __builtin_amdgcn_raw_buffer_load_b128(rs, moff, ho, 16); \
            if (UNLIKELY(!fresh(x))) { \
                for (;;) { asm volatile("" ::: "memory"); x = __builtin_amdgcn_raw_buffer_load_b128(rs, moff, ho, 16); CNT(1); if (fresh(x) || ++mspin >= SPIN_CAP) break; } \
            } \
            STAMP(1); \
            *(LAS v4u*)mrd[0] = x; \
            FENCE(); \
            y0 = smfmac(x, yw[0], y0, idx); y1 = smfmac(x, yw[7], y1, idx); \
            asm volatile("" : "+v"(y0), "+v"(y1)); \
            FENCE(); \
        } \
        BAR(); \
        STAMP(2); \
        v4u mp[7]; \
        if constexpr (YOUNG) delay_cycles<DLY_Y>(); \
        FENCE(); \
        _Pragma("unroll") for (int k = LAST ? 0 : 1; k < 7; ++k) mp[k] = *(const LAS v4u*)mrd[k]; \
        FENCE(); \
        MIDA; \
        _Pragma("unroll") for (int k = LAST ? 0 : 1; k < 7; ++k) { \
            if (k == (YOUNG ? PPOSY : PPOS)) { MIDB; } \
            y0 = smfmac(mp[k], yw[k], y0, idx); y1 = smfmac(mp[k], yw[7 + k], y1, idx); \
        } \
        if ((YOUNG ? PPOSY : PPOS) == 7) { MIDB; } \
        v2u yop = v2u{pack2(fold32(y0[0], y0[2]), fold32(y0[1], y0[3])), pack2(fold32(y1[0], y1[2]), fold32(y1[1], y1[3]))}; \
        asm volatile("" : "+v"(yop)); \
        FENCE(); \
        STAMP(3)
    for (int s = 0; s < NSEG - 1; ++s) {
        const int hn = hop_off(s + 1, parmask);
        const int uo = (int)(OFF_UF + (s + 1) * SEGB);
        if (UNLIKELY(ready <= s + 1)) ready = wait_ready<1, 3>(rs, 32 + v, s + 1, mspin);
        if constexpr (LAST) {
            if (UNLIKELY((s + 2) % SPP == 0)) {
                const int sh = 4 * ((s + 2) / SPP - 1);
                for (;;) {
                    asm volatile("" ::: "memory");
                    const unsigned c = __builtin_amdgcn_readfirstlane(__builtin_amdgcn_raw_buffer_load_b32(rs, CT_PZ, 0, 16));
                    if (((c >> sh) & 15u) >= 6u || ++mspin >= SPIN_CAP) break;
                }
            }
        }
        int ho1 = ho + CH, ho2 = ho + 4096, hn1 = hn + CH, hn2 = hn + 2 * CH, hn3 = hn + 3 * CH, hnn = hn;
        asm volatile("" : "+s"(ho1), "+s"(ho2), "+s"(hn1), "+s"(hn2), "+s"(hn3), "+s"(hnn));
        FENCE();
        STAMP(0);
        v4u ub, pv;
        V_Y_PHASE(ub = __builtin_amdgcn_raw_buffer_load_b128(rs, uoff + 1024, uo - (int)SEGB, 16); FENCE(),
                  FENCE(); pv = __builtin_amdgcn_raw_buffer_load_b128(rs, poff, ho, 16); FENCE());
        if ((YOUNG ? PPOSY : PPOS) == 8) { delay_cycles<YOUNG ? DLY_P : DLY_P + DLY_PO>(); FENCE(); pv = __builtin_amdgcn_raw_buffer_load_b128(rs, poff, ho, 16); FENCE(); }
        delay_cycles<DLY_Q>();
        FENCE();
        const v4u pq = __builtin_amdgcn_raw_buffer_load_b128(rs, poff, ho1, 16);
        FENCE();
        v2u sv, sw2 = v2u{0xFFFFFFFFu, 0xFFFFFFFFu};
#define S_A() do { FENCE(); STAMP(6); sv = __builtin_amdgcn_raw_buffer_load_b64(rs, soff, ho, 16); FENCE(); } while (0)
#define S_B() do { FENCE(); sw2 = __builtin_amdgcn_raw_buffer_load_b64(rs, soff, ho2, 16); FENCE(); } while (0)
        if (SPA == -1) { delay_cycles<YOUNG ? DLY_S : DLY_S + DLY_SO>(); S_A(); }
        if (UNLIKELY(!fresh(pv))) {
            CNT(1 << 20);
            asm volatile("v_mov_b64 %0, %1" : "=v"(pv.lo) : "v"(pq.lo)); asm volatile("v_mov_b64 %0, %1" : "=v"(pv.hi) : "v"(pq.hi));
            if (!fresh(pv)) {
                for (;;) {
                    asm volatile("" ::: "memory");
                    pv = __builtin_amdgcn_raw_buffer_load_b128(rs, poff, ho1, 16);
                    CNT(1 << 20);
                    if (fresh(pv) || ++mspin >= SPIN_CAP) break;
                }
            }
        }
        STAMP(7);
        if (SPA == 0) S_A();
        if (SPB == 0) S_B();
        if (PRIO_X == 1 && YOUNG) __builtin_amdgcn_s_setprio(3);
        const unsigned hm = pkmaxh(pkmaxh(pv.x, pv.y), pkmaxh(pv.z, pv.w));
        const float mx = xg_max_raw(vmax(hlo(hm), hhi(hm)));
        if (SPA == 1) S_A();
        if (SPB == 1) S_B();
        const unsigned e0 = pack2(ex2(subh_lo(pv.x, mx)), ex2(subh_hi(pv.x, mx))), e1 = pack2(ex2(subh_lo(pv.y, mx)), ex2(subh_hi(pv.y, mx)));
        if (SPA == 2) S_A();
        if (SPB == 2) S_B();
        if (PRIO_X == 1 && YOUNG) __builtin_amdgcn_s_setprio(0);
        if (PRIO_X == 2 && YOUNG) __builtin_amdgcn_s_setprio(3);
        const unsigned e2 = pack2(ex2(subh_lo(pv.z, mx)), ex2(subh_hi(pv.z, mx))), e3 = pack2(ex2(subh_lo(pv.w, mx)), ex2(subh_hi(pv.w, mx)));
        const f32x4 z4 = {0.f, 0.f, 0.f, 0.f};
        const v4u eop = v4u{e0, e1, e2, e3};
        const f32x4 oa = mfma32(ua, eop, z4);
        const f32x4 ob = mfma32(ub, eop, z4);
        const f32x4 lx4 = mfma32(ones, eop, z4);
        if (SPB == 3) S_B();
        FENCE();
        STAMP(8);
        if (UNLIKELY(!__all(max(sv.x, sv.y) != 0xFFFFFFFFu))) {
            CNT(1 << 10);
            asm volatile("v_mov_b64 %0, %1" : "=v"(sv) : "v"(sw2));
            if (!__all(max(sv.x, sv.y) != 0xFFFFFFFFu)) {
                for (;;) {
                    asm volatile("" ::: "memory");
                    sv = __builtin_amdgcn_raw_buffer_load_b64(rs, soff, ho2, 16);
                    CNT(1 << 10);
                    if (__all(max(sv.x, sv.y) != 0xFFFFFFFFu) || ++mspin >= SPIN_CAP) break;
                }
            }
        }
        STAMP(9);
        const v2u sw = __builtin_amdgcn_permlane32_swap(sv.x, sv.y, false, false);
        const float t0 = addh_lo(sw.x, sw.y), t1 = addh_hi(sw.x, sw.y);
        float m;
        asm("v_max3_f32 %0, %1, %2, %3" : "=v"(m) : "v"(t0), "v"(t1), "v"(mx));
        f32x4 ox = hh ? ob : oa;
        m = xg_max_raw(m);
        const unsigned pk = pack2(ex2(t0 - m), ex2(t1 - m));
        const float beta = ex2(mx - m);
        const v2u bop = v2u{pk & mlo, pk & mhi};
        f32x4 lx = lx4;
        lx[0] *= beta;
        ox *= beta;
        FENCE();
        const f32x4 ls = mfma16(v2u{ones.x, ones.y}, bop, lx);
        f32x4 d = mfma16(yop, bop, ox);
        FENCE();
        const float r = __builtin_amdgcn_rcpf(ls[0]);
        d *= r;
        float r0, r1;
        asm("s_nop 1\n\tv_add_f32_dpp %0, %2, %2 row_ror:8 row_mask:0xf bank_mask:0x3\n\tv_add_f32_dpp %1, %3, %3 row_ror:8 row_mask:0xf bank_mask:0x3\n\t"
            "v_add_f32_dpp %0, %4, %4 row_ror:8 row_mask:0xf bank_mask:0xc\n\tv_add_f32_dpp %1, %5, %5 row_ror:8 row_mask:0xf bank_mask:0xc"
            : "=&v"(r0), "=&v"(r1) : "v"(d[0]), "v"(d[1]), "v"(d[2]), "v"(d[3]));
        *(LAS f32x2*)(L + L_RED + w * 512 + i8 * 64 + ((g ^ (i8 >> 1)) * 2 + hh) * 8) = f32x2{r0, r1};
        if (PRIO_X == 2 && YOUNG) __builtin_amdgcn_s_setprio(0);
        STAMP(10);
        FENCE();
        if constexpr (!LAST) ua = __builtin_amdgcn_raw_buffer_load_b128(rs, uoff, uo, 16);
        FENCE();
        BAR();
        STAMP(11);
        if constexpr (LAST) {
            f32x2 q[8];
#pragma unroll
            for (int k = 0; k < 8; ++k) q[k] = *(const LAS f32x2*)(L + L_RED + k * 512 + (l ^ (2 * g)) * 8);
            FENCE();
            const f32x2 tot = ((q[0] + q[1]) + (q[2] + q[3])) + ((q[4] + q[5]) + (q[6] + q[7]));
            const unsigned mv = sane(pack2(tot[0], tot[1]));
            __builtin_amdgcn_raw_buffer_store_b32(mv, rs, mst_off, hn3, 16);
            __builtin_amdgcn_raw_buffer_store_b32(mv, rs, mst_off, hn2, 16);
            __builtin_amdgcn_raw_buffer_store_b32(mv, rs, mst_off, hn1, 16);
            __builtin_amdgcn_raw_buffer_store_b32(mv, rs, mst_off, hnn, 16);
            __builtin_amdgcn_raw_buffer_store_b32(mv, rs, mst_off, hn + C_M2, 16);
            __builtin_amdgcn_raw_buffer_store_b32(mv, rs, mst_off, hn + C_M3, 16);
            FENCE();
            asm volatile("" :: "v"(sw2), "v"(pq));
            ua = __builtin_amdgcn_raw_buffer_load_b128(rs, uoff, uo, 16);
        } else {
            delay_cycles<256>();
            asm volatile("" :: "v"(sw2), "v"(pq));
        }
        __builtin_amdgcn_raw_buffer_store_b64(v2u{sane(yop.x), sane(yop.y)}, rs, yst_off, s * 224 * 512, 16);
        ho = hn;
        if constexpr (LAST) PROF_ACC;
    }
    {
        const int s = NSEG - 1;
        V_Y_PHASE(, );
        __builtin_amdgcn_raw_buffer_store_b64(v2u{sane(yop.x), sane(yop.y)}, rs, yst_off, s * 224 * 512, 16);
    }
    PROF_OUT(256 + v * 8 + w);
}

__device__ __forceinline__ void glds16(const void* gsrc, unsigned lds_dst) {
    unsigned keep;
    asm volatile("s_mov_b32 %0, m0\n\ts_mov_b32 m0, %2\n\ts_nop 0\n\tglobal_load_lds_dwordx4 %1, off\n\ts_mov_b32 m0, %0"
                 : "=&s"(keep) : "v"(gsrc), "s"(lds_dst) : "memory");
}
__device__ __forceinline__ v4u ldc16(const __amdgpu_buffer_rsrc_t rs, int voff, int soff) { return __builtin_amdgcn_raw_buffer_load_b128(rs, voff, soff, 16); }
__device__ __forceinline__ v4u pack8(f32x4 a, f32x4 b) { return v4u{pack2(a[0], a[1]), pack2(a[2], a[3]), pack2(b[0], b[1]), pack2(b[2], b[3])}; }
constexpr int L_XB = 0, XBUF = 28672;

template <bool GTYPE>
__device__ __forceinline__ void sweep(const Params& p, LAS unsigned char* L, const __amdgpu_buffer_rsrc_t rs, const int grp, const int r, const int w) {
    constexpr int STR = GTYPE ? 2 : 3;
    const int l = threadIdx.x & 63;
    const unsigned lbase = (unsigned)(size_t)L;
    const unsigned char* wsrc;
    int obase, ostride, osec;
    float oscale = 1.0f;
    if (GTYPE) {
        const int ks = 7 * (grp & 1) + w;
        if (w < 7) { wsrc = p.ws + OFF_TW + (size_t)((grp >> 1) * 14 + ks) * 28672; obase = (int)OFF_GF + ((grp >> 1) * 28 + ks) * 1024; ostride = (int)SEGB; osec = 14336; }
        else { wsrc = p.ws + OFF_QW + (size_t)grp * 28672; obase = (int)OFF_QF + (grp >> 4) * (2048 * 1024) + (grp & 15) * 1024; ostride = 32768; osec = 16384;
               if (grp < 16) oscale = LOG2E * 0.18898223650461362f; }
    } else {
        osec = 1024;
        if (grp < 60) { wsrc = p.ws + OFF_YW + (size_t)((grp - 32) * 8 + w) * 28672; obase = (int)OFF_UF + ((grp - 32) * 8 + w) * 2048; ostride = (int)SEGB; }
        else { wsrc = p.ws + OFF_QW + (size_t)(32 + (grp - 60) * 8 + w) * 28672; obase = (int)OFF_VF + ((grp - 60) * 8 + w) * 2048; ostride = 32768; }
    }
    const int npc = w < 4 ? 4 : 3, pc0 = w < 4 ? 4 * w : 16 + 3 * (w - 4);
    const unsigned char* xsrc = p.ws + OFF_XP + pc0 * 1024 + l * 16;
    if (r > 0) __builtin_amdgcn_s_sleep(SW_DLY);
    for (int k = 0; k < npc; ++k) glds16(xsrc + (size_t)r * XBUF + k * 1024, lbase + L_XB + (pc0 + k) * 1024);
    v4u wt[2][14];
#pragma unroll
    for (int f = 0; f < 14; ++f) { wt[0][f] = ldg16(wsrc + f * 1024 + l * 16); wt[1][f] = ldg16(wsrc + (14 + f) * 1024 + l * 16); }
#pragma unroll
    for (int f = 0; f < 14; ++f) asm volatile("" : "+v"(wt[0][f]), "+v"(wt[1][f]));
    asm volatile("s_waitcnt vmcnt(0)" ::: "memory");
    BAR();
    TLS(1);
    const int nseg = (NSEG + STR - 1 - r) / STR;
    for (int i = 0; i < nseg; ++i) {
        const int seg = r + STR * i;
        if (i + 1 < nseg) for (int k = 0; k < npc; ++k) glds16(xsrc + (size_t)(seg + STR) * XBUF + k * 1024, lbase + L_XB + ((i + 1) & 1) * XBUF + (pc0 + k) * 1024);
        const LAS unsigned char* xb = L + L_XB + (i & 1) * XBUF + l * 16;
        f32x4 a[2][2];
#pragma unroll
        for (int t = 0; t < 4; ++t) a[t >> 1][t & 1] = f32x4{0.f, 0.f, 0.f, 0.f};
#pragma unroll
        for (int f = 0; f < 14; ++f) {
            const v4u x0 = *(const LAS v4u*)(xb + f * 1024), x1 = *(const LAS v4u*)(xb + (14 + f) * 1024);
            if (GTYPE) {
                a[0][0] = mfma32(wt[0][f], x0, a[0][0]); a[0][1] = mfma32(wt[1][f], x0, a[0][1]);
                a[1][0] = mfma32(wt[0][f], x1, a[1][0]); a[1][1] = mfma32(wt[1][f], x1, a[1][1]);
            } else {
                a[0][0] = mfma32(x0, wt[0][f], a[0][0]); a[0][1] = mfma32(x0, wt[1][f], a[0][1]);
                a[1][0] = mfma32(x1, wt[0][f], a[1][0]); a[1][1] = mfma32(x1, wt[1][f], a[1][1]);
            }
        }
        v4u o0 = GTYPE ? pack8(a[0][0] * oscale, a[0][1] * oscale) : pack8(a[0][0], a[1][0]);
        v4u o1 = GTYPE ? pack8(a[1][0] * oscale, a[1][1] * oscale) : pack8(a[0][1], a[1][1]);
        __builtin_amdgcn_raw_buffer_store_b128(o0, rs, l * 16, obase + seg * ostride, 16);
        __builtin_amdgcn_raw_buffer_store_b128(o1, rs, l * 16, obase + seg * ostride + osec, 16);
        const bool drain = (i < 2 || i + 1 == nseg);
        if (drain) asm volatile("s_waitcnt vmcnt(0)" ::: "memory"); else asm volatile("s_waitcnt vmcnt(2)" ::: "memory");
        BAR();
        if (threadIdx.x == 0) __builtin_amdgcn_raw_buffer_store_b32((unsigned)(drain ? i + 1 : i), rs, CT_PROG + (grp * 3 + r) * 128, 0, 16);
        if (i == 0) TLS(2);
    }
    if (threadIdx.x == 0) __hip_atomic_fetch_add((GAS unsigned*)(p.ws + CT_DONE), 1u, __ATOMIC_RELAXED, __HIP_MEMORY_SCOPE_AGENT);
    TLS(3);
}

__device__ __forceinline__ void serve(const Params& p, LAS unsigned char* L, const __amdgpu_buffer_rsrc_t rs, const int grp, const int w) {
    const int l = threadIdx.x & 63, n = l & 15, g = l >> 4;
    const int ks = 7 * (grp & 1) + w;
    v4u wt[2][14];
    {
        const unsigned char* wsrc = p.ws + OFF_TW + (size_t)((grp >> 1) * 14 + (w < 7 ? ks : 0)) * 28672;
        __builtin_amdgcn_s_sleep(100);
#pragma unroll
        for (int f = 0; f < 14; ++f) { wt[0][f] = ldg16(wsrc + f * 1024 + l * 16); wt[1][f] = ldg16(wsrc + (14 + f) * 1024 + l * 16); }
#pragma unroll
        for (int f = 0; f < 14; ++f) asm volatile("" : "+v"(wt[0][f]), "+v"(wt[1][f]));
    }
    const unsigned parmask = load_parmask(p.ws);
    unsigned mspin = 0;
    const int mrow = 4 * ((n >> 2) & 1) + 2 * (n >> 3) + (n & 1);
    const LAS unsigned char* mrd = L + (g >> 1) * 128 + mrow * 16 + (g & 1) * 8;
    const unsigned char* xr = p.ws + OFF_XP + (w < 7 ? ks : 0) * 1024 + l * 16;
    v4u mx_ = ldc16(rs, (w < 7 ? w : 0) * 1024 + l * 16, hop_off(0, parmask) + C_M3);
    for (int s = 0; s < NSEG - 1; ++s) {
        v4u xa, xb2;
        if (w < 7) {
            xa = ldg16(xr + (size_t)s * 28672); xb2 = ldg16(xr + (size_t)s * 28672 + 14336);
            const int ho = hop_off(s, parmask) + C_M3;
            while (!fresh(mx_) && ++mspin < SPIN_CAP) {
                __builtin_amdgcn_s_sleep(2);
                asm volatile("" ::: "memory");
                mx_ = ldc16(rs, w * 1024 + l * 16, ho);
            }
            *(LAS v4u*)(L + w * 1024 + l * 16) = mx_;
            FENCE();
            mx_ = ldc16(rs, w * 1024 + l * 16, hop_off(s + 1 < NSEG ? s + 1 : s, parmask) + C_M3);
        }
        BAR();
        if (w < 7) {
            v2u lo[14], hi[14];
#pragma unroll
            for (int f = 0; f < 14; ++f) {
                lo[f] = *(const LAS v2u*)(mrd + (f >> 1) * 1024 + (f & 1) * 256); hi[f] = *(const LAS v2u*)(mrd + (f >> 1) * 1024 + (f & 1) * 256 + 512);
            }
            FENCE();
            f32x4 a0 = {0.f, 0.f, 0.f, 0.f}, a1 = a0;
#pragma unroll
            for (int f = 0; f < 14; ++f) {
                const v4u mf = v4u{lo[f].x, hi[f].x, lo[f].y, hi[f].y};
                a0 = mfma32(wt[0][f], mf, a0); a1 = mfma32(wt[1][f], mf, a1);
            }
            const v4u gm = pack8(a0, a1);
            const f32x4 z4 = {0.f, 0.f, 0.f, 0.f};
            const f32x4 d0 = mfma32(gm, xa, z4), d1 = mfma32(gm, xb2, z4);
            *(LAS f32x4*)(L + 8192 + w * 1024 + l * 16) = f32x4{d0[0], d0[1], d1[0], d1[1]};
        }
        BAR();
        TLS(8 + s);
        if (w == 7) {
            f32x4 q[7];
#pragma unroll
            for (int k = 0; k < 7; ++k) q[k] = *(const LAS f32x4*)(L + 8192 + k * 1024 + l * 16);
            const f32x4 t = ((q[0] + q[1]) + (q[2] + q[3])) + ((q[4] + q[5]) + q[6]);
            __builtin_amdgcn_raw_buffer_store_b64(v2u{sane(fbits(t[0])), sane(fbits(t[1]))}, rs, l * 8, (int)OFF_SXM + (s * 32 + grp) * 1024, 16);
            __builtin_amdgcn_raw_buffer_store_b64(v2u{sane(fbits(t[2])), sane(fbits(t[3]))}, rs, l * 8 + 512, (int)OFF_SXM + (s * 32 + grp) * 1024, 16);
        }
    }
}

__device__ __forceinline__ void attn_item(const Params& p, const __amdgpu_buffer_rsrc_t rs, const int item) {
    const int l = threadIdx.x & 63, n = l & 15, g = l >> 4;
    const int rt = item >> 4, h = item & 15;
    const int s = rt >> 1, q = rt * 16 + n;
    const v4u qf = ldc16(rs, l * 16, (int)OFF_QF + (rt * 16 + h) * 1024);
    f32x4 sc[18];
    const f32x4 z4 = {0.f, 0.f, 0.f, 0.f};
    const float NEG = -3.0e38f;
    v4u kf[18];
#pragma unroll
    for (int k = 0; k < 18; ++k) { const int kt = max(2 * (s - 8) + k, 0); kf[k] = ldc16(rs, l * 16, (int)OFF_KF + (kt * 16 + h) * 1024); }
#pragma unroll
    for (int k = 0; k < 18; ++k) {
        const int kt = 2 * (s - 8) + k;
        f32x4 a = mfma32(kf[k], qf, z4);
        if (k < 2 || k >= 16) {
#pragma unroll
            for (int r = 0; r < 4; ++r) {
                const int dq = q - (16 * kt + 4 * g + r);
                a[r] = (dq >= 0 && dq < 256) ? a[r] : NEG;
            }
        }
        sc[k] = kt >= 0 ? a : f32x4{NEG, NEG, NEG, NEG};
    }
    FENCE();
    v4u vf[18];
#pragma unroll
    for (int k = 0; k < 18; ++k) { const int b = max(s - 8 + (k >> 1), 0); vf[k] = ldc16(rs, l * 16, (int)OFF_VF + ((b * 16 + h) * 2 + (k & 1)) * 1024); }
    float m = NEG;
#pragma unroll
    for (int k = 0; k < 18; ++k) m = fmaxf(m, fmaxf(fmaxf(sc[k][0], sc[k][1]), fmaxf(sc[k][2], sc[k][3])));
    m = xg_max(m);
    float sum = 0.f;
    f32x4 o0 = z4, o1 = z4;
#pragma unroll
    for (int blk = 0; blk < 9; ++blk) {
        f32x4 e0, e1;
#pragma unroll
        for (int r = 0; r < 4; ++r) { e0[r] = ex2(sc[2 * blk][r] - m); e1[r] = ex2(sc[2 * blk + 1][r] - m); }
        sum += ((e0[0] + e0[1]) + (e0[2] + e0[3])) + ((e1[0] + e1[1]) + (e1[2] + e1[3]));
        const v4u pb = pack8(e0, e1);
        o0 = mfma32(vf[2 * blk], pb, o0);
        o1 = mfma32(vf[2 * blk + 1], pb, o1);
    }
    sum = xg_sum(sum);
    const float f = ((const float*)(p.ws + OFF_GATE))[q] / sum;
    __builtin_amdgcn_raw_buffer_store_b128(pack8(o0 * f, o1 * f), rs, l * 16, (int)OFF_OF + (rt * 16 + h) * 1024, 16);
    asm volatile("s_waitcnt vmcnt(0)" ::: "memory");
    if (l == 0) __hip_atomic_fetch_add((GAS unsigned*)(p.ws + CT_OFC + s * 128), 1u, __ATOMIC_RELAXED, __HIP_MEMORY_SCOPE_AGENT);
}

__device__ __forceinline__ void out_item(const Params& p, LAS unsigned char* L, const __amdgpu_buffer_rsrc_t rs, const int s, const int q4, const int w, unsigned& mspin) {
    const int l = threadIdx.x & 63, n = l & 15, g = l >> 4;
    const f32x4 z4 = {0.f, 0.f, 0.f, 0.f};
    const float NEG = -3.0e38f;
    const int v = 7 * q4 + (w < 7 ? w : 0);
    f32x4 sxx[2][3];
#pragma unroll
    for (int a = 0; a < 2; ++a) { sxx[a][0] = z4; sxx[a][1] = z4; sxx[a][2] = z4; }
    {
        const int gp = (int)OFF_GF + s * (int)SEGB + (2 * w) * 28 * 1024;
        const unsigned char* xr = p.ws + OFF_XP + (size_t)s * 28672 + l * 16;
#pragma unroll 1
        for (int kh = 0; kh < 2; ++kh) {
            v4u x0[7], x1[7], kk[4][7];
#pragma unroll
            for (int j = 0; j < 7; ++j) {
                const int ks = 7 * kh + j;
                x0[j] = ldg16(xr + ks * 1024); x1[j] = ldg16(xr + (14 + ks) * 1024);
#pragma unroll
                for (int t = 0; t < 4; ++t) kk[t][j] = ldc16(rs, l * 16, gp + (t * 14 + ks) * 1024);
            }
            FENCE();
#pragma unroll
            for (int j = 0; j < 7; ++j) {
                const v4u xa = x0[j], xb = x1[j];
#pragma unroll
                for (int a = 0; a < 2; ++a) {
                    sxx[a][0] = mfma32(kk[2 * a][j], xa, sxx[a][0]);
                    sxx[a][1] = mfma32(kk[2 * a][j], xb, sxx[a][1]);
                    sxx[a][2] = mfma32(kk[2 * a + 1][j], xb, sxx[a][2]);
                }
            }
            FENCE();
        }
    }
    if (w == 0) {
        for (;;) {
            asm volatile("" ::: "memory");
            const unsigned c = __builtin_amdgcn_raw_buffer_load_b32(rs, CT_OFC + s * 128, 0, 16);
            if (__builtin_amdgcn_readfirstlane(c) >= 32u || ++mspin >= SPIN_CAP) break;
            __builtin_amdgcn_s_sleep(4);
        }
    }
    BAR();
    f32x4 o0 = z4, o1 = z4;
    {
        const unsigned char* wp = p.ws + OFF_WOF + (size_t)(v * 16) * 1024 + l * 16;
#pragma unroll 1
        for (int hb = 0; hb < 16; hb += 8) {
            v4u wf[8], oa[8], ob[8];
#pragma unroll
            for (int h = 0; h < 8; ++h) {
                wf[h] = ldg16(wp + (hb + h) * 1024);
                oa[h] = ldc16(rs, l * 16, (int)OFF_OF + (2 * s * 16 + hb + h) * 1024);
                ob[h] = ldc16(rs, l * 16, (int)OFF_OF + ((2 * s + 1) * 16 + hb + h) * 1024);
            }
            FENCE();
#pragma unroll
            for (int h = 0; h < 8; ++h) { o0 = mfma32(wf[h], oa[h], o0); o1 = mfma32(wf[h], ob[h], o1); }
            FENCE();
        }
    }
    v4u uf[16];
#pragma unroll
    for (int h = 0; h < 16; ++h) uf[h] = ldc16(rs, l * 16, (int)OFF_UF + s * (int)SEGB + (v * 16 + h) * 1024);
    const float gate0 = 1.0f - ((const float*)(p.ws + OFF_GATE))[32 * s + n], gate1 = 1.0f - ((const float*)(p.ws + OFF_GATE))[32 * s + 16 + n];
    float mxx[2][2];
#pragma unroll
    for (int a = 0; a < 2; ++a) {
#pragma unroll
        for (int r = 0; r < 4; ++r) { sxx[a][0][r] = (4 * g + r <= n) ? sxx[a][0][r] : NEG; sxx[a][2][r] = (4 * g + r <= n) ? sxx[a][2][r] : NEG; }
        mxx[a][0] = fmaxf(fmaxf(sxx[a][0][0], sxx[a][0][1]), fmaxf(sxx[a][0][2], sxx[a][0][3]));
        mxx[a][1] = fmaxf(fmaxf(fmaxf(sxx[a][1][0], sxx[a][1][1]), fmaxf(sxx[a][1][2], sxx[a][1][3])), fmaxf(fmaxf(sxx[a][2][0], sxx[a][2][1]), fmaxf(sxx[a][2][2], sxx[a][2][3])));
    }
    FENCE();
    TLS(s < 40 ? 8 : 10);
    v4u sm[4];
    for (;;) {
        asm volatile("" ::: "memory");
#pragma unroll
        for (int k = 0; k < 4; ++k) {
            const v2u q0 = __builtin_amdgcn_raw_buffer_load_b64(rs, l * 8, (int)OFF_SXM + (s * 32 + 4 * w + k) * 1024, 16),
                      q1 = __builtin_amdgcn_raw_buffer_load_b64(rs, l * 8 + 512, (int)OFF_SXM + (s * 32 + 4 * w + k) * 1024, 16);
            sm[k] = v4u{q0.x, q0.y, q1.x, q1.y};
        }
        if (fresh(__builtin_elementwise_max(__builtin_elementwise_max(sm[0], sm[1]), __builtin_elementwise_max(sm[2], sm[3]))) || ++mspin >= SPIN_CAP) break;
        __builtin_amdgcn_s_sleep(6);
    }
    TLS(s < 40 ? 9 : 11);
    if (s + 2 < NSEG) {
        for (;;) {
            asm volatile("" ::: "memory");
            const unsigned x = __builtin_amdgcn_raw_buffer_load_b32(rs, 0, (int)OFF_SXM + ((s + 2) * 32 + 4 * w) * 1024, 16);
            if (__builtin_amdgcn_readfirstlane(x) != 0xFFFFFFFFu || ++mspin >= SPIN_CAP) break;
            __builtin_amdgcn_s_sleep(6);
        }
        asm volatile("" ::: "memory");
    }
    v2u yf[8];
#pragma unroll
    for (int k = 0; k < 8; ++k) yf[k] = __builtin_amdgcn_raw_buffer_load_b64(rs, l * 8, (int)OFF_YM + (s * 224 + v * 8 + k) * 512, 16);
    unsigned px[2][2][4]; unsigned pm[2][2];
#pragma unroll
    for (int qt = 0; qt < 2; ++qt) {
#pragma unroll
        for (int a = 0; a < 2; ++a) {
            const float t0 = bitsf(sm[2 * a][2 * qt]) + bitsf(sm[2 * a + 1][2 * qt]), t1 = bitsf(sm[2 * a][2 * qt + 1]) + bitsf(sm[2 * a + 1][2 * qt + 1]);
            const float m = xg_max(fmaxf(mxx[a][qt], fmaxf(t0, t1)));
            const f32x4 dg = sxx[a][qt == 0 ? 0 : 2];
            float ed[4], ef[4];
            const float e0 = ex2(t0 - m), e1 = ex2(t1 - m);
            float sum = e0 + e1;
#pragma unroll
            for (int r = 0; r < 4; ++r) {
                ed[r] = ex2(dg[r] - m); sum += ed[r];
                if (qt == 1) { ef[r] = ex2(sxx[a][1][r] - m); sum += ef[r]; }
            }
            sum = xg_sum(sum);
            const float f = (qt == 0 ? gate0 : gate1) * __builtin_amdgcn_rcpf(sum);
            if (qt == 0) { px[a][0][0] = pack2(ed[0] * f, ed[1] * f); px[a][0][1] = pack2(ed[2] * f, ed[3] * f); }
            else {
                px[a][1][0] = pack2(ef[0] * f, ef[1] * f); px[a][1][1] = pack2(ef[2] * f, ef[3] * f);
                px[a][1][2] = pack2(ed[0] * f, ed[1] * f); px[a][1][3] = pack2(ed[2] * f, ed[3] * f);
            }
            pm[qt][a] = pack2(e0 * f, e1 * f);
        }
    }
    *(LAS v4u*)(L + w * 4096 + l * 16) = v4u{px[0][0][0], px[0][0][1], px[1][0][0], px[1][0][1]};
    *(LAS v4u*)(L + w * 4096 + 1024 + l * 16) = v4u{px[0][1][0], px[0][1][1], px[0][1][2], px[0][1][3]};
    *(LAS v4u*)(L + w * 4096 + 2048 + l * 16) = v4u{px[1][1][0], px[1][1][1], px[1][1][2], px[1][1][3]};
    *(LAS v4u*)(L + w * 4096 + 3072 + l * 16) = v4u{pm[0][0], pm[0][1], pm[1][0], pm[1][1]};
    for (;;) {
        unsigned mm = 0u;
#pragma unroll
        for (int k = 0; k < 8; ++k) mm = max(mm, max(yf[k].x, yf[k].y));
        if (__all(mm != 0xFFFFFFFFu) || ++mspin >= SPIN_CAP) break;
        asm volatile("" ::: "memory");
#pragma unroll
        for (int k = 0; k < 8; ++k) yf[k] = __builtin_amdgcn_raw_buffer_load_b64(rs, l * 8, (int)OFF_YM + (s * 224 + v * 8 + k) * 512, 16);
    }
    BAR();
    if (w < 7) {
#pragma unroll
        for (int pp = 0; pp < 4; ++pp) {
            v4u pmm[2];
#pragma unroll
            for (int z = 0; z < 2; ++z) {
                const int pr = 2 * pp + z;
                const v4u u0 = uf[2 * pr], u1 = uf[2 * pr + 1];
                const v4u pa = *(const LAS v4u*)(L + pr * 4096 + l * 16), pb = *(const LAS v4u*)(L + pr * 4096 + 1024 + l * 16),
                          pc = *(const LAS v4u*)(L + pr * 4096 + 2048 + l * 16);
                pmm[z] = *(const LAS v4u*)(L + pr * 4096 + 3072 + l * 16);
                o1 = mfma32(u0, pb, o1);
                o0 = mfma32(v4u{u0.x, u0.y, u1.x, u1.y}, pa, o0);
                o1 = mfma32(u1, pc, o1);
            }
            const v4u yy = v4u{yf[2 * pp].x, yf[2 * pp].y, yf[2 * pp + 1].x, yf[2 * pp + 1].y};
            o0 = mfma32(yy, v4u{pmm[0].x, pmm[0].y, pmm[1].x, pmm[1].y}, o0);
            o1 = mfma32(yy, v4u{pmm[0].z, pmm[0].w, pmm[1].z, pmm[1].w}, o1);
        }
        stg8(p.out + (size_t)(32 * s + n) * 448 + 16 * v + 4 * g, v2u{pack2(o0[0], o0[1]), pack2(o0[2], o0[3])});
        stg8(p.out + (size_t)(32 * s + 16 + n) * 448 + 16 * v + 4 * g, v2u{pack2(o1[0], o1[1]), pack2(o1[2], o1[3])});
    }
}

__global__ void __launch_bounds__(512, 2) k_main(Params p) {
    extern __shared__ __attribute__((aligned(16))) unsigned char lds_[];
    p.ws = align_ws(p.ws);
    const int rot = __builtin_amdgcn_readfirstlane(((const int*)(p.ws + OFF_CTL))[8]);
    const int grp = (blockIdx.x + rot) & 7, u = blockIdx.x >> 3;
    LAS unsigned char* L = (LAS unsigned char*)lds_;
    const int w = __builtin_amdgcn_readfirstlane(threadIdx.x >> 6);
    TLS(0);
    if (grp == 0) {
        if (w < 2) score_wave<1, 0>(p, L, u, w); else if (w == 2) score_wave<0, 0>(p, L, u, w); else if (w == 3) score_wave<0, DLY_3>(p, L, u, w);
        else if (w < 7) score_wave<0, DLY_4>(p, L, u, w); else score_wave<2, 0>(p, L, u, w);
        TLS(7);
        return;
    }
    if (grp == 1) {
        if (u < 28) { if (w < 3) value_wave<false, false>(p, L, u, w); else if (w < 7) value_wave<false, true>(p, L, u, w); else value_wave<true, false>(p, L, u, w); }
        TLS(7);
        return;
    }
    const __amdgpu_buffer_rsrc_t rs = __builtin_amdgcn_make_buffer_rsrc(p.ws, 0, (int)WS_END, 0x00020000);
    const int k = (grp - 2) * 32 + u;
    if (k < 96) {
        if (k % 3 == 0) { serve(p, L, rs, k / 3, w); TLS(7); return; }
        sweep<true>(p, L, rs, k / 3, k % 3 - 1, w);
    } else if (k < 186) sweep<false>(p, L, rs, k / 3, k % 3, w);
    else {
        const unsigned parmask = load_parmask(p.ws);
        const int l = threadIdx.x & 63, wi = (k - 186) * 8 + w;
        const v4u ff = v4u{0xFFFFFFFFu, 0xFFFFFFFFu, 0xFFFFFFFFu, 0xFFFFFFFFu};
        __builtin_amdgcn_s_sleep(127); __builtin_amdgcn_s_sleep(127);
        for (int pg = 1; pg < NPAGE; ++pg) {
            const int base = hop_off(pg * SPP, parmask);
            for (int i = wi; i < SPP * NCHUNK * 8; i += 48) __builtin_amdgcn_raw_buffer_store_b128(ff, rs, l * 16, base + (i >> 3) * CH + (i & 7) * 1024, 16);
            for (int i = wi; i < SPP * 32 && pg * SPP * 32 + i < NSEG * 32; i += 48) __builtin_amdgcn_raw_buffer_store_b128(ff, rs, l * 16, (int)OFF_SXM + (pg * SPP * 32 + i) * 1024, 16);
            asm volatile("s_waitcnt vmcnt(0)" ::: "memory");
            BAR();
            if (threadIdx.x == 0) __hip_atomic_fetch_add((GAS unsigned*)(p.ws + CT_PZ), 1u << (4 * (pg - 1)), __ATOMIC_RELAXED, __HIP_MEMORY_SCOPE_AGENT);
        }
        if (threadIdx.x == 0) __hip_atomic_fetch_add((GAS unsigned*)(p.ws + CT_DONE), 1u, __ATOMIC_RELAXED, __HIP_MEMORY_SCOPE_AGENT);
    }
    const int wk = k < 96 ? (k / 3) * 2 + (k % 3 - 1) : k - 32;
    unsigned mspin = 0;
    if (w == 0) {
        for (;;) {
            asm volatile("" ::: "memory");
            const unsigned c = __builtin_amdgcn_raw_buffer_load_b32(rs, CT_DONE, 0, 16);
            if (__builtin_amdgcn_readfirstlane(c) >= 160u || ++mspin >= SPIN_CAP) break;
            __builtin_amdgcn_s_sleep(16);
        }
    }
    BAR();
    TLS(4);
    for (int it = wk * 8 + w; it < 2048; it += 1280) attn_item(p, rs, it);
    TLS(5);
    for (int i = 0; i < (wk * STAG) >> 4; ++i) __builtin_amdgcn_s_sleep(64);
    for (int it = wk; it < 256; it += 160) { out_item(p, L, rs, it >> 2, it & 3, w, mspin); TLS(it < 160 ? 6 : 7); }
}

extern "C" void kernel_launch(void* const* d_in, const int* in_sizes, int n_in, void* d_out, int out_size, void* d_ws, size_t ws_size,
                              hipStream_t stream) {
    Params p;
    p.x = (const u16*)d_in[0]; p.wqkv = (const u16*)d_in[1]; p.wo = (const u16*)d_in[2]; p.mem0 = (const u16*)d_in[4];
    p.Wq = (const u16*)d_in[5]; p.Wk = (const u16*)d_in[6]; p.Wv = (const u16*)d_in[7]; p.Wo = (const u16*)d_in[8];
    p.gate_w = (const u16*)d_in[10];
    p.out = (u16*)d_out; p.ws = (unsigned char*)d_ws;
    (void)hipFuncSetAttribute((const void*)k_prep, hipFuncAttributeMaxDynamicSharedMemorySize, K1_LDS);
    (void)hipFuncSetAttribute((const void*)k_main, hipFuncAttributeMaxDynamicSharedMemorySize, L_REC_END);
    k_prep<<<256, 512, K1_LDS, stream>>>(p);
    k_main<<<256, 512, L_REC_END, stream>>>(p);
}
```

```cpp
#define DM0 19
#include <hip/hip_runtime.h>
#include <hip/hip_bf16.h>
#include <stdint.h>

#define GAS __attribute__((address_space(1)))
#define LAS __attribute__((address_space(3)))
typedef __bf16 bf16x2 __attribute__((ext_vector_type(2)));
typedef __bf16 bf16x4 __attribute__((ext_vector_type(4)));
typedef __bf16 bf16x8 __attribute__((ext_vector_type(8)));
typedef __bf16 bf16x16 __attribute__((ext_vector_type(16)));
typedef short s16x4 __attribute__((ext_vector_type(4)));
typedef float f32x2 __attribute__((ext_vector_type(2)));
typedef float f32x4 __attribute__((ext_vector_type(4)));
typedef unsigned v2u __attribute__((ext_vector_type(2)));
typedef unsigned v4u __attribute__((ext_vector_type(4)));
typedef unsigned v8u __attribute__((ext_vector_type(8)));
typedef unsigned short u16;

constexpr int S_ = 2048, D_ = 448, H_ = 16, NSEG = 64;
constexpr float LOG2E = 1.4426950408889634f;

constexpr size_t SZ_W = (size_t)16 * 28 * 14 * 1024;
constexpr size_t OFF_HOP = 0;
constexpr int PAGE = 2 << 20, CH = 16384, NCHUNK = 9, STEPB = NCHUNK * CH, NPAGE = 5, SPP = 14;
constexpr int C_M = 0, C_M2 = 4 * CH, C_M3 = 5 * CH, C_S = 6 * CH, C_P = 7 * CH;
constexpr size_t HOP_BYTES = (size_t)NPAGE * PAGE;
constexpr size_t OFF_CTL = OFF_HOP + HOP_BYTES;
constexpr size_t CTL_BYTES = 36864;
constexpr int CT_DONE = (int)OFF_CTL + 256, CT_PZ = (int)OFF_CTL + 384, CT_OFC = (int)OFF_CTL + 512, CT_PROG = CT_OFC + 64 * 128;
constexpr size_t OFF_TW = OFF_CTL + CTL_BYTES;
constexpr size_t OFF_YW = OFF_TW + SZ_W;
constexpr size_t OFF_QW = OFF_YW + SZ_W;
constexpr size_t OFF_VW = OFF_QW + (size_t)32 * 28 * 1024;
constexpr size_t OFF_WOF = OFF_VW + (size_t)16 * 28 * 1024;
constexpr size_t OFF_XP = OFF_WOF + (size_t)28 * 16 * 1024;
constexpr size_t OFF_GATE = OFF_XP + (size_t)128 * 14 * 1024;
constexpr size_t SEGB = (size_t)448 * 1024;
constexpr size_t OFF_GF = OFF_GATE + 8192;
constexpr size_t OFF_UF = OFF_GF + 64 * SEGB;
constexpr size_t OFF_QF = OFF_UF + 64 * SEGB;
constexpr size_t OFF_KF = OFF_QF + (size_t)2048 * 1024;
constexpr size_t OFF_VF = OFF_KF + (size_t)2048 * 1024;
constexpr size_t OFF_OF = OFF_VF + (size_t)2048 * 1024;
constexpr size_t OFF_SXM = OFF_OF + (size_t)2048 * 1024;
constexpr size_t OFF_YM = OFF_SXM + (size_t)64 * 32 * 1024;
constexpr size_t WS_END = OFF_YM + (size_t)64 * 224 * 512;

struct Params {
    const u16* x; const u16* wqkv; const u16* wo; const u16* mem0;
    const u16* Wq; const u16* Wk; const u16* Wv; const u16* Wo; const u16* gate_w;
    u16* out; unsigned char* ws;
};
__device__ __forceinline__ unsigned char* align_ws(unsigned char* w) {
    return w + ((0 - (size_t)w) & (size_t)(PAGE - 1));
}
__device__ __forceinline__ unsigned xcc_id() { return (unsigned)__builtin_amdgcn_s_getreg(20 | (3 << 11)) & 0xfu; }
__device__ __forceinline__ unsigned load_parmask(const unsigned char* ws) {
    const unsigned* c = (const unsigned*)(ws + OFF_CTL);
    unsigned m = 0;
#pragma unroll
    for (int k = 0; k < NPAGE; ++k) m |= (c[k] & 1u) << k;
    return __builtin_amdgcn_readfirstlane(m);
}
__device__ __forceinline__ int hop_off(int s, unsigned parmask) {
    const int pg = s / SPP, sl = s - pg * SPP;
    return pg * PAGE + sl * STEPB + (int)((parmask >> pg) & 1u) * 8192;
}

__device__ __forceinline__ unsigned pack2(float a, float b) {
    return __builtin_bit_cast(unsigned, __builtin_convertvector(f32x2{a, b}, bf16x2));
}
__device__ __forceinline__ float bf2f(u16 v) { return __builtin_bit_cast(float, (unsigned)v << 16); }
__device__ __forceinline__ f32x4 mfma32(v4u a, v4u b, f32x4 c) {
    return __builtin_amdgcn_mfma_f32_16x16x32_bf16(__builtin_bit_cast(bf16x8, a), __builtin_bit_cast(bf16x8, b), c, 0, 0, 0);
}
__device__ __forceinline__ f32x4 mfma16(v2u a, v2u b, f32x4 c) {
    return __builtin_amdgcn_mfma_f32_16x16x16bf16_1k(__builtin_bit_cast(s16x4, a), __builtin_bit_cast(s16x4, b), c, 0, 0, 0);
}
__device__ __forceinline__ f32x4 smfmac(v4u a, v8u b, f32x4 c, int idx) {
    return __builtin_amdgcn_smfmac_f32_16x16x64_bf16(__builtin_bit_cast(bf16x8, a), __builtin_bit_cast(bf16x16, b), c, idx, 0, 0);
}
__device__ __forceinline__ v4u ldg16(const void* p) { return *(const GAS v4u*)p; }
__device__ __forceinline__ v2u ldg8(const void* p) { return *(const GAS v2u*)p; }
__device__ __forceinline__ void stg16(void* p, v4u v) { *(GAS v4u*)p = v; }
__device__ __forceinline__ void stg8(void* p, v2u v) { *(GAS v2u*)p = v; }
__device__ __forceinline__ void stc16(v4u v, const __amdgpu_buffer_rsrc_t rs, int voff, int soff) { __builtin_amdgcn_raw_buffer_store_b128(v, rs, voff + soff, 0, 16); }
__device__ __forceinline__ v2u tr_read(const LAS void* p) {
    return __builtin_bit_cast(v2u, __builtin_amdgcn_ds_read_tr16_b64_v4i16((LAS s16x4*)p));
}
__device__ __forceinline__ float ror8(float v) {
    return __builtin_bit_cast(float, __builtin_amdgcn_update_dpp(0, __builtin_bit_cast(int, v), 0x128, 0xf, 0xf, false));
}
__device__ __forceinline__ unsigned fbits(float v) { return __builtin_bit_cast(unsigned, v); }
__device__ __forceinline__ float bitsf(unsigned v) { return __builtin_bit_cast(float, v); }
__device__ __forceinline__ float xg_max(float v) {
    const v2u a = __builtin_amdgcn_permlane16_swap(fbits(v), fbits(v), false, false);
    v = fmaxf(bitsf(a.x), bitsf(a.y));
    const v2u b = __builtin_amdgcn_permlane32_swap(fbits(v), fbits(v), false, false);
    return fmaxf(bitsf(b.x), bitsf(b.y));
}
__device__ __forceinline__ float xg_sum(float v) {
    const v2u a = __builtin_amdgcn_permlane16_swap(fbits(v), fbits(v), false, false);
    v = bitsf(a.x) + bitsf(a.y);
    const v2u b = __builtin_amdgcn_permlane32_swap(fbits(v), fbits(v), false, false);
    return bitsf(b.x) + bitsf(b.y);
}
__device__ __forceinline__ float fold32(float a, float b) {
    const v2u r = __builtin_amdgcn_permlane32_swap(fbits(a), fbits(b), false, false);
    return bitsf(r.x) + bitsf(r.y);
}
__device__ __forceinline__ float ex2(float v) { return __builtin_amdgcn_exp2f(v); }
__device__ __forceinline__ unsigned sane(unsigned v) { return v < 0xFFFFFFFEu ? v : 0xFFFFFFFEu; }

__device__ __forceinline__ int porder(int f, int g, int e) {
    return 64 * (f >> 1) + 16 * (f & 1) + 4 * g + 32 * ((e >> 1) & 1) + 2 * (e >> 2) + (e & 1);
}
constexpr int NU_HOP = SPP * NCHUNK * 16;
template <bool MIDBAR>
__device__ __forceinline__ void misc_wave(const Params& p, const int mw, const int l) {
    const int n = l & 15, g = l >> 4;
    v2u xl[4], xh[4];
#pragma unroll
    for (int k = 0; k < 4; ++k) {
        const int i = mw * 4 + k, rt = i / 14, f = i % 14;
        const u16* row = p.x + (size_t)(rt * 16 + n) * 448 + 64 * (f >> 1) + 16 * (f & 1) + 4 * g;
        xl[k] = ldg8(row); xh[k] = ldg8(row + 32);
    }
    unsigned qv[3][8];
    bool qok[3];
#pragma unroll
    for (int k = 0; k < 3; ++k) {
        const int i = mw * 3 + k;
        const bool isv = i >= 896;
        const int fr = isv ? i - 896 : i;
        const int unit = fr / 28, t = (fr % 28) / 14, f = fr % 14;
        const int d = isv ? 16 * t + n : 8 * (n >> 2) + 4 * t + (n & 3);
        const int col = (isv ? 896 + unit * 28 : (unit >> 4) * 448 + (unit & 15) * 28) + (d < 28 ? d : 0);
        qok[k] = d < 28;
#pragma unroll
        for (int e = 0; e < 8; ++e) qv[k][e] = p.wqkv[(size_t)porder(f, g, e) * 1344 + col];
    }
    unsigned wv[8];
#pragma unroll
    for (int e = 0; e < 8; ++e) {
        const int d = (e < 4) ? 4 * g + e : 16 + 4 * g + e - 4;
        wv[e] = p.wo[(size_t)((mw & 15) * 28 + (d < 28 ? d : 0)) * 448 + 16 * (mw >> 4) + n];
        if (d >= 28) wv[e] = 0u;
    }
    v4u gxv[7], ggv[7];
    const int row = (mw & 255) * 8 + (l >> 3), c0 = (l & 7) * 8;
#pragma unroll
    for (int k = 0; k < 7; ++k) { gxv[k] = ldg16(p.x + (size_t)row * 448 + k * 64 + c0); ggv[k] = ldg16(p.gate_w + k * 64 + c0); }
    for (int i = mw; i < NU_HOP; i += 448) {
        const int st = i / (NCHUNK * 16), ck = (i >> 4) % NCHUNK, kb = i & 15;
        v4u val = v4u{0xFFFFFFFFu, 0xFFFFFFFFu, 0xFFFFFFFFu, 0xFFFFFFFFu};
        if (st == 0 && ck < 6 && (kb & 7) < 7) {
            const int im = (kb & 7) * 64 + l;
            const v4u m = ldg16(p.mem0 + (size_t)(im & 7) * 448 + (im >> 3) * 8);
            val = v4u{sane(m.x), sane(m.y), sane(m.z), sane(m.w)};
        }
        stg16(p.ws + OFF_HOP + (size_t)hop_off(st, 0u) + ck * CH + kb * 1024 + l * 16, val);
    }
    if (MIDBAR) { __builtin_amdgcn_sched_barrier(0); __builtin_amdgcn_s_barrier(); __builtin_amdgcn_sched_barrier(0); }
    static_assert(SPP * 32 <= 448, "one KiB of the first page's S_xm slots per wave");
    if (mw < SPP * 32) stg16(p.ws + OFF_SXM + (size_t)mw * 1024 + l * 16, v4u{0xFFFFFFFFu, 0xFFFFFFFFu, 0xFFFFFFFFu, 0xFFFFFFFFu});
    if (mw < 336) stg16(p.ws + OFF_YM + (size_t)61 * 224 * 512 + (size_t)mw * 1024 + l * 16, v4u{0xFFFFFFFFu, 0xFFFFFFFFu, 0xFFFFFFFFu, 0xFFFFFFFFu});
#pragma unroll
    for (int k = 0; k < 4; ++k) stg16(p.ws + OFF_XP + (size_t)(mw * 4 + k) * 1024 + l * 16, v4u{xl[k].x, xh[k].x, xl[k].y, xh[k].y});
#pragma unroll
    for (int k = 0; k < 3; ++k) {
        const v4u o = v4u{qv[k][0] | (qv[k][1] << 16), qv[k][2] | (qv[k][3] << 16), qv[k][4] | (qv[k][5] << 16), qv[k][6] | (qv[k][7] << 16)};
        stg16(p.ws + OFF_QW + (size_t)(mw * 3 + k) * 1024 + l * 16, qok[k] ? o : v4u{0u, 0u, 0u, 0u});
    }
    stg16(p.ws + OFF_WOF + (size_t)mw * 1024 + l * 16, v4u{wv[0] | (wv[1] << 16), wv[2] | (wv[3] << 16), wv[4] | (wv[5] << 16), wv[6] | (wv[7] << 16)});
    float s = 0.f;
#pragma unroll
    for (int k = 0; k < 7; ++k)
#pragma unroll
        for (int j = 0; j < 4; ++j) {
            s += bitsf(gxv[k][j] << 16) * bitsf(ggv[k][j] << 16);
            s += bitsf(gxv[k][j] & 0xFFFF0000u) * bitsf(ggv[k][j] & 0xFFFF0000u);
        }
    s += __shfl_xor(s, 1); s += __shfl_xor(s, 2); s += __shfl_xor(s, 4);
    if ((l & 7) == 0 && mw < 256) ((float*)(p.ws + OFF_GATE))[row] = 1.0f / (1.0f + __expf(-s));
}

#define K1_AUX 16
#define MISC_MODE 0
#define K1_LAUX 0
#define K1_NORNT
__device__ __forceinline__ v4u ldr16(const void* q) { return ldg16(q); }
constexpr int K1_RS = 528, K1_OS = 144, K1_IMG = 36864, K1_IS = 136, K1_LDS = K1_IMG + 7 * 64 * K1_IS;
#define PT(k) do {} while (0)
#define PT0(k) do {} while (0)
template <int mat>
__device__ __forceinline__ void prep_heavy(const Params& p, LAS unsigned char* L, const int h, const int blk, const int w) {
    const int b = blockIdx.x, tid = threadIdx.x, l = tid & 63, n = l & 15, g = l >> 4;
    v4u t[4];
    if (mat == 0) {
#pragma unroll
        for (int k = 0; k < 4; ++k) { const int u = tid + 512 * k; t[k] = ldr16(p.Wq + ((size_t)(64 * blk + (u >> 5)) * 16 + h) * 256 + (u & 31) * 8); }
    } else {
#pragma unroll
        for (int k = 0; k < 4; ++k) { const int u = tid + 512 * k; t[k] = ldr16(p.Wo + ((size_t)h * 256 + (u >> 3)) * 448 + 64 * blk + (u & 7) * 8); }
    }
    __builtin_amdgcn_sched_barrier(0);
    const int rs_ = mat == 0 ? K1_RS : K1_OS, sh = mat == 0 ? 5 : 3;
    if (w == 7) {
#pragma unroll
        for (int k = 0; k < 4; ++k) { const int u = tid + 512 * k; *(LAS v4u*)(L + (u >> sh) * rs_ + (u & ((1 << sh) - 1)) * 16) = t[k]; }
        asm volatile("s_waitcnt lgkmcnt(0)" ::: "memory");
        misc_wave<true>(p, 224 + b, l);
        PT(3);
        return;
    }
    v4u cr[4][8];
    LAS unsigned char* img = L + K1_IMG + w * 64 * K1_IS;
    {
        const __amdgpu_buffer_rsrc_t wr = __builtin_amdgcn_make_buffer_rsrc((void*)(mat == 0 ? p.Wk : p.Wv), 0, 448 * 4096 * 2, 0x00020000);
        const int vo = ((64 * w + (l >> 5)) * 16 + h) * 512 + (l & 31) * 16;
#define K1_LOAD(tt) do { _Pragma("unroll") for (int j = 0; j < 8; ++j) \
            cr[tt][j] = __builtin_amdgcn_raw_buffer_load_b128(wr, vo, (16 * ((tt) >> 1) + 2 * ((tt) & 1) + 4 * (j >> 1) + 32 * (j & 1)) * 8192, K1_LAUX); \
            __builtin_amdgcn_sched_barrier(0); } while (0)
#define K1_TURN(tt) do { \
        _Pragma("unroll") for (int j = 0; j < 8; ++j) *(LAS v4u*)(img + (2 * j + (l >> 5)) * K1_RS + (l & 31) * 16) = cr[tt][j]; \
        asm volatile("" ::: "memory"); \
        _Pragma("unroll") for (int es = 0; es < 8; ++es) cr[tt][es] = *(const LAS v4u*)(img + n * K1_RS + es * 64 + g * 16); \
        asm volatile("" ::: "memory"); __builtin_amdgcn_sched_barrier(0); } while (0)
        K1_LOAD(0); K1_LOAD(1); K1_LOAD(2);
#pragma unroll
        for (int k = 0; k < 4; ++k) { const int u = tid + 512 * k; *(LAS v4u*)(L + (u >> sh) * rs_ + (u & ((1 << sh) - 1)) * 16) = t[k]; }
        __builtin_amdgcn_sched_barrier(0);
        K1_TURN(0);
        K1_LOAD(3);
        K1_TURN(1); K1_TURN(2); K1_TURN(3);
    }
    asm volatile("s_waitcnt lgkmcnt(0)" ::: "memory");
    __builtin_amdgcn_s_barrier();
    asm volatile("" ::: "memory");
    PT0(1);
    constexpr float SC = mat == 0 ? LOG2E / 16.0f : 1.0f;
    const __amdgpu_buffer_rsrc_t ors = __builtin_amdgcn_make_buffer_rsrc(p.ws, 0, (int)WS_END, 0x00020000);
#pragma unroll
    for (int y = 0; y < 4; ++y) {
        v4u bfr[8];
#pragma unroll
        for (int es = 0; es < 8; ++es) {
            if constexpr (mat == 0) {
                bfr[es] = *(const LAS v4u*)(L + (16 * (y >> 1) + 2 * (y & 1) + 4 * (n >> 2) + 32 * ((n >> 1) & 1) + (n & 1)) * K1_RS + es * 64 + g * 16);
            } else {
                const LAS unsigned char* rp = L + (32 * es + 8 * g + (n >> 2)) * K1_OS + y * 32 + (l & 3) * 8;
                const v2u lo = tr_read(rp), hi = tr_read(rp + 4 * K1_OS);
                bfr[es] = v4u{lo.x, lo.y, hi.x, hi.y};
            }
        }
        f32x4 acc[4];
#pragma unroll
        for (int x = 0; x < 4; ++x) acc[x] = f32x4{0.f, 0.f, 0.f, 0.f};
#pragma unroll
        for (int es = 0; es < 8; ++es)
#pragma unroll
            for (int x = 0; x < 4; ++x) acc[x] = mfma32(cr[x][es], bfr[es], acc[x]);
        const int d = mat == 0 ? (int)OFF_TW + ((h * 28 + 4 * blk + y) * 7 + w) * 2048 : (int)OFF_YW + (((4 * blk + y) * 16 + h) * 7 + w) * 2048;
#pragma unroll
        for (int a = 0; a < 2; ++a) {
            const f32x4 lo = acc[2 * a] * SC, hi = acc[2 * a + 1] * SC;
            stc16(v4u{pack2(lo[0], lo[1]), pack2(lo[2], lo[3]), pack2(hi[0], hi[1]), pack2(hi[2], hi[3])}, ors, l * 16, d + a * 1024);
        }
        if (y == 1) __builtin_amdgcn_sched_barrier(0);
    }
    PT0(2);
    PT(3);
}

__global__ void __launch_bounds__(512, 2) k_prep(Params p) {
    extern __shared__ __attribute__((aligned(16))) unsigned char lds_[];
    LAS unsigned char* L = (LAS unsigned char*)lds_;
    p.ws = align_ws(p.ws);
    const int b = blockIdx.x, tid = threadIdx.x;
    const int l = tid & 63, w = __builtin_amdgcn_readfirstlane(tid >> 6);
    PT0(0);
    if (b >= 224) {
        const int k = b - 224;
        if ((k & 7) == 0 && w < 2) {
            const int pg = (k >> 3) + 4 * w;
            if (pg < NPAGE && l == 0) {
                GAS unsigned* a = (GAS unsigned*)(p.ws + OFF_HOP + (size_t)pg * PAGE + 126 * CH);
                unsigned best[4] = {~0u, ~0u, ~0u, ~0u};
                for (int it = 0; it < 3; ++it) {
#pragma unroll
                    for (int q = 0; q < 4; ++q) {
                        const unsigned long long t0 = __builtin_amdgcn_s_memtime();
                        __hip_atomic_store(a + q * 1024, 0xFFFFFFFFu, __ATOMIC_RELAXED, __HIP_MEMORY_SCOPE_AGENT);
                        asm volatile("s_waitcnt vmcnt(0)" ::: "memory");
                        const unsigned dt = (unsigned)(__builtin_amdgcn_s_memtime() - t0);
                        best[q] = min(best[q], dt);
                    }
                }
                ((unsigned*)(p.ws + OFF_CTL))[pg] = ((best[0] + best[1] > best[2] + best[3]) ? 1u : 0u) ^ ((xcc_id() >> 2) & 1u);
            }
            PT(3);
            if (pg == 7) {
                if (l == 0) ((unsigned*)(p.ws + OFF_CTL))[8] = xcc_id() & 7u;
                for (int i = l; i < (int)(CTL_BYTES - 256) / 16; i += 64) stg16(p.ws + OFF_CTL + 256 + i * 16, v4u{0u, 0u, 0u, 0u});
            }
            return;
        }
        if ((k & 7) == 0) return;
        const int kk = k - (k >> 3) - 1;
        misc_wave<false>(p, kk * 8 + w, l);
        PT(3);
        return;
    }
    const int r = b >> 3, h = (b & 7) + 8 * (r & 1), q = r >> 1;
    if (q < 7) prep_heavy<0>(p, L, h, q, w); else prep_heavy<1>(p, L, h, q - 7, w);
}

constexpr int L_MST = 0, L_TIM = L_MST + 2 * 7168, L_RED = L_TIM + 8192, L_REC_END = 96 * 1024;
#define DLY_M 1024
#define DLY_P 0
#define DLY_PO 0
#define PPOSY 8
#define PRIO_X 1
#define PRIO_L 1
#define DLY_S 100
#define DLY_SO 40
#define GAP_M 150
#define DLY_G 300
#define DLY_3 130
#define DLY_4 60
#define DLY_V 240
#define DLY_Y 0
#define PPOS 8
#define DLY_Q 200
#define SPA 0
#define SPB 2
#define SW_DLY 60
#define STAG 1
__device__ __forceinline__ void delay_var(int n) {
    asm volatile("s_cmp_lt_i32 %0, 1\n\ts_cbranch_scc1 2f\n1:\n\ts_nop 5\n\ts_sub_i32 %0, %0, 1\n\ts_cmp_gt_i32 %0, 0\n\ts_cbranch_scc1 1b\n2:" : "+s"(n) :: "scc", "memory");
}
template <int C> __device__ __forceinline__ void delay_cycles() {
    if constexpr (C >= 64) __builtin_amdgcn_s_sleep(C / 64 > 127 ? 127 : C / 64);
    if constexpr ((C & 63) >= 4) asm volatile("s_nop %0" :: "i"(((C & 63) >> 2) - 1));
}

__device__ __forceinline__ bool fresh(v4u x) {
    const unsigned m = max(max(x.x, x.y), max(x.z, x.w));
    return __all(m != 0xFFFFFFFFu);
}
#define BAR() do { asm volatile("s_waitcnt lgkmcnt(0)" ::: "memory"); __builtin_amdgcn_s_barrier(); asm volatile("" ::: "memory"); } while (0)
#define FENCE() do { asm volatile("" ::: "memory"); __builtin_amdgcn_sched_barrier(0); } while (0)

#define STAMP(k) do {} while (0)
#define PROF_DECL
#define PROF_ACC do {} while (0)
#define PROF_OUT(slot) do {} while (0)
typedef _Float16 f16x2 __attribute__((ext_vector_type(2)));
__device__ __forceinline__ unsigned packh(float a, float b) { return __builtin_bit_cast(unsigned, __builtin_amdgcn_cvt_pkrtz(a, b)); }
__device__ __forceinline__ float hlo(unsigned v) { const f16x2 h = __builtin_bit_cast(f16x2, v); return (float)h.x; }
__device__ __forceinline__ float hhi(unsigned v) { const f16x2 h = __builtin_bit_cast(f16x2, v); return (float)h.y; }
constexpr unsigned SPIN_CAP = 1u << 20;

__device__ __forceinline__ unsigned pkmaxh(unsigned a, unsigned b) { unsigned r; asm("v_pk_max_f16 %0, %1, %2" : "=v"(r) : "v"(a), "v"(b)); return r; }
__device__ __forceinline__ float vmax(float a, float b) { float r; asm("v_max_f32 %0, %1, %2" : "=v"(r) : "v"(a), "v"(b)); return r; }
__device__ __forceinline__ float xg_max_raw(float v) {
    const v2u a = __builtin_amdgcn_permlane16_swap(fbits(v), fbits(v), false, false);
    v = vmax(bitsf(a.x), bitsf(a.y));
    const v2u b = __builtin_amdgcn_permlane32_swap(fbits(v), fbits(v), false, false);
    return vmax(bitsf(b.x), bitsf(b.y));
}
__device__ __forceinline__ float addh_lo(unsigned a, unsigned b) { float r; asm("v_fma_mix_f32 %0, %1, 1.0, %2 op_sel_hi:[1,0,1]" : "=v"(r) : "v"(a), "v"(b)); return r; }
__device__ __forceinline__ float addh_hi(unsigned a, unsigned b) { float r; asm("v_fma_mix_f32 %0, %1, 1.0, %2 op_sel:[1,0,1] op_sel_hi:[1,0,1]" : "=v"(r) : "v"(a), "v"(b)); return r; }
__device__ __forceinline__ float subh_lo(unsigned a, float b) { float r; asm("v_fma_mix_f32 %0, %1, 1.0, -%2 op_sel_hi:[1,0,0]" : "=v"(r) : "v"(a), "v"(b)); return r; }
__device__ __forceinline__ float subh_hi(unsigned a, float b) { float r; asm("v_fma_mix_f32 %0, %1, 1.0, -%2 op_sel:[1,0,0] op_sel_hi:[1,0,0]" : "=v"(r) : "v"(a), "v"(b)); return r; }
__device__ __forceinline__ float add_ror8(float a) { float r; asm("s_nop 1\n\tv_add_f32_dpp %0, %1, %1 row_ror:8 row_mask:0xf bank_mask:0xf" : "=v"(r) : "v"(a)); return r; }
#define UNLIKELY(x) __builtin_expect(!!(x), 0)
#define CNT(x) do {} while (0)

#define TLS(k) do {} while (0)
#define TLW(k) do {} while (0)
template <int NG, int STR>
__device__ __forceinline__ int wait_ready(const __amdgpu_buffer_rsrc_t rs, const int g0, const int need, unsigned& mspin) {
    const int l = threadIdx.x & 63;
    int ready;
    for (;;) {
        asm volatile("" ::: "memory");
        const int v = (int)__builtin_amdgcn_raw_buffer_load_b32(rs, CT_PROG + (g0 * 3 + (l < 3 * NG ? l : 0)) * 128, 0, 16);
        ready = 1 << 20;
#pragma unroll
        for (int k = 0; k < 3 * NG; ++k) if (k % 3 < STR) ready = min(ready, STR * __builtin_amdgcn_readlane(v, k) + (k % 3));
        if (ready > need || ++mspin >= SPIN_CAP) break;
        __builtin_amdgcn_s_sleep(2);
    }
    asm volatile("" ::: "memory");
    return ready;
}

template <int ROLE, int DLY1>
__device__ __forceinline__ void score_wave(const Params& p, LAS unsigned char* L, const int u, const int w) {
    constexpr bool SPECIAL = ROLE == 2;
    const int l = threadIdx.x & 63;
    const int n = l & 15, g = l >> 4, i8 = l & 7, hh = (l >> 3) & 1;
    const int half = u & 1;
    const __amdgpu_buffer_rsrc_t rs = __builtin_amdgcn_make_buffer_rsrc(p.ws, 0, (int)WS_END, 0x00020000);
    const int idx = hh ? 0xEEEE : 0x4444;
    const unsigned parmask = load_parmask(p.ws);
    v8u tw[SPECIAL ? 1 : 14];
    v4u gx[SPECIAL ? 14 : 1];
    const LAS unsigned char* mrd[SPECIAL ? 1 : 7];
    if constexpr (!SPECIAL) {
        const unsigned char* src = p.ws + OFF_TW + ((size_t)(u * 14 + 2 * w) * 7) * 2048 + l * 16;
#pragma unroll
        for (int k = 0; k < 7; ++k) {
            const int f = w + k < 7 ? w + k : w + k - 7;
            mrd[k] = L + L_MST + f * 1024 + l * 16;
#pragma unroll
            for (int t = 0; t < 2; ++t) {
                const v4u a = ldg16(src + (7 * t + f) * 2048), b = ldg16(src + (7 * t + f) * 2048 + 1024);
                tw[7 * t + k] = v8u{a.x, a.y, a.z, a.w, b.x, b.y, b.z, b.w};
            }
        }
    } else {
        *(LAS v4u*)(L + L_TIM + (half ? 0 : 14 * 512) + l * 16) = v4u{0u, 0u, 0u, 0u};
        __builtin_amdgcn_s_setprio(2);
    }
    const int moff = C_M + w * 1024 + hh * 512 + g * 128 + i8 * 16;
    const int sst_off = C_S + hh * 4096 + u * 128 + (4 * (g & 1) + 2 * (g >> 1) + (l & 1)) * 16 + (i8 >> 1) * 4;
    const unsigned ssel = (l & 1) ? 0x03020706u : 0x05040100u;
    const int pst_off = C_P + u * 256 + (4 * (g & 1) + 2 * (g >> 1) + (l & 1)) * 32 + (n >> 1) * 4;
    const int goff = ((u & ~1) + ((n >> 2) & 1)) * 14336 + (8 * half + 4 * (n >> 3) + (n & 3) + 16 * g) * 16;
    const int xoff = (int)OFF_XP + 7 * half * 1024 + l * 16;
    unsigned spins = 0, mspin = 0;
    int ready = 0;
    if constexpr (!SPECIAL) {
#pragma unroll
        for (int k = 0; k < 7; ++k) asm volatile("" : "+v"(mrd[k]));
    }
    v4u xq[14], x3 = v4u{0u, 0u, 0u, 0u};
    if constexpr (ROLE == 1) {
#pragma unroll
        for (int k = 0; k < 14; ++k) xq[k] = __builtin_amdgcn_raw_buffer_load_b128(rs, xoff + ((k / 7) * 14 + k % 7) * 1024, (NSEG - 1) * 28672, 0);
    }
    if constexpr (SPECIAL) {
        ready = wait_ready<2, 2>(rs, u & ~1, 0, mspin);
        TLW(1);
#pragma unroll
        for (int f = 0; f < 14; ++f) gx[f] = __builtin_amdgcn_raw_buffer_load_b128(rs, goff + f * 1024, (int)OFF_GF, 16);
    }
    PROF_DECL
    for (int s = 0; s < NSEG; ++s) {
        const int ho = hop_off(s, parmask);
        FENCE();
        STAMP(0);
        if constexpr (!SPECIAL) {
            if (s) { delay_cycles<DLY_M>(); delay_var(DM0); }
            FENCE();
            v4u xs = __builtin_amdgcn_raw_buffer_load_b128(rs, moff, ho, 16);
            FENCE(); delay_cycles<GAP_M>(); FENCE();
            const v4u x1 = __builtin_amdgcn_raw_buffer_load_b128(rs, moff, ho + CH, 16);
            FENCE(); delay_cycles<GAP_M>(); FENCE();
            const v4u x2 = __builtin_amdgcn_raw_buffer_load_b128(rs, moff, ho + 2 * CH, 16);
            FENCE();
#define MOV4(d_, s_) do { asm volatile("v_mov_b64 %0, %1" : "=v"(d_.lo) : "v"(s_.lo)); asm volatile("v_mov_b64 %0, %1" : "=v"(d_.hi) : "v"(s_.hi)); } while (0)
            if (UNLIKELY(!fresh(xs))) {
                FENCE();
                x3 = __builtin_amdgcn_raw_buffer_load_b128(rs, moff, ho + 3 * CH, 16);
                FENCE();
                CNT(1);
                MOV4(xs, x1);
                if (!fresh(xs)) {
                    CNT(1);
                    MOV4(xs, x2);
                    if (!fresh(xs)) {
                        CNT(1);
                        MOV4(xs, x3);
                        if (!fresh(xs)) {
                            for (;;) {
                                asm volatile("" ::: "memory");
                                xs = __builtin_amdgcn_raw_buffer_load_b128(rs, moff, ho + 3 * CH, 16);
                                CNT(1);
                                if (fresh(xs) || ++mspin >= SPIN_CAP) break;
                            }
                        }
                    }
                }
            }
            STAMP(1);
            *(LAS v4u*)mrd[0] = xs;
            FENCE();
            f32x4 a0 = {0.f, 0.f, 0.f, 0.f}, a1 = a0;
            a0 = smfmac(xs, tw[0], a0, idx); a1 = smfmac(xs, tw[7], a1, idx);
            asm volatile("" : "+v"(a0), "+v"(a1));
            FENCE();
            BAR();
            STAMP(2);
            v4u mp[7];
            delay_cycles<DLY1>();
            FENCE();
#pragma unroll
            for (int k = 1; k < 7; ++k) mp[k] = *(const LAS v4u*)mrd[k];
            FENCE();
#pragma unroll
            for (int k = 1; k < 7; ++k) { a0 = smfmac(mp[k], tw[k], a0, idx); a1 = smfmac(mp[k], tw[7 + k], a1, idx); }
            *(LAS v2u*)(L + L_TIM + (2 * w + 2 * half) * 512 + n * 32 + (g ^ (n >> 2)) * 8) = v2u{pack2(a0[0], a0[1]), pack2(a0[2], a0[3])};
            *(LAS v2u*)(L + L_TIM + (2 * w + 2 * half + 1) * 512 + n * 32 + (g ^ (n >> 2)) * 8) = v2u{pack2(a1[0], a1[1]), pack2(a1[2], a1[3])};
            STAMP(3);
            BAR();
            STAMP(4);
            asm volatile("" : "+v"(x3) : "v"(x1), "v"(x2));
            TLS(8 + s);
            if (ROLE == 1 && s == NSEG - 1) {
                v4u ta[7];
#pragma unroll
                for (int k = 0; k < 7; ++k) {
                    const v2u t0 = tr_read(L + L_TIM + (2 * k + 2 * half) * 512 + (l ^ g) * 8), t1 = tr_read(L + L_TIM + (2 * k + 2 * half + 1) * 512 + (l ^ g) * 8);
                    ta[k] = v4u{t0.x, t0.y, t1.x, t1.y};
                }
                FENCE();
                f32x4 d0 = {0.f, 0.f, 0.f, 0.f}, d1 = d0;
#pragma unroll
                for (int k = 0; k < 7; ++k) { d0 = mfma32(ta[k], xq[k], d0); d1 = mfma32(ta[k], xq[7 + k], d1); }
                stc16(v4u{sane(fbits(fold32(d0[0], d0[2]))), sane(fbits(fold32(d0[1], d0[3]))),
                                                           sane(fbits(fold32(d1[0], d1[2]))), sane(fbits(fold32(d1[1], d1[3])))},
                                                       rs, l * 16, (int)OFF_SXM + (s * 32 + u) * 1024);
                FENCE();
            }
        } else {
            f32x4 ae = {0.f, 0.f, 0.f, 0.f};
            asm volatile("" : "+v"(ae));
            BAR();
            STAMP(2);
            v4u mp[7];
#pragma unroll
            for (int f = 0; f < 7; ++f) mp[f] = *(const LAS v4u*)(L + L_MST + f * 1024 + l * 16);
            FENCE();
            v4u m2[4];
#pragma unroll
            for (int f = 0; f < 7; ++f) {
                const v4u ga = gx[2 * f], gb = gx[2 * f + 1];
                ae = smfmac(mp[f], v8u{ga.x, ga.y, ga.z, ga.w, gb.x, gb.y, gb.z, gb.w}, ae, idx);
            }
            const unsigned px = packh(fold32(ae[0], ae[2]), fold32(ae[1], ae[3]));
            const unsigned pv = sane(__builtin_amdgcn_perm(__builtin_amdgcn_update_dpp(0u, px, 0xB1, 0xf, 0xf, true), px, ssel));
            __builtin_amdgcn_raw_buffer_store_b32(pv, rs, pst_off, ho, 16);
            __builtin_amdgcn_raw_buffer_store_b32(pv, rs, pst_off, ho + CH, 16);
            FENCE();
#pragma unroll
            for (int b = 0; b < 4; ++b) m2[b] = *(const LAS v4u*)(L + L_MST + (3 * half + b) * 1024 + l * 16);
            FENCE();
            STAMP(3);
            f32x4 sa = {0.f, 0.f, 0.f, 0.f};
            asm volatile("" : "+v"(sa));
            BAR();
            STAMP(4);
            v8u tb[4];
#pragma unroll
            for (int b = 0; b < 4; ++b) {
                const v2u t0 = tr_read(L + L_TIM + (4 * b) * 512 + (l ^ g) * 8), t1 = tr_read(L + L_TIM + (4 * b + 1) * 512 + (l ^ g) * 8),
                          t2 = tr_read(L + L_TIM + (4 * b + 2) * 512 + (l ^ g) * 8), t3 = tr_read(L + L_TIM + (4 * b + 3) * 512 + (l ^ g) * 8);
                tb[b] = v8u{t0.x, t0.y, t1.x, t1.y, t2.x, t2.y, t3.x, t3.y};
            }
            FENCE();
#pragma unroll
            for (int b = 0; b < 4; ++b) sa = smfmac(m2[b], tb[b], sa, idx);
            const unsigned pk = packh(add_ror8(fold32(sa[0], sa[2])), add_ror8(fold32(sa[1], sa[3])));
            const unsigned nb = __builtin_amdgcn_update_dpp(0u, pk, 0xB1, 0xf, 0xf, true);
            __builtin_amdgcn_raw_buffer_store_b32(sane(__builtin_amdgcn_perm(nb, pk, ssel)), rs, sst_off, ho, 16);
            FENCE();
            STAMP(5);
            delay_cycles<DLY_G>();
            if (UNLIKELY(ready <= s + 1 && s + 1 < NSEG)) ready = wait_ready<2, 2>(rs, u & ~1, s + 1, mspin);
            FENCE();
#pragma unroll
            for (int f = 0; f < 14; ++f) gx[f] = __builtin_amdgcn_raw_buffer_load_b128(rs, goff + f * 1024, (int)(OFF_GF + (s + 1 < NSEG ? s + 1 : s) * SEGB), 16);
            FENCE();
        }
        PROF_ACC;
    }
    PROF_OUT(u * 8 + w);
}

template <bool LAST, bool YOUNG>
__device__ __forceinline__ void value_wave(const Params& p, LAS unsigned char* L, const int v, const int w) {
    const int l = threadIdx.x & 63;
    const int g = l >> 4, i8 = l & 7, hh = (l >> 3) & 1;
    const __amdgpu_buffer_rsrc_t rs = __builtin_amdgcn_make_buffer_rsrc(p.ws, 0, (int)WS_END, 0x00020000);
    const int idx = hh ? 0xEEEE : 0x4444;
    const unsigned parmask = load_parmask(p.ws);
    v8u yw[14];
    const LAS unsigned char* mrd[7];
    {
        const unsigned char* src = p.ws + OFF_YW + (size_t)(v * 8 + w) * 14 * 2048 + l * 16;
#pragma unroll
        for (int k = 0; k < 7; ++k) {
            const int f = w + k < 7 ? w + k : w + k - 7;
            mrd[k] = L + L_MST + f * 1024 + l * 16;
#pragma unroll
            for (int t = 0; t < 2; ++t) {
                const v4u a = ldg16(src + (7 * t + f) * 2048), b = ldg16(src + (7 * t + f) * 2048 + 1024);
                yw[7 * t + k] = v8u{a.x, a.y, a.z, a.w, b.x, b.y, b.z, b.w};
            }
        }
    }
    if (LAST && PRIO_L) __builtin_amdgcn_s_setprio(2);
    if (YOUNG && PRIO_X == 3) __builtin_amdgcn_s_setprio(3);
    const int moff = C_M2 + w * 1024 + hh * 512 + g * 128 + i8 * 16;
    const int mst_off = C_M + v * 256 + ((l >> 2) & 1) * 128 + (l >> 3) * 16 + (l & 3) * 4;
    const int soff = C_S + ((2 * w + hh) * 2 + (g >> 1)) * 128 + i8 * 16 + (g & 1) * 8;
    const int poff = C_P + ((2 * w + hh) * 2 + (g >> 1)) * 256 + i8 * 32 + (g & 1) * 16;
    const int yst_off = (int)OFF_YM + (v * 8 + w) * 512 + l * 8;
    const int uoff = (v * 8 + w) * 2048 + l * 16;
    unsigned mlo = hh ? 0u : 0xFFFFFFFFu, mhi = ~mlo;
    v4u ones = v4u{0x3F803F80u, 0x3F803F80u, 0x3F803F80u, 0x3F803F80u};
    asm volatile("" : "+v"(mlo), "+v"(mhi), "+v"(ones));
#pragma unroll
    for (int k = 0; k < 7; ++k) asm volatile("" : "+v"(mrd[k]));
    unsigned spins = 0, mspin = 0;
    PROF_DECL
    int ho = hop_off(0, parmask);
    int ready = wait_ready<1, 3>(rs, 32 + v, 0, mspin);
    TLS(1);
    v4u ua = __builtin_amdgcn_raw_buffer_load_b128(rs, uoff, (int)OFF_UF, 16);
#define V_Y_PHASE(MIDA, MIDB) \
        f32x4 y0 = {0.f, 0.f, 0.f, 0.f}, y1 = y0; \
        if constexpr (!LAST) { \
            delay_cycles<DLY_V>(); \
            FENCE(); \
            v4u x = __builtin_amdgcn_raw_buffer_load_b128(rs, moff, ho, 16); \
            if (UNLIKELY(!fresh(x))) { \
                for (;;) { asm volatile("" ::: "memory"); x = __builtin_amdgcn_raw_buffer_load_b128(rs, moff, ho, 16); CNT(1); if (fresh(x) || ++mspin >= SPIN_CAP) break; } \
            } \
            STAMP(1); \
            *(LAS v4u*)mrd[0] = x; \
            FENCE(); \
            y0 = smfmac(x, yw[0], y0, idx); y1 = smfmac(x, yw[7], y1, idx); \
            asm volatile("" : "+v"(y0), "+v"(y1)); \
            FENCE(); \
        } \
        BAR(); \
        STAMP(2); \
        v4u mp[7]; \
        if constexpr (YOUNG) delay_cycles<DLY_Y>(); \
        FENCE(); \
        _Pragma("unroll") for (int k = LAST ? 0 : 1; k < 7; ++k) mp[k] = *(const LAS v4u*)mrd[k]; \
        FENCE(); \
        MIDA; \
        _Pragma("unroll") for (int k = LAST ? 0 : 1; k < 7; ++k) { \
            if (k == (YOUNG ? PPOSY : PPOS)) { MIDB; } \
            y0 = smfmac(mp[k], yw[k], y0, idx); y1 = smfmac(mp[k], yw[7 + k], y1, idx); \
        } \
        if ((YOUNG ? PPOSY : PPOS) == 7) { MIDB; } \
        v2u yop = v2u{pack2(fold32(y0[0], y0[2]), fold32(y0[1], y0[3])), pack2(fold32(y1[0], y1[2]), fold32(y1[1], y1[3]))}; \
        asm volatile("" : "+v"(yop)); \
        FENCE(); \
        STAMP(3)
    for (int s = 0; s < NSEG - 1; ++s) {
        const int hn = hop_off(s + 1, parmask);
        const int uo = (int)(OFF_UF + (s + 1) * SEGB);
        if (UNLIKELY(ready <= s + 1)) ready = wait_ready<1, 3>(rs, 32 + v, s + 1, mspin);
        if constexpr (LAST) {
            if (UNLIKELY((s + 2) % SPP == 0)) {
                const int sh = 4 * ((s + 2) / SPP - 1);
                for (;;) {
                    asm volatile("" ::: "memory");
                    const unsigned c = __builtin_amdgcn_readfirstlane(__builtin_amdgcn_raw_buffer_load_b32(rs, CT_PZ, 0, 16));
                    if (((c >> sh) & 15u) >= 6u || ++mspin >= SPIN_CAP) break;
                }
            }
        }
        int ho1 = ho + CH, ho2 = ho + 4096, hn1 = hn + CH, hn2 = hn + 2 * CH, hn3 = hn + 3 * CH, hnn = hn;
        asm volatile("" : "+s"(ho1), "+s"(ho2), "+s"(hn1), "+s"(hn2), "+s"(hn3), "+s"(hnn));
        FENCE();
        STAMP(0);
        v4u ub, pv;
        V_Y_PHASE(ub = __builtin_amdgcn_raw_buffer_load_b128(rs, uoff + 1024, uo - (int)SEGB, 16); FENCE(),
                  FENCE(); pv = __builtin_amdgcn_raw_buffer_load_b128(rs, poff, ho, 16); FENCE());
        if ((YOUNG ? PPOSY : PPOS) == 8) { delay_cycles<YOUNG ? DLY_P : DLY_P + DLY_PO>(); FENCE(); pv = __builtin_amdgcn_raw_buffer_load_b128(rs, poff, ho, 16); FENCE(); }
        delay_cycles<DLY_Q>();
        FENCE();
        const v4u pq = __builtin_amdgcn_raw_buffer_load_b128(rs, poff, ho1, 16);
        FENCE();
        v2u sv, sw2 = v2u{0xFFFFFFFFu, 0xFFFFFFFFu};
#define S_A() do { FENCE(); STAMP(6); sv = __builtin_amdgcn_raw_buffer_load_b64(rs, soff, ho, 16); FENCE(); } while (0)
#define S_B() do { FENCE(); sw2 = __builtin_amdgcn_raw_buffer_load_b64(rs, soff, ho2, 16); FENCE(); } while (0)
        if (SPA == -1) { delay_cycles<YOUNG ? DLY_S : DLY_S + DLY_SO>(); S_A(); }
        if (UNLIKELY(!fresh(pv))) {
            CNT(1 << 20);
            asm volatile("v_mov_b64 %0, %1" : "=v"(pv.lo) : "v"(pq.lo)); asm volatile("v_mov_b64 %0, %1" : "=v"(pv.hi) : "v"(pq.hi));
            if (!fresh(pv)) {
                for (;;) {
                    asm volatile("" ::: "memory");
                    pv = __builtin_amdgcn_raw_buffer_load_b128(rs, poff, ho1, 16);
                    CNT(1 << 20);
                    if (fresh(pv) || ++mspin >= SPIN_CAP) break;
                }
            }
        }
        STAMP(7);
        if (SPA == 0) S_A();
        if (SPB == 0) S_B();
        if (PRIO_X == 1 && YOUNG) __builtin_amdgcn_s_setprio(3);
        const unsigned hm = pkmaxh(pkmaxh(pv.x, pv.y), pkmaxh(pv.z, pv.w));
        const float mx = xg_max_raw(vmax(hlo(hm), hhi(hm)));
        if (SPA == 1) S_A();
        if (SPB == 1) S_B();
        const unsigned e0 = pack2(ex2(subh_lo(pv.x, mx)), ex2(subh_hi(pv.x, mx))), e1 = pack2(ex2(subh_lo(pv.y, mx)), ex2(subh_hi(pv.y, mx)));
        if (SPA == 2) S_A();
        if (SPB == 2) S_B();
        if (PRIO_X == 1 && YOUNG) __builtin_amdgcn_s_setprio(0);
        if (PRIO_X == 2 && YOUNG) __builtin_amdgcn_s_setprio(3);
        const unsigned e2 = pack2(ex2(subh_lo(pv.z, mx)), ex2(subh_hi(pv.z, mx))), e3 = pack2(ex2(subh_lo(pv.w, mx)), ex2(subh_hi(pv.w, mx)));
        const f32x4 z4 = {0.f, 0.f, 0.f, 0.f};
        const v4u eop = v4u{e0, e1, e2, e3};
        const f32x4 oa = mfma32(ua, eop, z4);
        const f32x4 ob = mfma32(ub, eop, z4);
        const f32x4 lx4 = mfma32(ones, eop, z4);
        if (SPB == 3) S_B();
        FENCE();
        STAMP(8);
        if (UNLIKELY(!__all(max(sv.x, sv.y) != 0xFFFFFFFFu))) {
            CNT(1 << 10);
            asm volatile("v_mov_b64 %0, %1" : "=v"(sv) : "v"(sw2));
            if (!__all(max(sv.x, sv.y) != 0xFFFFFFFFu)) {
                for (;;) {
                    asm volatile("" ::: "memory");
                    sv = __builtin_amdgcn_raw_buffer_load_b64(rs, soff, ho2, 16);
                    CNT(1 << 10);
                    if (__all(max(sv.x, sv.y) != 0xFFFFFFFFu) || ++mspin >= SPIN_CAP) break;
                }
            }
        }
        STAMP(9);
        const v2u sw = __builtin_amdgcn_permlane32_swap(sv.x, sv.y, false, false);
        const float t0 = addh_lo(sw.x, sw.y), t1 = addh_hi(sw.x, sw.y);
        float m;
        asm("v_max3_f32 %0, %1, %2, %3" : "=v"(m) : "v"(t0), "v"(t1), "v"(mx));
        f32x4 ox = hh ? ob : oa;
        m = xg_max_raw(m);
        const unsigned pk = pack2(ex2(t0 - m), ex2(t1 - m));
        const float beta = ex2(mx - m);
        const v2u bop = v2u{pk & mlo, pk & mhi};
        f32x4 lx = lx4;
        lx[0] *= beta;
        ox *= beta;
        FENCE();
        const f32x4 ls = mfma16(v2u{ones.x, ones.y}, bop, lx);
        f32x4 d = mfma16(yop, bop, ox);
        FENCE();
        const float r = __builtin_amdgcn_rcpf(ls[0]);
        d *= r;
        float r0, r1;
        asm("s_nop 1\n\tv_add_f32_dpp %0, %2, %2 row_ror:8 row_mask:0xf bank_mask:0x3\n\tv_add_f32_dpp %1, %3, %3 row_ror:8 row_mask:0xf bank_mask:0x3\n\t"
            "v_add_f32_dpp %0, %4, %4 row_ror:8 row_mask:0xf bank_mask:0xc\n\tv_add_f32_dpp %1, %5, %5 row_ror:8 row_mask:0xf bank_mask:0xc"
            : "=&v"(r0), "=&v"(r1) : "v"(d[0]), "v"(d[1]), "v"(d[2]), "v"(d[3]));
        *(LAS f32x2*)(L + L_RED + w * 512 + i8 * 64 + ((g ^ (i8 >> 1)) * 2 + hh) * 8) = f32x2{r0, r1};
        if (PRIO_X == 2 && YOUNG) __builtin_amdgcn_s_setprio(0);
        STAMP(10);
        FENCE();
        if constexpr (!LAST) ua = __builtin_amdgcn_raw_buffer_load_b128(rs, uoff, uo, 16);
        FENCE();
        BAR();
        STAMP(11);
        if constexpr (LAST) {
            f32x2 q[8];
#pragma unroll
            for (int k = 0; k < 8; ++k) q[k] = *(const LAS f32x2*)(L + L_RED + k * 512 + (l ^ (2 * g)) * 8);
            FENCE();
            const f32x2 tot = ((q[0] + q[1]) + (q[2] + q[3])) + ((q[4] + q[5]) + (q[6] + q[7]));
            const unsigned mv = sane(pack2(tot[0], tot[1]));
            __builtin_amdgcn_raw_buffer_store_b32(mv, rs, mst_off, hn3, 16);
            __builtin_amdgcn_raw_buffer_store_b32(mv, rs, mst_off, hn2, 16);
            __builtin_amdgcn_raw_buffer_store_b32(mv, rs, mst_off, hn1, 16);
            __builtin_amdgcn_raw_buffer_store_b32(mv, rs, mst_off, hnn, 16);
            __builtin_amdgcn_raw_buffer_store_b32(mv, rs, mst_off, hn + C_M2, 16);
            __builtin_amdgcn_raw_buffer_store_b32(mv, rs, mst_off, hn + C_M3, 16);
            FENCE();
            asm volatile("" :: "v"(sw2), "v"(pq));
            ua = __builtin_amdgcn_raw_buffer_load_b128(rs, uoff, uo, 16);
        } else {
            delay_cycles<256>();
            asm volatile("" :: "v"(sw2), "v"(pq));
        }
        __builtin_amdgcn_raw_buffer_store_b64(v2u{sane(yop.x), sane(yop.y)}, rs, yst_off, s * 224 * 512, 16);
        ho = hn;
        if constexpr (LAST) PROF_ACC;
    }
    {
        const int s = NSEG - 1;
        V_Y_PHASE(, );
        __builtin_amdgcn_raw_buffer_store_b64(v2u{sane(yop.x), sane(yop.y)}, rs, yst_off, s * 224 * 512, 16);
    }
    PROF_OUT(256 + v * 8 + w);
}

__device__ __forceinline__ void glds16(const void* gsrc, unsigned lds_dst) {
    unsigned keep;
    asm volatile("s_mov_b32 %0, m0\n\ts_mov_b32 m0, %2\n\ts_nop 0\n\tglobal_load_lds_dwordx4 %1, off\n\ts_mov_b32 m0, %0"
                 : "=&s"(keep) : "v"(gsrc), "s"(lds_dst) : "memory");
}
__device__ __forceinline__ v4u ldc16(const __amdgpu_buffer_rsrc_t rs, int voff, int soff) { return __builtin_amdgcn_raw_buffer_load_b128(rs, voff, soff, 16); }
__device__ __forceinline__ v4u pack8(f32x4 a, f32x4 b) { return v4u{pack2(a[0], a[1]), pack2(a[2], a[3]), pack2(b[0], b[1]), pack2(b[2], b[3])}; }
constexpr int L_XB = 0, XBUF = 28672;

template <bool GTYPE>
__device__ __forceinline__ void sweep(const Params& p, LAS unsigned char* L, const __amdgpu_buffer_rsrc_t rs, const int grp, const int r, const int w) {
    constexpr int STR = GTYPE ? 2 : 3;
    const int l = threadIdx.x & 63;
    const unsigned lbase = (unsigned)(size_t)L;
    const unsigned char* wsrc;
    int obase, ostride, osec;
    float oscale = 1.0f;
    if (GTYPE) {
        const int ks = 7 * (grp & 1) + w;
        if (w < 7) { wsrc = p.ws + OFF_TW + (size_t)((grp >> 1) * 14 + ks) * 28672; obase = (int)OFF_GF + ((grp >> 1) * 28 + ks) * 1024; ostride = (int)SEGB; osec = 14336; }
        else { wsrc = p.ws + OFF_QW + (size_t)grp * 28672; obase = (int)OFF_QF + (grp >> 4) * (2048 * 1024) + (grp & 15) * 1024; ostride = 32768; osec = 16384;
               if (grp < 16) oscale = LOG2E * 0.18898223650461362f; }
    } else {
        osec = 1024;
        if (grp < 60) { wsrc = p.ws + OFF_YW + (size_t)((grp - 32) * 8 + w) * 28672; obase = (int)OFF_UF + ((grp - 32) * 8 + w) * 2048; ostride = (int)SEGB; }
        else { wsrc = p.ws + OFF_QW + (size_t)(32 + (grp - 60) * 8 + w) * 28672; obase = (int)OFF_VF + ((grp - 60) * 8 + w) * 2048; ostride = 32768; }
    }
    const int npc = w < 4 ? 4 : 3, pc0 = w < 4 ? 4 * w : 16 + 3 * (w - 4);
    const unsigned char* xsrc = p.ws + OFF_XP + pc0 * 1024 + l * 16;
    if (r > 0) __builtin_amdgcn_s_sleep(SW_DLY);
    for (int k = 0; k < npc; ++k) glds16(xsrc + (size_t)r * XBUF + k * 1024, lbase + L_XB + (pc0 + k) * 1024);
    v4u wt[2][14];
#pragma unroll
    for (int f = 0; f < 14; ++f) { wt[0][f] = ldg16(wsrc + f * 1024 + l * 16); wt[1][f] = ldg16(wsrc + (14 + f) * 1024 + l * 16); }
#pragma unroll
    for (int f = 0; f < 14; ++f) asm volatile("" : "+v"(wt[0][f]), "+v"(wt[1][f]));
    asm volatile("s_waitcnt vmcnt(0)" ::: "memory");
    BAR();
    TLS(1);
    const int nseg = (NSEG + STR - 1 - r) / STR;
    for (int i = 0; i < nseg; ++i) {
        const int seg = r + STR * i;
        if (i + 1 < nseg) for (int k = 0; k < npc; ++k) glds16(xsrc + (size_t)(seg + STR) * XBUF + k * 1024, lbase + L_XB + ((i + 1) & 1) * XBUF + (pc0 + k) * 1024);
        const LAS unsigned char* xb = L + L_XB + (i & 1) * XBUF + l * 16;
        f32x4 a[2][2];
#pragma unroll
        for (int t = 0; t < 4; ++t) a[t >> 1][t & 1] = f32x4{0.f, 0.f, 0.f, 0.f};
#pragma unroll
        for (int f = 0; f < 14; ++f) {
            const v4u x0 = *(const LAS v4u*)(xb + f * 1024), x1 = *(const LAS v4u*)(xb + (14 + f) * 1024);
            if (GTYPE) {
                a[0][0] = mfma32(wt[0][f], x0, a[0][0]); a[0][1] = mfma32(wt[1][f], x0, a[0][1]);
                a[1][0] = mfma32(wt[0][f], x1, a[1][0]); a[1][1] = mfma32(wt[1][f], x1, a[1][1]);
            } else {
                a[0][0] = mfma32(x0, wt[0][f], a[0][0]); a[0][1] = mfma32(x0, wt[1][f], a[0][1]);
                a[1][0] = mfma32(x1, wt[0][f], a[1][0]); a[1][1] = mfma32(x1, wt[1][f], a[1][1]);
            }
        }
        v4u o0 = GTYPE ? pack8(a[0][0] * oscale, a[0][1] * oscale) : pack8(a[0][0], a[1][0]);
        v4u o1 = GTYPE ? pack8(a[1][0] * oscale, a[1][1] * oscale) : pack8(a[0][1], a[1][1]);
        stc16(o0, rs, l * 16, obase + seg * ostride);
        stc16(o1, rs, l * 16, obase + seg * ostride + osec);
        const bool drain = (i < 2 || i + 1 == nseg);
        if (drain) asm volatile("s_waitcnt vmcnt(0)" ::: "memory"); else asm volatile("s_waitcnt vmcnt(2)" ::: "memory");
        BAR();
        if (threadIdx.x == 0) __builtin_amdgcn_raw_buffer_store_b32((unsigned)(drain ? i + 1 : i), rs, CT_PROG + (grp * 3 + r) * 128, 0, 16);
        if (i == 0) TLS(2);
    }
    if (threadIdx.x == 0) __hip_atomic_fetch_add((GAS unsigned*)(p.ws + CT_DONE), 1u, __ATOMIC_RELAXED, __HIP_MEMORY_SCOPE_AGENT);
    TLS(3);
}

__device__ __forceinline__ void serve(const Params& p, LAS unsigned char* L, const __amdgpu_buffer_rsrc_t rs, const int grp, const int w) {
    const int l = threadIdx.x & 63, n = l & 15, g = l >> 4;
    const int ks = 7 * (grp & 1) + w;
    v4u wt[2][14];
    {
        const unsigned char* wsrc = p.ws + OFF_TW + (size_t)((grp >> 1) * 14 + (w < 7 ? ks : 0)) * 28672;
        __builtin_amdgcn_s_sleep(100);
#pragma unroll
        for (int f = 0; f < 14; ++f) { wt[0][f] = ldg16(wsrc + f * 1024 + l * 16); wt[1][f] = ldg16(wsrc + (14 + f) * 1024 + l * 16); }
#pragma unroll
        for (int f = 0; f < 14; ++f) asm volatile("" : "+v"(wt[0][f]), "+v"(wt[1][f]));
    }
    const unsigned parmask = load_parmask(p.ws);
    unsigned mspin = 0;
    const int mrow = 4 * ((n >> 2) & 1) + 2 * (n >> 3) + (n & 1);
    const LAS unsigned char* mrd = L + (g >> 1) * 128 + mrow * 16 + (g & 1) * 8;
    const unsigned char* xr = p.ws + OFF_XP + (w < 7 ? ks : 0) * 1024 + l * 16;
    v4u mx_ = ldc16(rs, (w < 7 ? w : 0) * 1024 + l * 16, hop_off(0, parmask) + C_M3);
    for (int s = 0; s < NSEG - 1; ++s) {
        v4u xa, xb2;
        if (w < 7) {
            xa = ldg16(xr + (size_t)s * 28672); xb2 = ldg16(xr + (size_t)s * 28672 + 14336);
            const int ho = hop_off(s, parmask) + C_M3;
            while (!fresh(mx_) && ++mspin < SPIN_CAP) {
                __builtin_amdgcn_s_sleep(2);
                asm volatile("" ::: "memory");
                mx_ = ldc16(rs, w * 1024 + l * 16, ho);
            }
            *(LAS v4u*)(L + w * 1024 + l * 16) = mx_;
            FENCE();
            mx_ = ldc16(rs, w * 1024 + l * 16, hop_off(s + 1 < NSEG ? s + 1 : s, parmask) + C_M3);
        }
        BAR();
        if (w < 7) {
            v2u lo[14], hi[14];
#pragma unroll
            for (int f = 0; f < 14; ++f) {
                lo[f] = *(const LAS v2u*)(mrd + (f >> 1) * 1024 + (f & 1) * 256); hi[f] = *(const LAS v2u*)(mrd + (f >> 1) * 1024 + (f & 1) * 256 + 512);
            }
            FENCE();
            f32x4 a0 = {0.f, 0.f, 0.f, 0.f}, a1 = a0;
#pragma unroll
            for (int f = 0; f < 14; ++f) {
                const v4u mf = v4u{lo[f].x, hi[f].x, lo[f].y, hi[f].y};
                a0 = mfma32(wt[0][f], mf, a0); a1 = mfma32(wt[1][f], mf, a1);
            }
            const v4u gm = pack8(a0, a1);
            const f32x4 z4 = {0.f, 0.f, 0.f, 0.f};
            const f32x4 d0 = mfma32(gm, xa, z4), d1 = mfma32(gm, xb2, z4);
            *(LAS f32x4*)(L + 8192 + w * 1024 + l * 16) = f32x4{d0[0], d0[1], d1[0], d1[1]};
        }
        BAR();
        TLS(8 + s);
        if (w == 7) {
            f32x4 q[7];
#pragma unroll
            for (int k = 0; k < 7; ++k) q[k] = *(const LAS f32x4*)(L + 8192 + k * 1024 + l * 16);
            const f32x4 t = ((q[0] + q[1]) + (q[2] + q[3])) + ((q[4] + q[5]) + q[6]);
            stc16(v4u{sane(fbits(t[0])), sane(fbits(t[1])), sane(fbits(t[2])), sane(fbits(t[3]))}, rs, l * 16,
                                                   (int)OFF_SXM + (s * 32 + grp) * 1024);
        }
    }
}

__device__ __forceinline__ void attn_item(const Params& p, const __amdgpu_buffer_rsrc_t rs, const int item) {
    const int l = threadIdx.x & 63, n = l & 15, g = l >> 4;
    const int rt = item >> 4, h = item & 15;
    const int s = rt >> 1, q = rt * 16 + n;
    const v4u qf = ldc16(rs, l * 16, (int)OFF_QF + (rt * 16 + h) * 1024);
    f32x4 sc[18];
    const f32x4 z4 = {0.f, 0.f, 0.f, 0.f};
    const float NEG = -3.0e38f;
    v4u kf[18];
#pragma unroll
    for (int k = 0; k < 18; ++k) { const int kt = max(2 * (s - 8) + k, 0); kf[k] = ldc16(rs, l * 16, (int)OFF_KF + (kt * 16 + h) * 1024); }
#pragma unroll
    for (int k = 0; k < 18; ++k) {
        const int kt = 2 * (s - 8) + k;
        f32x4 a = mfma32(kf[k], qf, z4);
        if (k < 2 || k >= 16) {
#pragma unroll
            for (int r = 0; r < 4; ++r) {
                const int dq = q - (16 * kt + 4 * g + r);
                a[r] = (dq >= 0 && dq < 256) ? a[r] : NEG;
            }
        }
        sc[k] = kt >= 0 ? a : f32x4{NEG, NEG, NEG, NEG};
    }
    FENCE();
    v4u vf[18];
#pragma unroll
    for (int k = 0; k < 18; ++k) { const int b = max(s - 8 + (k >> 1), 0); vf[k] = ldc16(rs, l * 16, (int)OFF_VF + ((b * 16 + h) * 2 + (k & 1)) * 1024); }
    float m = NEG;
#pragma unroll
    for (int k = 0; k < 18; ++k) m = fmaxf(m, fmaxf(fmaxf(sc[k][0], sc[k][1]), fmaxf(sc[k][2], sc[k][3])));
    m = xg_max(m);
    float sum = 0.f;
    f32x4 o0 = z4, o1 = z4;
#pragma unroll
    for (int blk = 0; blk < 9; ++blk) {
        f32x4 e0, e1;
#pragma unroll
        for (int r = 0; r < 4; ++r) { e0[r] = ex2(sc[2 * blk][r] - m); e1[r] = ex2(sc[2 * blk + 1][r] - m); }
        sum += ((e0[0] + e0[1]) + (e0[2] + e0[3])) + ((e1[0] + e1[1]) + (e1[2] + e1[3]));
        const v4u pb = pack8(e0, e1);
        o0 = mfma32(vf[2 * blk], pb, o0);
        o1 = mfma32(vf[2 * blk + 1], pb, o1);
    }
    sum = xg_sum(sum);
    const float f = ((const float*)(p.ws + OFF_GATE))[q] / sum;
    stc16(pack8(o0 * f, o1 * f), rs, l * 16, (int)OFF_OF + (rt * 16 + h) * 1024);
    asm volatile("s_waitcnt vmcnt(0)" ::: "memory");
    if (l == 0) __hip_atomic_fetch_add((GAS unsigned*)(p.ws + CT_OFC + s * 128), 1u, __ATOMIC_RELAXED, __HIP_MEMORY_SCOPE_AGENT);
}

__device__ __forceinline__ void out_item(const Params& p, LAS unsigned char* L, const __amdgpu_buffer_rsrc_t rs, const int s, const int q4, const int w, unsigned& mspin) {
    const int l = threadIdx.x & 63, n = l & 15, g = l >> 4;
    const f32x4 z4 = {0.f, 0.f, 0.f, 0.f};
    const float NEG = -3.0e38f;
    const int v = 7 * q4 + (w < 7 ? w : 0);
    f32x4 sxx[2][3];
#pragma unroll
    for (int a = 0; a < 2; ++a) { sxx[a][0] = z4; sxx[a][1] = z4; sxx[a][2] = z4; }
    {
        const int gp = (int)OFF_GF + s * (int)SEGB + (2 * w) * 28 * 1024;
        const unsigned char* xr = p.ws + OFF_XP + (size_t)s * 28672 + l * 16;
#pragma unroll 1
        for (int kh = 0; kh < 2; ++kh) {
            v4u x0[7], x1[7], kk[4][7];
#pragma unroll
            for (int j = 0; j < 7; ++j) {
                const int ks = 7 * kh + j;
                x0[j] = ldg16(xr + ks * 1024); x1[j] = ldg16(xr + (14 + ks) * 1024);
#pragma unroll
                for (int t = 0; t < 4; ++t) kk[t][j] = ldc16(rs, l * 16, gp + (t * 14 + ks) * 1024);
            }
            FENCE();
#pragma unroll
            for (int j = 0; j < 7; ++j) {
                const v4u xa = x0[j], xb = x1[j];
#pragma unroll
                for (int a = 0; a < 2; ++a) {
                    sxx[a][0] = mfma32(kk[2 * a][j], xa, sxx[a][0]);
                    sxx[a][1] = mfma32(kk[2 * a][j], xb, sxx[a][1]);
                    sxx[a][2] = mfma32(kk[2 * a + 1][j], xb, sxx[a][2]);
                }
            }
            FENCE();
        }
    }
    if (w == 0) {
        for (;;) {
            asm volatile("" ::: "memory");
            const unsigned c = __builtin_amdgcn_raw_buffer_load_b32(rs, CT_OFC + s * 128, 0, 16);
            if (__builtin_amdgcn_readfirstlane(c) >= 32u || ++mspin >= SPIN_CAP) break;
            __builtin_amdgcn_s_sleep(4);
        }
    }
    BAR();
    f32x4 o0 = z4, o1 = z4;
    {
        const unsigned char* wp = p.ws + OFF_WOF + (size_t)(v * 16) * 1024 + l * 16;
#pragma unroll 1
        for (int hb = 0; hb < 16; hb += 8) {
            v4u wf[8], oa[8], ob[8];
#pragma unroll
            for (int h = 0; h < 8; ++h) {
                wf[h] = ldg16(wp + (hb + h) * 1024);
                oa[h] = ldc16(rs, l * 16, (int)OFF_OF + (2 * s * 16 + hb + h) * 1024);
                ob[h] = ldc16(rs, l * 16, (int)OFF_OF + ((2 * s + 1) * 16 + hb + h) * 1024);
            }
            FENCE();
#pragma unroll
            for (int h = 0; h < 8; ++h) { o0 = mfma32(wf[h], oa[h], o0); o1 = mfma32(wf[h], ob[h], o1); }
            FENCE();
        }
    }
    v4u uf[16];
#pragma unroll
    for (int h = 0; h < 16; ++h) uf[h] = ldc16(rs, l * 16, (int)OFF_UF + s * (int)SEGB + (v * 16 + h) * 1024);
    float gate0 = 1.0f - ((const float*)(p.ws + OFF_GATE))[32 * s + n], gate1 = 1.0f - ((const float*)(p.ws + OFF_GATE))[32 * s + 16 + n];
    float mxx[2][2], lxx[2][2];
#pragma unroll
    for (int a = 0; a < 2; ++a) {
#pragma unroll
        for (int r = 0; r < 4; ++r) { sxx[a][0][r] = (4 * g + r <= n) ? sxx[a][0][r] : NEG; sxx[a][2][r] = (4 * g + r <= n) ? sxx[a][2][r] : NEG; }
        mxx[a][0] = xg_max(fmaxf(fmaxf(sxx[a][0][0], sxx[a][0][1]), fmaxf(sxx[a][0][2], sxx[a][0][3])));
        mxx[a][1] = xg_max(fmaxf(fmaxf(fmaxf(sxx[a][1][0], sxx[a][1][1]), fmaxf(sxx[a][1][2], sxx[a][1][3])), fmaxf(fmaxf(sxx[a][2][0], sxx[a][2][1]), fmaxf(sxx[a][2][2], sxx[a][2][3]))));
#pragma unroll
        for (int r = 0; r < 4; ++r) {
            sxx[a][0][r] = ex2(sxx[a][0][r] - mxx[a][0]); sxx[a][1][r] = ex2(sxx[a][1][r] - mxx[a][1]); sxx[a][2][r] = ex2(sxx[a][2][r] - mxx[a][1]);
        }
        lxx[a][0] = (sxx[a][0][0] + sxx[a][0][1]) + (sxx[a][0][2] + sxx[a][0][3]);
        lxx[a][1] = ((sxx[a][1][0] + sxx[a][1][1]) + (sxx[a][1][2] + sxx[a][1][3])) + ((sxx[a][2][0] + sxx[a][2][1]) + (sxx[a][2][2] + sxx[a][2][3]));
    }
#pragma unroll
    for (int h = 0; h < 16; ++h) asm volatile("" : "+v"(uf[h]));
#pragma unroll
    for (int a = 0; a < 2; ++a)
        asm volatile("" : "+v"(sxx[a][0]), "+v"(sxx[a][1]), "+v"(sxx[a][2]), "+v"(mxx[a][0]), "+v"(mxx[a][1]), "+v"(lxx[a][0]), "+v"(lxx[a][1]));
    asm volatile("" : "+v"(gate0), "+v"(gate1), "+v"(o0), "+v"(o1));
    FENCE();
    TLS(s < 40 ? 8 : 10);
    if (s + 3 < NSEG) {
        for (;;) {
            asm volatile("" ::: "memory");
            const unsigned x = __builtin_amdgcn_raw_buffer_load_b32(rs, 0, (int)OFF_SXM + ((s + 2) * 32 + 4 * w) * 1024, 16);
            if (__builtin_amdgcn_readfirstlane(x) != 0xFFFFFFFFu || ++mspin >= SPIN_CAP) break;
            __builtin_amdgcn_s_sleep(6);
        }
    } else {
        for (;;) {
            asm volatile("" ::: "memory");
            const unsigned x = __builtin_amdgcn_raw_buffer_load_b32(rs, 0, (int)OFF_SXM + ((s - 2) * 32 + 4 * w) * 1024, 16);
            if (__builtin_amdgcn_readfirstlane(x) != 0xFFFFFFFFu || ++mspin >= SPIN_CAP) break;
            __builtin_amdgcn_s_sleep(6);
        }
    }
    asm volatile("" ::: "memory");
    v4u sm[4];
    v2u yf[8];
    for (;;) {
        asm volatile("" ::: "memory");
#pragma unroll
        for (int k = 0; k < 4; ++k) sm[k] = ldc16(rs, l * 16, (int)OFF_SXM + (s * 32 + 4 * w + k) * 1024);
        if (fresh(__builtin_elementwise_max(__builtin_elementwise_max(sm[0], sm[1]), __builtin_elementwise_max(sm[2], sm[3]))) || ++mspin >= SPIN_CAP) break;
    }
    TLS(s < 40 ? 9 : 11);
    FENCE();
    unsigned px[2][2][4]; unsigned pm[2][2];
#pragma unroll
    for (int qt = 0; qt < 2; ++qt) {
#pragma unroll
        for (int a = 0; a < 2; ++a) {
            const unsigned u00 = sm[2 * a][2 * qt], u01 = sm[2 * a + 1][2 * qt], u10 = sm[2 * a][2 * qt + 1], u11 = sm[2 * a + 1][2 * qt + 1];
            const float t0 = bitsf(u00) + bitsf(u01), t1 = bitsf(u10) + bitsf(u11);
            const float m = xg_max_raw(fmaxf(mxx[a][qt], fmaxf(t0, t1)));
            const float e0 = ex2(t0 - m), e1 = ex2(t1 - m), be = ex2(mxx[a][qt] - m);
            const float f = (qt == 0 ? gate0 : gate1) * __builtin_amdgcn_rcpf(xg_sum((e0 + e1) + be * lxx[a][qt]));
            const float fx = f * be;
            if (qt == 0) { px[a][0][0] = pack2(sxx[a][0][0] * fx, sxx[a][0][1] * fx); px[a][0][1] = pack2(sxx[a][0][2] * fx, sxx[a][0][3] * fx); }
            else {
                px[a][1][0] = pack2(sxx[a][1][0] * fx, sxx[a][1][1] * fx); px[a][1][1] = pack2(sxx[a][1][2] * fx, sxx[a][1][3] * fx);
                px[a][1][2] = pack2(sxx[a][2][0] * fx, sxx[a][2][1] * fx); px[a][1][3] = pack2(sxx[a][2][2] * fx, sxx[a][2][3] * fx);
            }
            pm[qt][a] = pack2(e0 * f, e1 * f);
        }
    }
    *(LAS v4u*)(L + w * 4096 + l * 16) = v4u{px[0][0][0], px[0][0][1], px[1][0][0], px[1][0][1]};
    *(LAS v4u*)(L + w * 4096 + 1024 + l * 16) = v4u{px[0][1][0], px[0][1][1], px[0][1][2], px[0][1][3]};
    *(LAS v4u*)(L + w * 4096 + 2048 + l * 16) = v4u{px[1][1][0], px[1][1][1], px[1][1][2], px[1][1][3]};
    *(LAS v4u*)(L + w * 4096 + 3072 + l * 16) = v4u{pm[0][0], pm[0][1], pm[1][0], pm[1][1]};
    BAR();
    if (w < 7) {
#pragma unroll
        for (int k = 0; k < 8; ++k) yf[k] = __builtin_amdgcn_raw_buffer_load_b64(rs, l * 8, (int)OFF_YM + (s * 224 + v * 8 + k) * 512, 16);
        v4u pmm[8];
#pragma unroll
        for (int pr = 0; pr < 8; ++pr) {
            const v4u u0 = uf[2 * pr], u1 = uf[2 * pr + 1];
            const v4u pa = *(const LAS v4u*)(L + pr * 4096 + l * 16), pb = *(const LAS v4u*)(L + pr * 4096 + 1024 + l * 16),
                      pc = *(const LAS v4u*)(L + pr * 4096 + 2048 + l * 16);
            pmm[pr] = *(const LAS v4u*)(L + pr * 4096 + 3072 + l * 16);
            o1 = mfma32(u0, pb, o1);
            o0 = mfma32(v4u{u0.x, u0.y, u1.x, u1.y}, pa, o0);
            o1 = mfma32(u1, pc, o1);
        }
        FENCE();
        for (;;) {
            unsigned mm = 0u;
#pragma unroll
            for (int k = 0; k < 8; ++k) mm = max(mm, max(yf[k].x, yf[k].y));
            if (__all(mm != 0xFFFFFFFFu) || ++mspin >= SPIN_CAP) break;
            asm volatile("" ::: "memory");
            yf[0] = __builtin_amdgcn_raw_buffer_load_b64(rs, l * 8, (int)OFF_YM + (s * 224 + v * 8 + 0) * 512, 16);
            yf[1] = __builtin_amdgcn_raw_buffer_load_b64(rs, l * 8, (int)OFF_YM + (s * 224 + v * 8 + 1) * 512, 16);
            yf[2] = __builtin_amdgcn_raw_buffer_load_b64(rs, l * 8, (int)OFF_YM + (s * 224 + v * 8 + 2) * 512, 16);
            yf[3] = __builtin_amdgcn_raw_buffer_load_b64(rs, l * 8, (int)OFF_YM + (s * 224 + v * 8 + 3) * 512, 16);
            yf[4] = __builtin_amdgcn_raw_buffer_load_b64(rs, l * 8, (int)OFF_YM + (s * 224 + v * 8 + 4) * 512, 16);
            yf[5] = __builtin_amdgcn_raw_buffer_load_b64(rs, l * 8, (int)OFF_YM + (s * 224 + v * 8 + 5) * 512, 16);
            yf[6] = __builtin_amdgcn_raw_buffer_load_b64(rs, l * 8, (int)OFF_YM + (s * 224 + v * 8 + 6) * 512, 16);
            yf[7] = __builtin_amdgcn_raw_buffer_load_b64(rs, l * 8, (int)OFF_YM + (s * 224 + v * 8 + 7) * 512, 16);
        }
#pragma unroll
        for (int pp = 0; pp < 4; ++pp) {
            const v4u yy = v4u{yf[2 * pp].x, yf[2 * pp].y, yf[2 * pp + 1].x, yf[2 * pp + 1].y};
            o0 = mfma32(yy, v4u{pmm[2 * pp].x, pmm[2 * pp].y, pmm[2 * pp + 1].x, pmm[2 * pp + 1].y}, o0);
            o1 = mfma32(yy, v4u{pmm[2 * pp].z, pmm[2 * pp].w, pmm[2 * pp + 1].z, pmm[2 * pp + 1].w}, o1);
        }
        stg8(p.out + (size_t)(32 * s + n) * 448 + 16 * v + 4 * g, v2u{pack2(o0[0], o0[1]), pack2(o0[2], o0[3])});
        stg8(p.out + (size_t)(32 * s + 16 + n) * 448 + 16 * v + 4 * g, v2u{pack2(o1[0], o1[1]), pack2(o1[2], o1[3])});
    }
}

__global__ void __launch_bounds__(512, 2) k_main(Params p) {
    extern __shared__ __attribute__((aligned(16))) unsigned char lds_[];
    p.ws = align_ws(p.ws);
    const int rot = __builtin_amdgcn_readfirstlane(((const int*)(p.ws + OFF_CTL))[8]);
    const int grp = (blockIdx.x + rot) & 7, u = blockIdx.x >> 3;
    LAS unsigned char* L = (LAS unsigned char*)lds_;
    const int w = __builtin_amdgcn_readfirstlane(threadIdx.x >> 6);
    TLS(0);
    if (grp == 0) {
        if (w == 0) score_wave<1, 0>(p, L, u, w); else if (w < 3) score_wave<0, 0>(p, L, u, w); else if (w == 3) score_wave<0, DLY_3>(p, L, u, w);
        else if (w < 7) score_wave<0, DLY_4>(p, L, u, w); else score_wave<2, 0>(p, L, u, w);
        TLS(7);
        return;
    }
    if (grp == 1) {
        if (u < 28) { if (w < 3) value_wave<false, false>(p, L, u, w); else if (w < 7) value_wave<false, true>(p, L, u, w); else value_wave<true, false>(p, L, u, w); }
        TLS(7);
        return;
    }
    const __amdgpu_buffer_rsrc_t rs = __builtin_amdgcn_make_buffer_rsrc(p.ws, 0, (int)WS_END, 0x00020000);
    const int k = (grp - 2) * 32 + u;
    if (k < 96) {
        if (k % 3 == 0) { serve(p, L, rs, k / 3, w); TLS(7); return; }
        sweep<true>(p, L, rs, k / 3, k % 3 - 1, w);
    } else if (k < 186) sweep<false>(p, L, rs, k / 3, k % 3, w);
    else {
        const unsigned parmask = load_parmask(p.ws);
        const int l = threadIdx.x & 63, wi = (k - 186) * 8 + w;
        const v4u ff = v4u{0xFFFFFFFFu, 0xFFFFFFFFu, 0xFFFFFFFFu, 0xFFFFFFFFu};
        __builtin_amdgcn_s_sleep(127); __builtin_amdgcn_s_sleep(127);
        for (int pg = 1; pg < NPAGE; ++pg) {
            const int base = hop_off(pg * SPP, parmask);
            for (int i = wi; i < SPP * NCHUNK * 8; i += 48) stc16(ff, rs, l * 16, base + (i >> 3) * CH + (i & 7) * 1024);
            for (int i = wi; i < SPP * 32 && pg * SPP * 32 + i < NSEG * 32; i += 48) stc16(ff, rs, l * 16, (int)OFF_SXM + (pg * SPP * 32 + i) * 1024);
            asm volatile("s_waitcnt vmcnt(0)" ::: "memory");
            BAR();
            if (threadIdx.x == 0) __hip_atomic_fetch_add((GAS unsigned*)(p.ws + CT_PZ), 1u << (4 * (pg - 1)), __ATOMIC_RELAXED, __HIP_MEMORY_SCOPE_AGENT);
        }
        if (threadIdx.x == 0) __hip_atomic_fetch_add((GAS unsigned*)(p.ws + CT_DONE), 1u, __ATOMIC_RELAXED, __HIP_MEMORY_SCOPE_AGENT);
    }
    const int wk = k < 96 ? (k / 3) * 2 + (k % 3 - 1) : k - 32;
    unsigned mspin = 0;
    if (w == 0) {
        for (;;) {
            asm volatile("" ::: "memory");
            const unsigned c = __builtin_amdgcn_raw_buffer_load_b32(rs, CT_DONE, 0, 16);
            if (__builtin_amdgcn_readfirstlane(c) >= 160u || ++mspin >= SPIN_CAP) break;
            __builtin_amdgcn_s_sleep(16);
        }
    }
    BAR();
    TLS(4);
    for (int it = wk * 8 + w; it < 2048; it += 1280) attn_item(p, rs, it);
    TLS(5);
    for (int i = 0; i < (wk * STAG) >> 4; ++i) __builtin_amdgcn_s_sleep(64);
    for (int it = wk; it < 256; it += 160) { out_item(p, L, rs, it >> 2, it & 3, w, mspin); TLS(it < 160 ? 6 : 7); }
}

extern "C" void kernel_launch(void* const* d_in, const int* in_sizes, int n_in, void* d_out, int out_size, void* d_ws, size_t ws_size,
                              hipStream_t stream) {
    Params p;
    p.x = (const u16*)d_in[0]; p.wqkv = (const u16*)d_in[1]; p.wo = (const u16*)d_in[2]; p.mem0 = (const u16*)d_in[4];
    p.Wq = (const u16*)d_in[5]; p.Wk = (const u16*)d_in[6]; p.Wv = (const u16*)d_in[7]; p.Wo = (const u16*)d_in[8];
    p.gate_w = (const u16*)d_in[10];
    p.out = (u16*)d_out; p.ws = (unsigned char*)d_ws;
    (void)hipFuncSetAttribute((const void*)k_prep, hipFuncAttributeMaxDynamicSharedMemorySize, K1_LDS);
    (void)hipFuncSetAttribute((const void*)k_main, hipFuncAttributeMaxDynamicSharedMemorySize, L_REC_END);
    k_prep<<<256, 512, K1_LDS, stream>>>(p);
    k_main<<<256, 512, L_REC_END, stream>>>(p);
}
```

```cpp
#include <hip/hip_runtime.h>
#include <hip/hip_bf16.h>
#include <stdint.h>

#define GAS __attribute__((address_space(1)))
#define LAS __attribute__((address_space(3)))
typedef __bf16 bf16x2 __attribute__((ext_vector_type(2)));
typedef __bf16 bf16x4 __attribute__((ext_vector_type(4)));
typedef __bf16 bf16x8 __attribute__((ext_vector_type(8)));
typedef __bf16 bf16x16 __attribute__((ext_vector_type(16)));
typedef short s16x4 __attribute__((ext_vector_type(4)));
typedef float f32x2 __attribute__((ext_vector_type(2)));
typedef float f32x4 __attribute__((ext_vector_type(4)));
typedef unsigned v2u __attribute__((ext_vector_type(2)));
typedef unsigned v4u __attribute__((ext_vector_type(4)));
typedef unsigned v8u __attribute__((ext_vector_type(8)));
typedef unsigned short u16;

constexpr int S_ = 2048, D_ = 448, H_ = 16, NSEG = 64;
constexpr float LOG2E = 1.4426950408889634f;

constexpr size_t SZ_W = (size_t)16 * 28 * 14 * 1024;
constexpr size_t OFF_HOP = 0;
constexpr int PAGE = 2 << 20, CH = 16384, NCHUNK = 9, STEPB = NCHUNK * CH, NPAGE = 5, SPP = 14;
constexpr int C_M = 0, C_M2 = 4 * CH, C_M3 = 5 * CH, C_S = 6 * CH, C_P = 7 * CH;
constexpr size_t HOP_BYTES = (size_t)NPAGE * PAGE;
constexpr size_t OFF_CTL = OFF_HOP + HOP_BYTES;
constexpr size_t CTL_BYTES = 36864;
constexpr int CT_DONE = (int)OFF_CTL + 256, CT_PZ = (int)OFF_CTL + 384, CT_OFC = (int)OFF_CTL + 512, CT_PROG = CT_OFC + 64 * 128;
constexpr size_t OFF_TW = OFF_CTL + CTL_BYTES;
constexpr size_t OFF_YW = OFF_TW + SZ_W;
constexpr size_t OFF_QW = OFF_YW + SZ_W;
constexpr size_t OFF_VW = OFF_QW + (size_t)32 * 28 * 1024;
constexpr size_t OFF_WOF = OFF_VW + (size_t)16 * 28 * 1024;
constexpr size_t OFF_XP = OFF_WOF + (size_t)28 * 16 * 1024;
constexpr size_t OFF_GATE = OFF_XP + (size_t)128 * 14 * 1024;
constexpr size_t SEGB = (size_t)448 * 1024;
constexpr size_t OFF_GF = OFF_GATE + 8192;
constexpr size_t OFF_UF = OFF_GF + 64 * SEGB;
constexpr size_t OFF_QF = OFF_UF + 64 * SEGB;
constexpr size_t OFF_KF = OFF_QF + (size_t)2048 * 1024;
constexpr size_t OFF_VF = OFF_KF + (size_t)2048 * 1024;
constexpr size_t OFF_OF = OFF_VF + (size_t)2048 * 1024;
constexpr size_t OFF_SXM = OFF_OF + (size_t)2048 * 1024;
constexpr size_t OFF_YM = OFF_SXM + (size_t)64 * 32 * 1024;
constexpr size_t WS_END = OFF_YM + (size_t)64 * 224 * 512;

struct Params {
    const u16* x; const u16* wqkv; const u16* wo; const u16* mem0;
    const u16* Wq; const u16* Wk; const u16* Wv; const u16* Wo; const u16* gate_w;
    u16* out; unsigned char* ws;
};
__device__ __forceinline__ unsigned char* align_ws(unsigned char* w) {
    return w + ((0 - (size_t)w) & (size_t)(PAGE - 1));
}
__device__ __forceinline__ unsigned xcc_id() { return (unsigned)__builtin_amdgcn_s_getreg(20 | (3 << 11)) & 0xfu; }
__device__ __forceinline__ unsigned load_parmask(const unsigned char* ws) {
    const unsigned* c = (const unsigned*)(ws + OFF_CTL);
    unsigned m = 0;
#pragma unroll
    for (int k = 0; k < NPAGE; ++k) m |= (c[k] & 1u) << k;
    return __builtin_amdgcn_readfirstlane(m);
}
__device__ __forceinline__ int hop_off(int s, unsigned parmask) {
    const int pg = s / SPP, sl = s - pg * SPP;
    return pg * PAGE + sl * STEPB + (int)((parmask >> pg) & 1u) * 8192;
}

__device__ __forceinline__ unsigned pack2(float a, float b) {
    return __builtin_bit_cast(unsigned, __builtin_convertvector(f32x2{a, b}, bf16x2));
}
__device__ __forceinline__ float bf2f(u16 v) { return __builtin_bit_cast(float, (unsigned)v << 16); }
__device__ __forceinline__ f32x4 mfma32(v4u a, v4u b, f32x4 c) {
    return __builtin_amdgcn_mfma_f32_16x16x32_bf16(__builtin_bit_cast(bf16x8, a), __builtin_bit_cast(bf16x8, b), c, 0, 0, 0);
}
__device__ __forceinline__ f32x4 mfma16(v2u a, v2u b, f32x4 c) {
    return __builtin_amdgcn_mfma_f32_16x16x16bf16_1k(__builtin_bit_cast(s16x4, a), __builtin_bit_cast(s16x4, b), c, 0, 0, 0);
}
__device__ __forceinline__ f32x4 smfmac(v4u a, v8u b, f32x4 c, int idx) {
    return __builtin_amdgcn_smfmac_f32_16x16x64_bf16(__builtin_bit_cast(bf16x8, a), __builtin_bit_cast(bf16x16, b), c, idx, 0, 0);
}
__device__ __forceinline__ v4u ldg16(const void* p) { return *(const GAS v4u*)p; }
__device__ __forceinline__ v2u ldg8(const void* p) { return *(const GAS v2u*)p; }
__device__ __forceinline__ void stg16(void* p, v4u v) { *(GAS v4u*)p = v; }
__device__ __forceinline__ void stg8(void* p, v2u v) { *(GAS v2u*)p = v; }
__device__ __forceinline__ void stc16(v4u v, const __amdgpu_buffer_rsrc_t rs, int voff, int soff) { __builtin_amdgcn_raw_buffer_store_b128(v, rs, voff + soff, 0, 16); }
__device__ __forceinline__ v2u tr_read(const LAS void* p) {
    return __builtin_bit_cast(v2u, __builtin_amdgcn_ds_read_tr16_b64_v4i16((LAS s16x4*)p));
}
__device__ __forceinline__ float ror8(float v) {
    return __builtin_bit_cast(float, __builtin_amdgcn_update_dpp(0, __builtin_bit_cast(int, v), 0x128, 0xf, 0xf, false));
}
__device__ __forceinline__ unsigned fbits(float v) { return __builtin_bit_cast(unsigned, v); }
__device__ __forceinline__ float bitsf(unsigned v) { return __builtin_bit_cast(float, v); }
__device__ __forceinline__ float xg_max(float v) {
    const v2u a = __builtin_amdgcn_permlane16_swap(fbits(v), fbits(v), false, false);
    v = fmaxf(bitsf(a.x), bitsf(a.y));
    const v2u b = __builtin_amdgcn_permlane32_swap(fbits(v), fbits(v), false, false);
    return fmaxf(bitsf(b.x), bitsf(b.y));
}
__device__ __forceinline__ float xg_sum(float v) {
    const v2u a = __builtin_amdgcn_permlane16_swap(fbits(v), fbits(v), false, false);
    v = bitsf(a.x) + bitsf(a.y);
    const v2u b = __builtin_amdgcn_permlane32_swap(fbits(v), fbits(v), false, false);
    return bitsf(b.x) + bitsf(b.y);
}
__device__ __forceinline__ float fold32(float a, float b) {
    const v2u r = __builtin_amdgcn_permlane32_swap(fbits(a), fbits(b), false, false);
    return bitsf(r.x) + bitsf(r.y);
}
__device__ __forceinline__ float ex2(float v) { return __builtin_amdgcn_exp2f(v); }
__device__ __forceinline__ unsigned sane(unsigned v) { return v < 0xFFFFFFFEu ? v : 0xFFFFFFFEu; }

__device__ __forceinline__ int porder(int f, int g, int e) {
    return 64 * (f >> 1) + 16 * (f & 1) + 4 * g + 32 * ((e >> 1) & 1) + 2 * (e >> 2) + (e & 1);
}
constexpr int NU_HOP = SPP * NCHUNK * 16;
template <bool MIDBAR>
__device__ __forceinline__ void misc_wave(const Params& p, const int mw, const int l) {
    const int n = l & 15, g = l >> 4;
    v2u xl[4], xh[4];
#pragma unroll
    for (int k = 0; k < 4; ++k) {
        const int i = mw * 4 + k, rt = i / 14, f = i % 14;
        const u16* row = p.x + (size_t)(rt * 16 + n) * 448 + 64 * (f >> 1) + 16 * (f & 1) + 4 * g;
        xl[k] = ldg8(row); xh[k] = ldg8(row + 32);
    }
    unsigned qv[3][8];
    bool qok[3];
#pragma unroll
    for (int k = 0; k < 3; ++k) {
        const int i = mw * 3 + k;
        const bool isv = i >= 896;
        const int fr = isv ? i - 896 : i;
        const int unit = fr / 28, t = (fr % 28) / 14, f = fr % 14;
        const int d = isv ? 16 * t + n : 8 * (n >> 2) + 4 * t + (n & 3);
        const int col = (isv ? 896 + unit * 28 : (unit >> 4) * 448 + (unit & 15) * 28) + (d < 28 ? d : 0);
        qok[k] = d < 28;
#pragma unroll
        for (int e = 0; e < 8; ++e) qv[k][e] = p.wqkv[(size_t)porder(f, g, e) * 1344 + col];
    }
    unsigned wv[8];
#pragma unroll
    for (int e = 0; e < 8; ++e) {
        const int d = (e < 4) ? 4 * g + e : 16 + 4 * g + e - 4;
        wv[e] = p.wo[(size_t)((mw & 15) * 28 + (d < 28 ? d : 0)) * 448 + 16 * (mw >> 4) + n];
        if (d >= 28) wv[e] = 0u;
    }
    v4u gxv[7], ggv[7];
    const int row = (mw & 255) * 8 + (l >> 3), c0 = (l & 7) * 8;
#pragma unroll
    for (int k = 0; k < 7; ++k) { gxv[k] = ldg16(p.x + (size_t)row * 448 + k * 64 + c0); ggv[k] = ldg16(p.gate_w + k * 64 + c0); }
    for (int i = mw; i < NU_HOP; i += 448) {
        const int st = i / (NCHUNK * 16), ck = (i >> 4) % NCHUNK, kb = i & 15;
        v4u val = v4u{0xFFFFFFFFu, 0xFFFFFFFFu, 0xFFFFFFFFu, 0xFFFFFFFFu};
        if (st == 0 && ck < 6 && (kb & 7) < 7) {
            const int im = (kb & 7) * 64 + l;
            const v4u m = ldg16(p.mem0 + (size_t)(im & 7) * 448 + (im >> 3) * 8);
            val = v4u{sane(m.x), sane(m.y), sane(m.z), sane(m.w)};
        }
        stg16(p.ws + OFF_HOP + (size_t)hop_off(st, 0u) + ck * CH + kb * 1024 + l * 16, val);
    }
    if (MIDBAR) { __builtin_amdgcn_sched_barrier(0); __builtin_amdgcn_s_barrier(); __builtin_amdgcn_sched_barrier(0); }
    static_assert(SPP * 32 <= 448, "one KiB of the first page's S_xm slots per wave");
    if (mw < SPP * 32) stg16(p.ws + OFF_SXM + (size_t)mw * 1024 + l * 16, v4u{0xFFFFFFFFu, 0xFFFFFFFFu, 0xFFFFFFFFu, 0xFFFFFFFFu});
    if (mw < 336) stg16(p.ws + OFF_YM + (size_t)61 * 224 * 512 + (size_t)mw * 1024 + l * 16, v4u{0xFFFFFFFFu, 0xFFFFFFFFu, 0xFFFFFFFFu, 0xFFFFFFFFu});
#pragma unroll
    for (int k = 0; k < 4; ++k) stg16(p.ws + OFF_XP + (size_t)(mw * 4 + k) * 1024 + l * 16, v4u{xl[k].x, xh[k].x, xl[k].y, xh[k].y});
#pragma unroll
    for (int k = 0; k < 3; ++k) {
        const v4u o = v4u{qv[k][0] | (qv[k][1] << 16), qv[k][2] | (qv[k][3] << 16), qv[k][4] | (qv[k][5] << 16), qv[k][6] | (qv[k][7] << 16)};
        stg16(p.ws + OFF_QW + (size_t)(mw * 3 + k) * 1024 + l * 16, qok[k] ? o : v4u{0u, 0u, 0u, 0u});
    }
    stg16(p.ws + OFF_WOF + (size_t)mw * 1024 + l * 16, v4u{wv[0] | (wv[1] << 16), wv[2] | (wv[3] << 16), wv[4] | (wv[5] << 16), wv[6] | (wv[7] << 16)});
    float s = 0.f;
#pragma unroll
    for (int k = 0; k < 7; ++k)
#pragma unroll
        for (int j = 0; j < 4; ++j) {
            s += bitsf(gxv[k][j] << 16) * bitsf(ggv[k][j] << 16);
            s += bitsf(gxv[k][j] & 0xFFFF0000u) * bitsf(ggv[k][j] & 0xFFFF0000u);
        }
    s += __shfl_xor(s, 1); s += __shfl_xor(s, 2); s += __shfl_xor(s, 4);
    if ((l & 7) == 0 && mw < 256) ((float*)(p.ws + OFF_GATE))[row] = 1.0f / (1.0f + __expf(-s));
}

#define K1_AUX 16
#define MISC_MODE 0
#define K1_LAUX 0
#define K1_NORNT
__device__ __forceinline__ v4u ldr16(const void* q) { return ldg16(q); }
constexpr int K1_RS = 528, K1_OS = 144, K1_IMG = 36864, K1_IS = 136, K1_LDS = K1_IMG + 7 * 64 * K1_IS;
#define PT(k) do {} while (0)
#define PT0(k) do {} while (0)
template <int mat>
__device__ __forceinline__ void prep_heavy(const Params& p, LAS unsigned char* L, const int h, const int blk, const int w) {
    const int b = blockIdx.x, tid = threadIdx.x, l = tid & 63, n = l & 15, g = l >> 4;
    v4u t[4];
    if (mat == 0) {
#pragma unroll
        for (int k = 0; k < 4; ++k) { const int u = tid + 512 * k; t[k] = ldr16(p.Wq + ((size_t)(64 * blk + (u >> 5)) * 16 + h) * 256 + (u & 31) * 8); }
    } else {
#pragma unroll
        for (int k = 0; k < 4; ++k) { const int u = tid + 512 * k; t[k] = ldr16(p.Wo + ((size_t)h * 256 + (u >> 3)) * 448 + 64 * blk + (u & 7) * 8); }
    }
    __builtin_amdgcn_sched_barrier(0);
    const int rs_ = mat == 0 ? K1_RS : K1_OS, sh = mat == 0 ? 5 : 3;
    if (w == 7) {
#pragma unroll
        for (int k = 0; k < 4; ++k) { const int u = tid + 512 * k; *(LAS v4u*)(L + (u >> sh) * rs_ + (u & ((1 << sh) - 1)) * 16) = t[k]; }
        asm volatile("s_waitcnt lgkmcnt(0)" ::: "memory");
        misc_wave<true>(p, 224 + b, l);
        PT(3);
        return;
    }
    v4u cr[4][8];
    LAS unsigned char* img = L + K1_IMG + w * 64 * K1_IS;
    {
        const __amdgpu_buffer_rsrc_t wr = __builtin_amdgcn_make_buffer_rsrc((void*)(mat == 0 ? p.Wk : p.Wv), 0, 448 * 4096 * 2, 0x00020000);
        const int vo = ((64 * w + (l >> 5)) * 16 + h) * 512 + (l & 31) * 16;
#define K1_LOAD(tt) do { _Pragma("unroll") for (int j = 0; j < 8; ++j) \
            cr[tt][j] = __builtin_amdgcn_raw_buffer_load_b128(wr, vo, (16 * ((tt) >> 1) + 2 * ((tt) & 1) + 4 * (j >> 1) + 32 * (j & 1)) * 8192, K1_LAUX); \
            __builtin_amdgcn_sched_barrier(0); } while (0)
#define K1_TURN(tt) do { \
        _Pragma("unroll") for (int j = 0; j < 8; ++j) *(LAS v4u*)(img + (2 * j + (l >> 5)) * K1_RS + (l & 31) * 16) = cr[tt][j]; \
        asm volatile("" ::: "memory"); \
        _Pragma("unroll") for (int es = 0; es < 8; ++es) cr[tt][es] = *(const LAS v4u*)(img + n * K1_RS + es * 64 + g * 16); \
        asm volatile("" ::: "memory"); __builtin_amdgcn_sched_barrier(0); } while (0)
        K1_LOAD(0); K1_LOAD(1); K1_LOAD(2);
#pragma unroll
        for (int k = 0; k < 4; ++k) { const int u = tid + 512 * k; *(LAS v4u*)(L + (u >> sh) * rs_ + (u & ((1 << sh) - 1)) * 16) = t[k]; }
        __builtin_amdgcn_sched_barrier(0);
        K1_TURN(0);
        K1_LOAD(3);
        K1_TURN(1); K1_TURN(2); K1_TURN(3);
    }
    asm volatile("s_waitcnt lgkmcnt(0)" ::: "memory");
    __builtin_amdgcn_s_barrier();
    asm volatile("" ::: "memory");
    PT0(1);
    constexpr float SC = mat == 0 ? LOG2E / 16.0f : 1.0f;
    const __amdgpu_buffer_rsrc_t ors = __builtin_amdgcn_make_buffer_rsrc(p.ws, 0, (int)WS_END, 0x00020000);
#pragma unroll
    for (int y = 0; y < 4; ++y) {
        v4u bfr[8];
#pragma unroll
        for (int es = 0; es < 8; ++es) {
            if constexpr (mat == 0) {
                bfr[es] = *(const LAS v4u*)(L + (16 * (y >> 1) + 2 * (y & 1) + 4 * (n >> 2) + 32 * ((n >> 1) & 1) + (n & 1)) * K1_RS + es * 64 + g * 16);
            } else {
                const LAS unsigned char* rp = L + (32 * es + 8 * g + (n >> 2)) * K1_OS + y * 32 + (l & 3) * 8;
                const v2u lo = tr_read(rp), hi = tr_read(rp + 4 * K1_OS);
                bfr[es] = v4u{lo.x, lo.y, hi.x, hi.y};
            }
        }
        f32x4 acc[4];
#pragma unroll
        for (int x = 0; x < 4; ++x) acc[x] = f32x4{0.f, 0.f, 0.f, 0.f};
#pragma unroll
        for (int es = 0; es < 8; ++es)
#pragma unroll
            for (int x = 0; x < 4; ++x) acc[x] = mfma32(cr[x][es], bfr[es], acc[x]);
        const int d = mat == 0 ? (int)OFF_TW + ((h * 28 + 4 * blk + y) * 7 + w) * 2048 : (int)OFF_YW + (((4 * blk + y) * 16 + h) * 7 + w) * 2048;
#pragma unroll
        for (int a = 0; a < 2; ++a) {
            const f32x4 lo = acc[2 * a] * SC, hi = acc[2 * a + 1] * SC;
            stc16(v4u{pack2(lo[0], lo[1]), pack2(lo[2], lo[3]), pack2(hi[0], hi[1]), pack2(hi[2], hi[3])}, ors, l * 16, d + a * 1024);
        }
        if (y == 1) __builtin_amdgcn_sched_barrier(0);
    }
    PT0(2);
    PT(3);
}

__global__ void __launch_bounds__(512, 2) k_prep(Params p) {
    extern __shared__ __attribute__((aligned(16))) unsigned char lds_[];
    LAS unsigned char* L = (LAS unsigned char*)lds_;
    p.ws = align_ws(p.ws);
    const int b = blockIdx.x, tid = threadIdx.x;
    const int l = tid & 63, w = __builtin_amdgcn_readfirstlane(tid >> 6);
    PT0(0);
    if (b >= 224) {
        const int k = b - 224;
        if ((k & 7) == 0 && w < 2) {
            const int pg = (k >> 3) + 4 * w;
            if (pg < NPAGE && l == 0) {
                GAS unsigned* a = (GAS unsigned*)(p.ws + OFF_HOP + (size_t)pg * PAGE + 126 * CH);
                unsigned best[4] = {~0u, ~0u, ~0u, ~0u};
                for (int it = 0; it < 3; ++it) {
#pragma unroll
                    for (int q = 0; q < 4; ++q) {
                        const unsigned long long t0 = __builtin_amdgcn_s_memtime();
                        __hip_atomic_store(a + q * 1024, 0xFFFFFFFFu, __ATOMIC_RELAXED, __HIP_MEMORY_SCOPE_AGENT);
                        asm volatile("s_waitcnt vmcnt(0)" ::: "memory");
                        const unsigned dt = (unsigned)(__builtin_amdgcn_s_memtime() - t0);
                        best[q] = min(best[q], dt);
                    }
                }
                ((unsigned*)(p.ws + OFF_CTL))[pg] = ((best[0] + best[1] > best[2] + best[3]) ? 1u : 0u) ^ ((xcc_id() >> 2) & 1u);
            }
            PT(3);
            if (pg == 7) {
                if (l == 0) ((unsigned*)(p.ws + OFF_CTL))[8] = xcc_id() & 7u;
                for (int i = l; i < (int)(CTL_BYTES - 256) / 16; i += 64) stg16(p.ws + OFF_CTL + 256 + i * 16, v4u{0u, 0u, 0u, 0u});
            }
            return;
        }
        if ((k & 7) == 0) return;
        const int kk = k - (k >> 3) - 1;
        misc_wave<false>(p, kk * 8 + w, l);
        PT(3);
        return;
    }
    const int r = b >> 3, h = (b & 7) + 8 * (r & 1), q = r >> 1;
    if (q < 7) prep_heavy<0>(p, L, h, q, w); else prep_heavy<1>(p, L, h, q - 7, w);
}

constexpr int L_MST = 0, L_TIM = L_MST + 2 * 7168, L_RED = L_TIM + 8192, L_REC_END = 96 * 1024;
#define DLY_M 2040
#define DM0 18
#define DLY_P 30
#define DLY_PO 0
#define PPOSY 8
#define PRIO_X 1
#define PRIO_L 1
#define DLY_S 100
#define DLY_SO 40
#define UPOS 0
#define YPOS 0
#define ORD 2
#define PACE_G 0
#define PACE_U 0
#define PACE_Z 0
#define GAP_M 150
#define DLY_G 300
#define DLY_3 130
#define DLY_4 60
#define DLY_V 240
#define DLY_Y 0
#define PPOS 8
#define DLY_Q 200
#define SPA 0
#define SPB 2
#define SW_DLY 60
#define STAG 1
__device__ __forceinline__ void delay_var(int n) {
    asm volatile("s_cmp_lt_i32 %0, 1\n\ts_cbranch_scc1 2f\n1:\n\ts_nop 5\n\ts_sub_i32 %0, %0, 1\n\ts_cmp_gt_i32 %0, 0\n\ts_cbranch_scc1 1b\n2:" : "+s"(n) :: "scc", "memory");
}
template <int C> __device__ __forceinline__ void delay_cycles() {
    if constexpr (C >= 64) __builtin_amdgcn_s_sleep(C / 64 > 127 ? 127 : C / 64);
    if constexpr ((C & 63) >= 4) asm volatile("s_nop %0" :: "i"(((C & 63) >> 2) - 1));
}

__device__ __forceinline__ bool fresh(v4u x) {
    const unsigned m = max(max(x.x, x.y), max(x.z, x.w));
    return __all(m != 0xFFFFFFFFu);
}
#define BAR() do { asm volatile("s_waitcnt lgkmcnt(0)" ::: "memory"); __builtin_amdgcn_s_barrier(); asm volatile("" ::: "memory"); } while (0)
#define FENCE() do { asm volatile("" ::: "memory"); __builtin_amdgcn_sched_barrier(0); } while (0)

#define STAMP(k) do {} while (0)
#define PROF_DECL
#define PROF_ACC do {} while (0)
#define PROF_OUT(slot) do {} while (0)
typedef _Float16 f16x2 __attribute__((ext_vector_type(2)));
__device__ __forceinline__ unsigned packh(float a, float b) { return __builtin_bit_cast(unsigned, __builtin_amdgcn_cvt_pkrtz(a, b)); }
__device__ __forceinline__ float hlo(unsigned v) { const f16x2 h = __builtin_bit_cast(f16x2, v); return (float)h.x; }
__device__ __forceinline__ float hhi(unsigned v) { const f16x2 h = __builtin_bit_cast(f16x2, v); return (float)h.y; }
constexpr unsigned SPIN_CAP = 1u << 20;

template <bool COLFOLD>
__device__ __forceinline__ unsigned fold_pack(f32x4 a, unsigned sel) {
    unsigned r;
    if constexpr (COLFOLD)
        asm("s_nop 7\n\tv_permlane32_swap_b32 v200, v202\n\tv_permlane32_swap_b32 v201, v203\n\tv_add_f32 v200, v200, v202\n\tv_add_f32 v201, v201, v203\n\t"
            "s_nop 0\n\tv_add_f32_dpp v200, v200, v200 row_ror:8 row_mask:0xf bank_mask:0xf\n\tv_add_f32_dpp v201, v201, v201 row_ror:8 row_mask:0xf bank_mask:0xf\n\t"
            "v_cvt_pkrtz_f16_f32 v200, v200, v201\n\ts_nop 1\n\tv_mov_b32_dpp v201, v200 quad_perm:[1,0,3,2] row_mask:0xf bank_mask:0xf\n\t"
            "v_perm_b32 %0, v201, v200, %2\n\tv_min_u32 %0, -2, %0" : "=v"(r), "+{v[200:203]}"(a) : "v"(sel));
    else
        asm("s_nop 7\n\tv_permlane32_swap_b32 v200, v202\n\tv_permlane32_swap_b32 v201, v203\n\tv_add_f32 v200, v200, v202\n\tv_add_f32 v201, v201, v203\n\t"
            "v_cvt_pkrtz_f16_f32 v200, v200, v201\n\ts_nop 1\n\tv_mov_b32_dpp v201, v200 quad_perm:[1,0,3,2] row_mask:0xf bank_mask:0xf\n\t"
            "v_perm_b32 %0, v201, v200, %2\n\tv_min_u32 %0, -2, %0" : "=v"(r), "+{v[200:203]}"(a) : "v"(sel));
    return r;
}
__device__ __forceinline__ unsigned pkmaxh(unsigned a, unsigned b) { unsigned r; asm("v_pk_max_f16 %0, %1, %2" : "=v"(r) : "v"(a), "v"(b)); return r; }
__device__ __forceinline__ float vmax(float a, float b) { float r; asm("v_max_f32 %0, %1, %2" : "=v"(r) : "v"(a), "v"(b)); return r; }
__device__ __forceinline__ float xg_max_raw(float v) {
    const v2u a = __builtin_amdgcn_permlane16_swap(fbits(v), fbits(v), false, false);
    v = vmax(bitsf(a.x), bitsf(a.y));
    const v2u b = __builtin_amdgcn_permlane32_swap(fbits(v), fbits(v), false, false);
    return vmax(bitsf(b.x), bitsf(b.y));
}
__device__ __forceinline__ float addh_lo(unsigned a, unsigned b) { float r; asm("v_fma_mix_f32 %0, %1, 1.0, %2 op_sel_hi:[1,0,1]" : "=v"(r) : "v"(a), "v"(b)); return r; }
__device__ __forceinline__ float addh_hi(unsigned a, unsigned b) { float r; asm("v_fma_mix_f32 %0, %1, 1.0, %2 op_sel:[1,0,1] op_sel_hi:[1,0,1]" : "=v"(r) : "v"(a), "v"(b)); return r; }
__device__ __forceinline__ float subh_lo(unsigned a, float b) { float r; asm("v_fma_mix_f32 %0, %1, 1.0, -%2 op_sel_hi:[1,0,0]" : "=v"(r) : "v"(a), "v"(b)); return r; }
__device__ __forceinline__ float subh_hi(unsigned a, float b) { float r; asm("v_fma_mix_f32 %0, %1, 1.0, -%2 op_sel:[1,0,0] op_sel_hi:[1,0,0]" : "=v"(r) : "v"(a), "v"(b)); return r; }
__device__ __forceinline__ float add_ror8(float a) { float r; asm("s_nop 1\n\tv_add_f32_dpp %0, %1, %1 row_ror:8 row_mask:0xf bank_mask:0xf" : "=v"(r) : "v"(a)); return r; }
#define UNLIKELY(x) __builtin_expect(!!(x), 0)
#define CNT(x) do {} while (0)

#define TLS(k) do {} while (0)
#define TLW(k) do {} while (0)
template <int NG, int STR>
__device__ __forceinline__ int wait_ready(const __amdgpu_buffer_rsrc_t rs, const int g0, const int need, unsigned& mspin) {
    const int l = threadIdx.x & 63;
    int ready;
    for (;;) {
        asm volatile("" ::: "memory");
        const int v = (int)__builtin_amdgcn_raw_buffer_load_b32(rs, CT_PROG + (g0 * 3 + (l < 3 * NG ? l : 0)) * 128, 0, 16);
        ready = 1 << 20;
#pragma unroll
        for (int k = 0; k < 3 * NG; ++k) if (k % 3 < STR) ready = min(ready, STR * __builtin_amdgcn_readlane(v, k) + (k % 3));
        if (ready > need || ++mspin >= SPIN_CAP) break;
        __builtin_amdgcn_s_sleep(2);
    }
    asm volatile("" ::: "memory");
    return ready;
}

template <int ROLE, int DLY1>
__device__ __forceinline__ void score_wave(const Params& p, LAS unsigned char* L, const int u, const int w) {
    constexpr bool SPECIAL = ROLE == 2;
    const int l = threadIdx.x & 63;
    const int n = l & 15, g = l >> 4, i8 = l & 7, hh = (l >> 3) & 1;
    const int half = u & 1;
    const __amdgpu_buffer_rsrc_t rs = __builtin_amdgcn_make_buffer_rsrc(p.ws, 0, (int)WS_END, 0x00020000);
    const int idx = hh ? 0xEEEE : 0x4444;
    const unsigned parmask = load_parmask(p.ws);
    v8u tw[SPECIAL ? 1 : 14];
    v4u gx[SPECIAL ? 14 : 1];
    const LAS unsigned char* mrd[SPECIAL ? 1 : 7];
    if constexpr (!SPECIAL) {
        const unsigned char* src = p.ws + OFF_TW + ((size_t)(u * 14 + 2 * w) * 7) * 2048 + l * 16;
#pragma unroll
        for (int k = 0; k < 7; ++k) {
            const int f = w + k < 7 ? w + k : w + k - 7;
            mrd[k] = L + L_MST + f * 1024 + l * 16;
#pragma unroll
            for (int t = 0; t < 2; ++t) {
                const v4u a = ldg16(src + (7 * t + f) * 2048), b = ldg16(src + (7 * t + f) * 2048 + 1024);
                tw[7 * t + k] = v8u{a.x, a.y, a.z, a.w, b.x, b.y, b.z, b.w};
            }
        }
    } else {
        *(LAS v4u*)(L + L_TIM + (half ? 0 : 14 * 512) + l * 16) = v4u{0u, 0u, 0u, 0u};
        __builtin_amdgcn_s_setprio(2);
    }
    const int moff = C_M + w * 1024 + hh * 512 + g * 128 + i8 * 16;
    const int sst_off = C_S + hh * 4096 + u * 128 + (4 * (g & 1) + 2 * (g >> 1) + (l & 1)) * 16 + (i8 >> 1) * 4;
    const unsigned ssel = (l & 1) ? 0x03020706u : 0x05040100u;
    const int pst_off = C_P + u * 256 + (4 * (g & 1) + 2 * (g >> 1) + (l & 1)) * 32 + (n >> 1) * 4;
    const int goff = ((u & ~1) + ((n >> 2) & 1)) * 14336 + (8 * half + 4 * (n >> 3) + (n & 3) + 16 * g) * 16;
    const int xoff = (int)OFF_XP + 7 * half * 1024 + l * 16;
    unsigned spins = 0, mspin = 0;
    int ready = 0;
    if constexpr (!SPECIAL) {
#pragma unroll
        for (int k = 0; k < 7; ++k) asm volatile("" : "+v"(mrd[k]));
    }
    v4u xq[14], x3 = v4u{0u, 0u, 0u, 0u};
    if constexpr (ROLE == 1) {
#pragma unroll
        for (int k = 0; k < 14; ++k) xq[k] = __builtin_amdgcn_raw_buffer_load_b128(rs, xoff + ((k / 7) * 14 + k % 7) * 1024, (NSEG - 1) * 28672, 0);
    }
    if constexpr (SPECIAL) {
        ready = wait_ready<2, 2>(rs, u & ~1, 0, mspin);
        TLW(1);
#pragma unroll
        for (int f = 0; f < 14; ++f) gx[f] = __builtin_amdgcn_raw_buffer_load_b128(rs, goff + f * 1024, (int)OFF_GF, 16);
    }
    PROF_DECL
    for (int s = 0; s < NSEG; ++s) {
        const int ho = hop_off(s, parmask);
        FENCE();
        STAMP(0);
        if constexpr (!SPECIAL) {
            if (s) delay_cycles<DLY_M>();
            FENCE();
            v4u xs = __builtin_amdgcn_raw_buffer_load_b128(rs, moff, ho, 16);
            FENCE(); delay_cycles<GAP_M>(); FENCE();
            const v4u x1 = __builtin_amdgcn_raw_buffer_load_b128(rs, moff, ho + CH, 16);
            FENCE(); delay_cycles<GAP_M>(); FENCE();
            const v4u x2 = __builtin_amdgcn_raw_buffer_load_b128(rs, moff, ho + 2 * CH, 16);
            FENCE();
#define MOV4(d_, s_) do { asm volatile("v_mov_b64 %0, %1" : "=v"(d_.lo) : "v"(s_.lo)); asm volatile("v_mov_b64 %0, %1" : "=v"(d_.hi) : "v"(s_.hi)); } while (0)
            f32x4 a0 = {0.f, 0.f, 0.f, 0.f}, a1 = a0;
            asm volatile("" : "+v"(a0), "+v"(a1));
            FENCE();
            *(LAS v4u*)mrd[0] = xs;
            FENCE();
            if (UNLIKELY(!fresh(xs))) {
                FENCE();
                x3 = __builtin_amdgcn_raw_buffer_load_b128(rs, moff, ho + 3 * CH, 16);
                FENCE();
                CNT(1);
                MOV4(xs, x1);
                if (!fresh(xs)) {
                    CNT(1);
                    MOV4(xs, x2);
                    if (!fresh(xs)) {
                        CNT(1);
                        MOV4(xs, x3);
                        if (!fresh(xs)) {
                            for (;;) {
                                asm volatile("" ::: "memory");
                                xs = __builtin_amdgcn_raw_buffer_load_b128(rs, moff, ho + 3 * CH, 16);
                                CNT(1);
                                if (fresh(xs) || ++mspin >= SPIN_CAP) break;
                            }
                        }
                    }
                }
                *(LAS v4u*)mrd[0] = xs;
            }
            STAMP(1);
            FENCE();
            a0 = smfmac(xs, tw[0], a0, idx); a1 = smfmac(xs, tw[7], a1, idx);
            asm volatile("" : "+v"(a0), "+v"(a1));
            FENCE();
            BAR();
            STAMP(2);
            v4u mp[7];
            delay_cycles<DLY1>();
            FENCE();
#pragma unroll
            for (int k = 1; k < 7; ++k) mp[k] = *(const LAS v4u*)mrd[k];
            FENCE();
#pragma unroll
            for (int k = 1; k < 7; ++k) { a0 = smfmac(mp[k], tw[k], a0, idx); a1 = smfmac(mp[k], tw[7 + k], a1, idx); }
            *(LAS v2u*)(L + L_TIM + (2 * w + 2 * half) * 512 + n * 32 + (g ^ (n >> 2)) * 8) = v2u{pack2(a0[0], a0[1]), pack2(a0[2], a0[3])};
            *(LAS v2u*)(L + L_TIM + (2 * w + 2 * half + 1) * 512 + n * 32 + (g ^ (n >> 2)) * 8) = v2u{pack2(a1[0], a1[1]), pack2(a1[2], a1[3])};
            STAMP(3);
            BAR();
            STAMP(4);
            asm volatile("" : "+v"(x3) : "v"(x1), "v"(x2));
            TLS(8 + s);
            if (ROLE == 1 && s == NSEG - 1) {
                v4u ta[7];
#pragma unroll
                for (int k = 0; k < 7; ++k) {
                    const v2u t0 = tr_read(L + L_TIM + (2 * k + 2 * half) * 512 + (l ^ g) * 8), t1 = tr_read(L + L_TIM + (2 * k + 2 * half + 1) * 512 + (l ^ g) * 8);
                    ta[k] = v4u{t0.x, t0.y, t1.x, t1.y};
                }
                FENCE();
                f32x4 d0 = {0.f, 0.f, 0.f, 0.f}, d1 = d0;
#pragma unroll
                for (int k = 0; k < 7; ++k) { d0 = mfma32(ta[k], xq[k], d0); d1 = mfma32(ta[k], xq[7 + k], d1); }
                stc16(v4u{sane(fbits(fold32(d0[0], d0[2]))), sane(fbits(fold32(d0[1], d0[3]))),
                                                           sane(fbits(fold32(d1[0], d1[2]))), sane(fbits(fold32(d1[1], d1[3])))},
                                                       rs, l * 16, (int)OFF_SXM + (s * 32 + u) * 1024);
                FENCE();
            }
        } else {
            f32x4 ae = {0.f, 0.f, 0.f, 0.f};
            asm volatile("" : "+v"(ae));
            BAR();
            STAMP(2);
            v4u mp[7];
#pragma unroll
            for (int f = 0; f < 7; ++f) mp[f] = *(const LAS v4u*)(L + L_MST + f * 1024 + l * 16);
            FENCE();
            v4u m2[4];
#pragma unroll
            for (int f = 0; f < 7; ++f) {
                const v4u ga = gx[2 * f], gb = gx[2 * f + 1];
                ae = smfmac(mp[f], v8u{ga.x, ga.y, ga.z, ga.w, gb.x, gb.y, gb.z, gb.w}, ae, idx);
            }
            const unsigned pv = fold_pack<false>(ae, ssel);
            __builtin_amdgcn_raw_buffer_store_b32(pv, rs, pst_off, ho, 16);
            __builtin_amdgcn_raw_buffer_store_b32(pv, rs, pst_off, ho + CH, 16);
            FENCE();
#pragma unroll
            for (int b = 0; b < 4; ++b) m2[b] = *(const LAS v4u*)(L + L_MST + (3 * half + b) * 1024 + l * 16);
            FENCE();
            STAMP(3);
            f32x4 sa = {0.f, 0.f, 0.f, 0.f};
            asm volatile("" : "+v"(sa));
            BAR();
            STAMP(4);
            v8u tb[4];
#pragma unroll
            for (int b = 0; b < 4; ++b) {
                const v2u t0 = tr_read(L + L_TIM + (4 * b) * 512 + (l ^ g) * 8), t1 = tr_read(L + L_TIM + (4 * b + 1) * 512 + (l ^ g) * 8),
                          t2 = tr_read(L + L_TIM + (4 * b + 2) * 512 + (l ^ g) * 8), t3 = tr_read(L + L_TIM + (4 * b + 3) * 512 + (l ^ g) * 8);
                tb[b] = v8u{t0.x, t0.y, t1.x, t1.y, t2.x, t2.y, t3.x, t3.y};
            }
            FENCE();
#pragma unroll
            for (int b = 0; b < 4; ++b) sa = smfmac(m2[b], tb[b], sa, idx);
            __builtin_amdgcn_raw_buffer_store_b32(fold_pack<true>(sa, ssel), rs, sst_off, ho, 16);
            FENCE();
            STAMP(5);
            delay_cycles<DLY_G>();
            if (UNLIKELY(ready <= s + 1 && s + 1 < NSEG)) ready = wait_ready<2, 2>(rs, u & ~1, s + 1, mspin);
            FENCE();
#pragma unroll
            for (int f = 0; f < 14; ++f) gx[f] = __builtin_amdgcn_raw_buffer_load_b128(rs, goff + f * 1024, (int)(OFF_GF + (s + 1 < NSEG ? s + 1 : s) * SEGB), 16);
            FENCE();
            if (UNLIKELY(ready <= s + 3 && ready < NSEG)) ready = wait_ready<2, 2>(rs, u & ~1, -1, mspin);
        }
        PROF_ACC;
    }
    PROF_OUT(u * 8 + w);
}

template <bool LAST, bool YOUNG>
__device__ __forceinline__ void value_wave(const Params& p, LAS unsigned char* L, const int v, const int w) {
    const int l = threadIdx.x & 63;
    const int g = l >> 4, i8 = l & 7, hh = (l >> 3) & 1;
    const __amdgpu_buffer_rsrc_t rs = __builtin_amdgcn_make_buffer_rsrc(p.ws, 0, (int)WS_END, 0x00020000);
    const int idx = hh ? 0xEEEE : 0x4444;
    const unsigned parmask = load_parmask(p.ws);
    v8u yw[14];
    const LAS unsigned char* mrd[7];
    {
        const unsigned char* src = p.ws + OFF_YW + (size_t)(v * 8 + w) * 14 * 2048 + l * 16;
#pragma unroll
        for (int k = 0; k < 7; ++k) {
            const int f = w + k < 7 ? w + k : w + k - 7;
            mrd[k] = L + L_MST + f * 1024 + l * 16;
#pragma unroll
            for (int t = 0; t < 2; ++t) {
                const v4u a = ldg16(src + (7 * t + f) * 2048), b = ldg16(src + (7 * t + f) * 2048 + 1024);
                yw[7 * t + k] = v8u{a.x, a.y, a.z, a.w, b.x, b.y, b.z, b.w};
            }
        }
    }
    if (LAST && PRIO_L) __builtin_amdgcn_s_setprio(2);
    if (YOUNG && PRIO_X == 3) __builtin_amdgcn_s_setprio(3);
    const int moff = C_M2 + w * 1024 + hh * 512 + g * 128 + i8 * 16;
    const int mst_off = C_M + v * 256 + ((l >> 2) & 1) * 128 + (l >> 3) * 16 + (l & 3) * 4;
    const int soff = C_S + ((2 * w + hh) * 2 + (g >> 1)) * 128 + i8 * 16 + (g & 1) * 8;
    const int poff = C_P + ((2 * w + hh) * 2 + (g >> 1)) * 256 + i8 * 32 + (g & 1) * 16;
    const int yst_off = (int)OFF_YM + (v * 8 + w) * 512 + l * 8;
    const int uoff = (v * 8 + w) * 2048 + l * 16;
    unsigned mlo = hh ? 0u : 0xFFFFFFFFu, mhi = ~mlo;
    v4u ones = v4u{0x3F803F80u, 0x3F803F80u, 0x3F803F80u, 0x3F803F80u};
    asm volatile("" : "+v"(mlo), "+v"(mhi), "+v"(ones));
#pragma unroll
    for (int k = 0; k < 7; ++k) asm volatile("" : "+v"(mrd[k]));
    unsigned spins = 0, mspin = 0;
    PROF_DECL
    int ho = hop_off(0, parmask);
    int ready = wait_ready<1, 3>(rs, 32 + v, 0, mspin);
    TLS(1);
    v4u ua = __builtin_amdgcn_raw_buffer_load_b128(rs, uoff, (int)OFF_UF, 16);
#define V_Y_PHASE(MIDA, MIDB) \
        f32x4 y0 = {0.f, 0.f, 0.f, 0.f}, y1 = y0; \
        if constexpr (!LAST) { \
            delay_cycles<DLY_V>(); \
            FENCE(); \
            v4u x = __builtin_amdgcn_raw_buffer_load_b128(rs, moff, ho, 16); \
            asm volatile("" : "+v"(y0), "+v"(y1)); \
            FENCE(); \
            *(LAS v4u*)mrd[0] = x; \
            FENCE(); \
            if (UNLIKELY(!fresh(x))) { \
                for (;;) { asm volatile("" ::: "memory"); x = __builtin_amdgcn_raw_buffer_load_b128(rs, moff, ho, 16); CNT(1); if (fresh(x) || ++mspin >= SPIN_CAP) break; } \
                *(LAS v4u*)mrd[0] = x; \
            } \
            STAMP(1); \
            FENCE(); \
            y0 = smfmac(x, yw[0], y0, idx); y1 = smfmac(x, yw[7], y1, idx); \
            asm volatile("" : "+v"(y0), "+v"(y1)); \
            FENCE(); \
        } \
        BAR(); \
        STAMP(2); \
        v4u mp[7]; \
        if constexpr (YOUNG) delay_cycles<DLY_Y>(); \
        if constexpr (LAST) asm volatile("s_waitcnt vmcnt(0)" ::: "memory");       \
        FENCE(); \
        _Pragma("unroll") for (int k = LAST ? 0 : 1; k < 7; ++k) mp[k] = *(const LAS v4u*)mrd[k]; \
        FENCE(); \
        MIDA; \
        _Pragma("unroll") for (int k = LAST ? 0 : 1; k < 7; ++k) { \
            if (k == (YOUNG ? PPOSY : PPOS)) { MIDB; } \
            y0 = smfmac(mp[k], yw[k], y0, idx); y1 = smfmac(mp[k], yw[7 + k], y1, idx); \
        } \
        if ((YOUNG ? PPOSY : PPOS) == 7) { MIDB; } \
        v2u yop = v2u{pack2(fold32(y0[0], y0[2]), fold32(y0[1], y0[3])), pack2(fold32(y1[0], y1[2]), fold32(y1[1], y1[3]))}; \
        asm volatile("" : "+v"(yop)); \
        FENCE(); \
        STAMP(3)
    for (int s = 0; s < NSEG - 1; ++s) {
        const int hn = hop_off(s + 1, parmask);
        const int uo = (int)(OFF_UF + (s + 1) * SEGB);
        if constexpr (LAST) {
            if (UNLIKELY(ready <= s + 3 && ready < NSEG)) ready = wait_ready<1, 3>(rs, 32 + v, s + 1, mspin);
            if (UNLIKELY((s + 2) % SPP == 0)) {
                const int sh = 4 * ((s + 2) / SPP - 1);
                for (;;) {
                    asm volatile("" ::: "memory");
                    const unsigned c = __builtin_amdgcn_readfirstlane(__builtin_amdgcn_raw_buffer_load_b32(rs, CT_PZ, 0, 16));
                    if (((c >> sh) & 15u) >= 6u || ++mspin >= SPIN_CAP) break;
                }
            }
        }
        int ho1 = ho + CH, ho2 = ho + 4096, hn1 = hn + CH, hn2 = hn + 2 * CH, hn3 = hn + 3 * CH, hnn = hn;
        asm volatile("" : "+s"(ho1), "+s"(ho2), "+s"(hn1), "+s"(hn2), "+s"(hn3), "+s"(hnn));
        FENCE();
        STAMP(0);
        v4u ub, pv;
        V_Y_PHASE(ub = __builtin_amdgcn_raw_buffer_load_b128(rs, uoff + 1024, uo - (int)SEGB, 16); FENCE(),
                  FENCE(); pv = __builtin_amdgcn_raw_buffer_load_b128(rs, poff, ho, 16); FENCE());
        if ((YOUNG ? PPOSY : PPOS) == 8) { delay_cycles<YOUNG ? DLY_P : DLY_P + DLY_PO>(); FENCE(); pv = __builtin_amdgcn_raw_buffer_load_b128(rs, poff, ho, 16); FENCE(); }
        delay_cycles<DLY_Q>();
        FENCE();
        const v4u pq = __builtin_amdgcn_raw_buffer_load_b128(rs, poff, ho1, 16);
        FENCE();
        v2u sv, sw2 = v2u{0xFFFFFFFFu, 0xFFFFFFFFu};
#define S_A() do { FENCE(); STAMP(6); sv = __builtin_amdgcn_raw_buffer_load_b64(rs, soff, ho, 16); FENCE(); } while (0)
#define S_B() do { FENCE(); sw2 = __builtin_amdgcn_raw_buffer_load_b64(rs, soff, ho2, 16); FENCE(); } while (0)
        if (SPA == -1) { delay_cycles<YOUNG ? DLY_S : DLY_S + DLY_SO>(); S_A(); }
#define X_MAX() do { const unsigned hm = pkmaxh(pkmaxh(pv.x, pv.y), pkmaxh(pv.z, pv.w)); mx = xg_max_raw(vmax(hlo(hm), hhi(hm))); } while (0)
        float mx;
        asm volatile("" : "+v"(pv));
        S_A();
        if (YOUNG) __builtin_amdgcn_s_setprio(3);
        const bool pok = fresh(pv);
        X_MAX();
        asm volatile("" : "+v"(mx));
        FENCE();
        if (UNLIKELY(!pok)) {
            CNT(1 << 20);
            asm volatile("v_mov_b64 %0, %1" : "=v"(pv.lo) : "v"(pq.lo)); asm volatile("v_mov_b64 %0, %1" : "=v"(pv.hi) : "v"(pq.hi));
            if (!fresh(pv)) {
                for (;;) {
                    asm volatile("" ::: "memory");
                    pv = __builtin_amdgcn_raw_buffer_load_b128(rs, poff, ho1, 16);
                    CNT(1 << 20);
                    if (fresh(pv) || ++mspin >= SPIN_CAP) break;
                }
            }
            X_MAX();
        }
        STAMP(7);
        if (SPA == 1) S_A();
        if (SPB == 1) S_B();
        const unsigned e0 = pack2(ex2(subh_lo(pv.x, mx)), ex2(subh_hi(pv.x, mx))), e1 = pack2(ex2(subh_lo(pv.y, mx)), ex2(subh_hi(pv.y, mx)));
        if (SPA == 2) S_A();
        if (SPB == 2) S_B();
        if (PRIO_X == 1 && YOUNG) __builtin_amdgcn_s_setprio(0);
        if (PRIO_X == 2 && YOUNG) __builtin_amdgcn_s_setprio(3);
        const unsigned e2 = pack2(ex2(subh_lo(pv.z, mx)), ex2(subh_hi(pv.z, mx))), e3 = pack2(ex2(subh_lo(pv.w, mx)), ex2(subh_hi(pv.w, mx)));
        const f32x4 z4 = {0.f, 0.f, 0.f, 0.f};
        const v4u eop = v4u{e0, e1, e2, e3};
        const f32x4 oa = mfma32(ua, eop, z4);
        const f32x4 ob = mfma32(ub, eop, z4);
        const f32x4 lx4 = mfma32(ones, eop, z4);
        if (SPB == 3) S_B();
        FENCE();
        STAMP(8);
#define S_UNPACK() do { const v2u sw = __builtin_amdgcn_permlane32_swap(sv.x, sv.y, false, false); t0 = addh_lo(sw.x, sw.y); t1 = addh_hi(sw.x, sw.y); \
                        asm("v_max3_f32 %0, %1, %2, %3" : "=v"(m) : "v"(t0), "v"(t1), "v"(mx)); m = xg_max_raw(m); } while (0)
        float t0, t1, m;
        const bool sok = __all(max(sv.x, sv.y) != 0xFFFFFFFFu);
        S_UNPACK();
        f32x4 ox = hh ? ob : oa;
        asm volatile("" : "+v"(m), "+v"(t0), "+v"(t1), "+v"(ox));
        FENCE();
        if (UNLIKELY(!sok)) {
            CNT(1 << 10);
            asm volatile("v_mov_b64 %0, %1" : "=v"(sv) : "v"(sw2));
            if (!__all(max(sv.x, sv.y) != 0xFFFFFFFFu)) {
                for (;;) {
                    asm volatile("" ::: "memory");
                    sv = __builtin_amdgcn_raw_buffer_load_b64(rs, soff, ho2, 16);
                    CNT(1 << 10);
                    if (__all(max(sv.x, sv.y) != 0xFFFFFFFFu) || ++mspin >= SPIN_CAP) break;
                }
            }
            S_UNPACK();
        }
        STAMP(9);
        const unsigned pk = pack2(ex2(t0 - m), ex2(t1 - m));
        const float beta = ex2(mx - m);
        const v2u bop = v2u{pk & mlo, pk & mhi};
        f32x4 lx = lx4;
        lx[0] *= beta;
        ox *= beta;
        FENCE();
        const f32x4 ls = mfma16(v2u{ones.x, ones.y}, bop, lx);
        f32x4 d = mfma16(yop, bop, ox);
        FENCE();
        const float r = __builtin_amdgcn_rcpf(ls[0]);
        d *= r;
        float r0, r1;
        asm("s_nop 1\n\tv_add_f32_dpp %0, %2, %2 row_ror:8 row_mask:0xf bank_mask:0x3\n\tv_add_f32_dpp %1, %3, %3 row_ror:8 row_mask:0xf bank_mask:0x3\n\t"
            "v_add_f32_dpp %0, %4, %4 row_ror:8 row_mask:0xf bank_mask:0xc\n\tv_add_f32_dpp %1, %5, %5 row_ror:8 row_mask:0xf bank_mask:0xc"
            : "=&v"(r0), "=&v"(r1) : "v"(d[0]), "v"(d[1]), "v"(d[2]), "v"(d[3]));
        *(LAS f32x2*)(L + L_RED + (w >> 1) * 1024 + i8 * 128 + ((g ^ (i8 & 3)) * 2 + hh) * 16 + (w & 1) * 8) = f32x2{r0, r1};
        if (PRIO_X == 2 && YOUNG) __builtin_amdgcn_s_setprio(0);
        STAMP(10);
        FENCE();
        if constexpr (!LAST) ua = __builtin_amdgcn_raw_buffer_load_b128(rs, uoff, uo, 16);
        FENCE();
        BAR();
        STAMP(11);
        if constexpr (LAST) {
            f32x4 q[4];
#pragma unroll
            for (int k = 0; k < 4; ++k) q[k] = *(const LAS f32x4*)(L + L_RED + k * 1024 + (l ^ (2 * ((l >> 3) & 3))) * 16);
            FENCE();
            const f32x2 ta = (q[0].lo + q[0].hi) + (q[1].lo + q[1].hi);
            const f32x2 tot = (ta + (q[2].lo + q[2].hi)) + (q[3].lo + q[3].hi);
            const unsigned mv = sane(pack2(tot[0], tot[1]));
            __builtin_amdgcn_raw_buffer_store_b32(mv, rs, mst_off, hnn, 16);
            __builtin_amdgcn_raw_buffer_store_b32(mv, rs, mst_off + C_M2, hnn, 16);
            __builtin_amdgcn_raw_buffer_store_b32(mv, rs, mst_off, hn1, 16);
            __builtin_amdgcn_raw_buffer_store_b32(mv, rs, mst_off, hn2, 16);
            __builtin_amdgcn_raw_buffer_store_b32(mv, rs, mst_off, hn3, 16);
            __builtin_amdgcn_raw_buffer_store_b32(mv, rs, mst_off, hn + C_M3, 16);
            FENCE();
            asm volatile("" :: "v"(sw2), "v"(pq));
            ua = __builtin_amdgcn_raw_buffer_load_b128(rs, uoff, uo, 16);
        } else {
            delay_cycles<256>();
            asm volatile("" :: "v"(sw2), "v"(pq));
        }
        __builtin_amdgcn_raw_buffer_store_b64(v2u{sane(yop.x), sane(yop.y)}, rs, yst_off, s * 224 * 512, 16);
        ho = hn;
        if constexpr (LAST) PROF_ACC;
    }
    {
        const int s = NSEG - 1;
        V_Y_PHASE(, );
        __builtin_amdgcn_raw_buffer_store_b64(v2u{sane(yop.x), sane(yop.y)}, rs, yst_off, s * 224 * 512, 16);
    }
    PROF_OUT(256 + v * 8 + w);
}

__device__ __forceinline__ void glds16(const void* gsrc, unsigned lds_dst) {
    unsigned keep;
    asm volatile("s_mov_b32 %0, m0\n\ts_mov_b32 m0, %2\n\ts_nop 0\n\tglobal_load_lds_dwordx4 %1, off\n\ts_mov_b32 m0, %0"
                 : "=&s"(keep) : "v"(gsrc), "s"(lds_dst) : "memory");
}
__device__ __forceinline__ v4u ldc16(const __amdgpu_buffer_rsrc_t rs, int voff, int soff) { return __builtin_amdgcn_raw_buffer_load_b128(rs, voff, soff, 16); }
__device__ __forceinline__ v4u pack8(f32x4 a, f32x4 b) { return v4u{pack2(a[0], a[1]), pack2(a[2], a[3]), pack2(b[0], b[1]), pack2(b[2], b[3])}; }
constexpr int L_XB = 0, XBUF = 28672;

template <bool GTYPE>
__device__ __forceinline__ void sweep(const Params& p, LAS unsigned char* L, const __amdgpu_buffer_rsrc_t rs, const int grp, const int r, const int w) {
    constexpr int STR = GTYPE ? 2 : 3;
    const int l = threadIdx.x & 63;
    const unsigned lbase = (unsigned)(size_t)L;
    const unsigned char* wsrc;
    int obase, ostride, osec;
    float oscale = 1.0f;
    if (GTYPE) {
        const int ks = 7 * (grp & 1) + w;
        if (w < 7) { wsrc = p.ws + OFF_TW + (size_t)((grp >> 1) * 14 + ks) * 28672; obase = (int)OFF_GF + ((grp >> 1) * 28 + ks) * 1024; ostride = (int)SEGB; osec = 14336; }
        else { wsrc = p.ws + OFF_QW + (size_t)grp * 28672; obase = (int)OFF_QF + (grp >> 4) * (2048 * 1024) + (grp & 15) * 1024; ostride = 32768; osec = 16384;
               if (grp < 16) oscale = LOG2E * 0.18898223650461362f; }
    } else {
        osec = 1024;
        if (grp < 60) { wsrc = p.ws + OFF_YW + (size_t)((grp - 32) * 8 + w) * 28672; obase = (int)OFF_UF + ((grp - 32) * 8 + w) * 2048; ostride = (int)SEGB; }
        else { wsrc = p.ws + OFF_QW + (size_t)(32 + (grp - 60) * 8 + w) * 28672; obase = (int)OFF_VF + ((grp - 60) * 8 + w) * 2048; ostride = 32768; }
    }
    const int npc = w < 4 ? 4 : 3, pc0 = w < 4 ? 4 * w : 16 + 3 * (w - 4);
    const unsigned char* xsrc = p.ws + OFF_XP + pc0 * 1024 + l * 16;
    if (r > 0) __builtin_amdgcn_s_sleep(SW_DLY);
    for (int k = 0; k < npc; ++k) glds16(xsrc + (size_t)r * XBUF + k * 1024, lbase + L_XB + (pc0 + k) * 1024);
    v4u wt[2][14];
#pragma unroll
    for (int f = 0; f < 14; ++f) { wt[0][f] = ldg16(wsrc + f * 1024 + l * 16); wt[1][f] = ldg16(wsrc + (14 + f) * 1024 + l * 16); }
#pragma unroll
    for (int f = 0; f < 14; ++f) asm volatile("" : "+v"(wt[0][f]), "+v"(wt[1][f]));
    asm volatile("s_waitcnt vmcnt(0)" ::: "memory");
    BAR();
    TLS(1);
    const int nseg = (NSEG + STR - 1 - r) / STR;
    for (int i = 0; i < nseg; ++i) {
        const int seg = r + STR * i;
        if (i + 1 < nseg) for (int k = 0; k < npc; ++k) glds16(xsrc + (size_t)(seg + STR) * XBUF + k * 1024, lbase + L_XB + ((i + 1) & 1) * XBUF + (pc0 + k) * 1024);
        const LAS unsigned char* xb = L + L_XB + (i & 1) * XBUF + l * 16;
        f32x4 a[2][2];
#pragma unroll
        for (int t = 0; t < 4; ++t) a[t >> 1][t & 1] = f32x4{0.f, 0.f, 0.f, 0.f};
#pragma unroll
        for (int f = 0; f < 14; ++f) {
            const v4u x0 = *(const LAS v4u*)(xb + f * 1024), x1 = *(const LAS v4u*)(xb + (14 + f) * 1024);
            if (GTYPE) {
                a[0][0] = mfma32(wt[0][f], x0, a[0][0]); a[0][1] = mfma32(wt[1][f], x0, a[0][1]);
                a[1][0] = mfma32(wt[0][f], x1, a[1][0]); a[1][1] = mfma32(wt[1][f], x1, a[1][1]);
            } else {
                a[0][0] = mfma32(x0, wt[0][f], a[0][0]); a[0][1] = mfma32(x0, wt[1][f], a[0][1]);
                a[1][0] = mfma32(x1, wt[0][f], a[1][0]); a[1][1] = mfma32(x1, wt[1][f], a[1][1]);
            }
        }
        v4u o0 = GTYPE ? pack8(a[0][0] * oscale, a[0][1] * oscale) : pack8(a[0][0], a[1][0]);
        v4u o1 = GTYPE ? pack8(a[1][0] * oscale, a[1][1] * oscale) : pack8(a[0][1], a[1][1]);
        stc16(o0, rs, l * 16, obase + seg * ostride);
        stc16(o1, rs, l * 16, obase + seg * ostride + osec);
        const bool drain = (i < 2 || i + 1 == nseg);
        if (drain) asm volatile("s_waitcnt vmcnt(0)" ::: "memory"); else asm volatile("s_waitcnt vmcnt(2)" ::: "memory");
        BAR();
        if (threadIdx.x == 0) __builtin_amdgcn_raw_buffer_store_b32((unsigned)(drain ? i + 1 : i), rs, CT_PROG + (grp * 3 + r) * 128, 0, 16);
        if (i == 0) TLS(2);
        if (i >= 1) { if (GTYPE) { if (PACE_G) __builtin_amdgcn_s_sleep(PACE_G); } else { if (PACE_U) __builtin_amdgcn_s_sleep(PACE_U); } }
    }
    if (threadIdx.x == 0) __hip_atomic_fetch_add((GAS unsigned*)(p.ws + CT_DONE), 1u, __ATOMIC_RELAXED, __HIP_MEMORY_SCOPE_AGENT);
    TLS(3);
}

__device__ __forceinline__ void serve(const Params& p, LAS unsigned char* L, const __amdgpu_buffer_rsrc_t rs, const int grp, const int w) {
    const int l = threadIdx.x & 63, n = l & 15, g = l >> 4;
    const int ks = 7 * (grp & 1) + w;
    v4u wt[2][14];
    {
        const unsigned char* wsrc = p.ws + OFF_TW + (size_t)((grp >> 1) * 14 + (w < 7 ? ks : 0)) * 28672;
        __builtin_amdgcn_s_sleep(100);
#pragma unroll
        for (int f = 0; f < 14; ++f) { wt[0][f] = ldg16(wsrc + f * 1024 + l * 16); wt[1][f] = ldg16(wsrc + (14 + f) * 1024 + l * 16); }
#pragma unroll
        for (int f = 0; f < 14; ++f) asm volatile("" : "+v"(wt[0][f]), "+v"(wt[1][f]));
    }
    const unsigned parmask = load_parmask(p.ws);
    unsigned mspin = 0;
    const int mrow = 4 * ((n >> 2) & 1) + 2 * (n >> 3) + (n & 1);
    const LAS unsigned char* mrd = L + (g >> 1) * 128 + mrow * 16 + (g & 1) * 8;
    const unsigned char* xr = p.ws + OFF_XP + (w < 7 ? ks : 0) * 1024 + l * 16;
    v4u mx_ = ldc16(rs, (w < 7 ? w : 0) * 1024 + l * 16, hop_off(0, parmask) + C_M3);
    for (int s = 0; s < NSEG - 1; ++s) {
        v4u xa, xb2;
        if (w < 7) {
            xa = ldg16(xr + (size_t)s * 28672); xb2 = ldg16(xr + (size_t)s * 28672 + 14336);
            const int ho = hop_off(s, parmask) + C_M3;
            while (!fresh(mx_) && ++mspin < SPIN_CAP) {
                __builtin_amdgcn_s_sleep(2);
                asm volatile("" ::: "memory");
                mx_ = ldc16(rs, w * 1024 + l * 16, ho);
            }
            *(LAS v4u*)(L + w * 1024 + l * 16) = mx_;
            FENCE();
            mx_ = ldc16(rs, w * 1024 + l * 16, hop_off(s + 1 < NSEG ? s + 1 : s, parmask) + C_M3);
        }
        BAR();
        if (w < 7) {
            v2u lo[14], hi[14];
#pragma unroll
            for (int f = 0; f < 14; ++f) {
                lo[f] = *(const LAS v2u*)(mrd + (f >> 1) * 1024 + (f & 1) * 256); hi[f] = *(const LAS v2u*)(mrd + (f >> 1) * 1024 + (f & 1) * 256 + 512);
            }
            FENCE();
            f32x4 a0 = {0.f, 0.f, 0.f, 0.f}, a1 = a0;
#pragma unroll
            for (int f = 0; f < 14; ++f) {
                const v4u mf = v4u{lo[f].x, hi[f].x, lo[f].y, hi[f].y};
                a0 = mfma32(wt[0][f], mf, a0); a1 = mfma32(wt[1][f], mf, a1);
            }
            const v4u gm = pack8(a0, a1);
            const f32x4 z4 = {0.f, 0.f, 0.f, 0.f};
            const f32x4 d0 = mfma32(gm, xa, z4), d1 = mfma32(gm, xb2, z4);
            *(LAS f32x4*)(L + 8192 + w * 1024 + l * 16) = f32x4{d0[0], d0[1], d1[0], d1[1]};
        }
        BAR();
        TLS(8 + s);
        if (w == 7) {
            f32x4 q[7];
#pragma unroll
            for (int k = 0; k < 7; ++k) q[k] = *(const LAS f32x4*)(L + 8192 + k * 1024 + l * 16);
            const f32x4 t = ((q[0] + q[1]) + (q[2] + q[3])) + ((q[4] + q[5]) + q[6]);
            stc16(v4u{sane(fbits(t[0])), sane(fbits(t[1])), sane(fbits(t[2])), sane(fbits(t[3]))}, rs, l * 16,
                                                   (int)OFF_SXM + (s * 32 + grp) * 1024);
        }
    }
}

__device__ __forceinline__ void attn_item(const Params& p, const __amdgpu_buffer_rsrc_t rs, const int item) {
    const int l = threadIdx.x & 63, n = l & 15, g = l >> 4;
    const int rt = item >> 4, h = item & 15;
    const int s = rt >> 1, q = rt * 16 + n;
    const v4u qf = ldc16(rs, l * 16, (int)OFF_QF + (rt * 16 + h) * 1024);
    f32x4 sc[18];
    const f32x4 z4 = {0.f, 0.f, 0.f, 0.f};
    const float NEG = -3.0e38f;
    v4u kf[18];
#pragma unroll
    for (int k = 0; k < 18; ++k) { const int kt = max(2 * (s - 8) + k, 0); kf[k] = ldc16(rs, l * 16, (int)OFF_KF + (kt * 16 + h) * 1024); }
#pragma unroll
    for (int k = 0; k < 18; ++k) {
        const int kt = 2 * (s - 8) + k;
        f32x4 a = mfma32(kf[k], qf, z4);
        if (k < 2 || k >= 16) {
#pragma unroll
            for (int r = 0; r < 4; ++r) {
                const int dq = q - (16 * kt + 4 * g + r);
                a[r] = (dq >= 0 && dq < 256) ? a[r] : NEG;
            }
        }
        sc[k] = kt >= 0 ? a : f32x4{NEG, NEG, NEG, NEG};
    }
    FENCE();
    v4u vf[18];
#pragma unroll
    for (int k = 0; k < 18; ++k) { const int b = max(s - 8 + (k >> 1), 0); vf[k] = ldc16(rs, l * 16, (int)OFF_VF + ((b * 16 + h) * 2 + (k & 1)) * 1024); }
    float m = NEG;
#pragma unroll
    for (int k = 0; k < 18; ++k) m = fmaxf(m, fmaxf(fmaxf(sc[k][0], sc[k][1]), fmaxf(sc[k][2], sc[k][3])));
    m = xg_max(m);
    float sum = 0.f;
    f32x4 o0 = z4, o1 = z4;
#pragma unroll
    for (int blk = 0; blk < 9; ++blk) {
        f32x4 e0, e1;
#pragma unroll
        for (int r = 0; r < 4; ++r) { e0[r] = ex2(sc[2 * blk][r] - m); e1[r] = ex2(sc[2 * blk + 1][r] - m); }
        sum += ((e0[0] + e0[1]) + (e0[2] + e0[3])) + ((e1[0] + e1[1]) + (e1[2] + e1[3]));
        const v4u pb = pack8(e0, e1);
        o0 = mfma32(vf[2 * blk], pb, o0);
        o1 = mfma32(vf[2 * blk + 1], pb, o1);
    }
    sum = xg_sum(sum);
    const float f = ((const float*)(p.ws + OFF_GATE))[q] / sum;
    stc16(pack8(o0 * f, o1 * f), rs, l * 16, (int)OFF_OF + (rt * 16 + h) * 1024);
    asm volatile("s_waitcnt vmcnt(0)" ::: "memory");
    if (l == 0) __hip_atomic_fetch_add((GAS unsigned*)(p.ws + CT_OFC + s * 128), 1u, __ATOMIC_RELAXED, __HIP_MEMORY_SCOPE_AGENT);
}

__device__ __forceinline__ void out_item(const Params& p, LAS unsigned char* L, const __amdgpu_buffer_rsrc_t rs, const int s, const int q4, const int w, unsigned& mspin) {
    const int l = threadIdx.x & 63, n = l & 15, g = l >> 4;
    const f32x4 z4 = {0.f, 0.f, 0.f, 0.f};
    const float NEG = -3.0e38f;
    const int v = 7 * q4 + (w < 7 ? w : 0);
    f32x4 sxx[2][3];
#pragma unroll
    for (int a = 0; a < 2; ++a) { sxx[a][0] = z4; sxx[a][1] = z4; sxx[a][2] = z4; }
    {
        const int gp = (int)OFF_GF + s * (int)SEGB + (2 * w) * 28 * 1024;
        const unsigned char* xr = p.ws + OFF_XP + (size_t)s * 28672 + l * 16;
#pragma unroll 1
        for (int kh = 0; kh < 2; ++kh) {
            v4u x0[7], x1[7], kk[4][7];
#pragma unroll
            for (int j = 0; j < 7; ++j) {
                const int ks = 7 * kh + j;
                x0[j] = ldg16(xr + ks * 1024); x1[j] = ldg16(xr + (14 + ks) * 1024);
#pragma unroll
                for (int t = 0; t < 4; ++t) kk[t][j] = ldc16(rs, l * 16, gp + (t * 14 + ks) * 1024);
            }
            FENCE();
#pragma unroll
            for (int j = 0; j < 7; ++j) {
                const v4u xa = x0[j], xb = x1[j];
#pragma unroll
                for (int a = 0; a < 2; ++a) {
                    sxx[a][0] = mfma32(kk[2 * a][j], xa, sxx[a][0]);
                    sxx[a][1] = mfma32(kk[2 * a][j], xb, sxx[a][1]);
                    sxx[a][2] = mfma32(kk[2 * a + 1][j], xb, sxx[a][2]);
                }
            }
            FENCE();
        }
    }
    if (w == 0) {
        for (;;) {
            asm volatile("" ::: "memory");
            const unsigned c = __builtin_amdgcn_raw_buffer_load_b32(rs, CT_OFC + s * 128, 0, 16);
            if (__builtin_amdgcn_readfirstlane(c) >= 32u || ++mspin >= SPIN_CAP) break;
            __builtin_amdgcn_s_sleep(4);
        }
    }
    BAR();
    f32x4 o0 = z4, o1 = z4;
    {
        const unsigned char* wp = p.ws + OFF_WOF + (size_t)(v * 16) * 1024 + l * 16;
#pragma unroll 1
        for (int hb = 0; hb < 16; hb += 8) {
            v4u wf[8], oa[8], ob[8];
#pragma unroll
            for (int h = 0; h < 8; ++h) {
                wf[h] = ldg16(wp + (hb + h) * 1024);
                oa[h] = ldc16(rs, l * 16, (int)OFF_OF + (2 * s * 16 + hb + h) * 1024);
                ob[h] = ldc16(rs, l * 16, (int)OFF_OF + ((2 * s + 1) * 16 + hb + h) * 1024);
            }
            FENCE();
#pragma unroll
            for (int h = 0; h < 8; ++h) { o0 = mfma32(wf[h], oa[h], o0); o1 = mfma32(wf[h], ob[h], o1); }
            FENCE();
        }
    }
    v4u uf[16];
#pragma unroll
    for (int h = 0; h < 16; ++h) uf[h] = ldc16(rs, l * 16, (int)OFF_UF + s * (int)SEGB + (v * 16 + h) * 1024);
    float gate0 = 1.0f - ((const float*)(p.ws + OFF_GATE))[32 * s + n], gate1 = 1.0f - ((const float*)(p.ws + OFF_GATE))[32 * s + 16 + n];
    float mxx[2][2], lxx[2][2];
#pragma unroll
    for (int a = 0; a < 2; ++a) {
#pragma unroll
        for (int r = 0; r < 4; ++r) { sxx[a][0][r] = (4 * g + r <= n) ? sxx[a][0][r] : NEG; sxx[a][2][r] = (4 * g + r <= n) ? sxx[a][2][r] : NEG; }
        mxx[a][0] = xg_max(fmaxf(fmaxf(sxx[a][0][0], sxx[a][0][1]), fmaxf(sxx[a][0][2], sxx[a][0][3])));
        mxx[a][1] = xg_max(fmaxf(fmaxf(fmaxf(sxx[a][1][0], sxx[a][1][1]), fmaxf(sxx[a][1][2], sxx[a][1][3])), fmaxf(fmaxf(sxx[a][2][0], sxx[a][2][1]), fmaxf(sxx[a][2][2], sxx[a][2][3]))));
#pragma unroll
        for (int r = 0; r < 4; ++r) {
            sxx[a][0][r] = ex2(sxx[a][0][r] - mxx[a][0]); sxx[a][1][r] = ex2(sxx[a][1][r] - mxx[a][1]); sxx[a][2][r] = ex2(sxx[a][2][r] - mxx[a][1]);
        }
        lxx[a][0] = (sxx[a][0][0] + sxx[a][0][1]) + (sxx[a][0][2] + sxx[a][0][3]);
        lxx[a][1] = ((sxx[a][1][0] + sxx[a][1][1]) + (sxx[a][1][2] + sxx[a][1][3])) + ((sxx[a][2][0] + sxx[a][2][1]) + (sxx[a][2][2] + sxx[a][2][3]));
    }
#pragma unroll
    for (int h = 0; h < 16; ++h) asm volatile("" : "+v"(uf[h]));
#pragma unroll
    for (int a = 0; a < 2; ++a)
        asm volatile("" : "+v"(sxx[a][0]), "+v"(sxx[a][1]), "+v"(sxx[a][2]), "+v"(mxx[a][0]), "+v"(mxx[a][1]), "+v"(lxx[a][0]), "+v"(lxx[a][1]));
    asm volatile("" : "+v"(gate0), "+v"(gate1), "+v"(o0), "+v"(o1));
    FENCE();
    TLS(s < 40 ? 8 : 10);
    if (s + 3 < NSEG) {
        for (;;) {
            asm volatile("" ::: "memory");
            const unsigned x = __builtin_amdgcn_raw_buffer_load_b32(rs, 0, (int)OFF_SXM + ((s + 2) * 32 + 4 * w) * 1024, 16);
            if (__builtin_amdgcn_readfirstlane(x) != 0xFFFFFFFFu || ++mspin >= SPIN_CAP) break;
            __builtin_amdgcn_s_sleep(6);
        }
    } else {
        for (;;) {
            asm volatile("" ::: "memory");
            const unsigned x = __builtin_amdgcn_raw_buffer_load_b32(rs, 0, (int)OFF_SXM + ((s - 2) * 32 + 4 * w) * 1024, 16);
            if (__builtin_amdgcn_readfirstlane(x) != 0xFFFFFFFFu || ++mspin >= SPIN_CAP) break;
            __builtin_amdgcn_s_sleep(6);
        }
    }
    asm volatile("" ::: "memory");
    v4u sm[4];
    v2u yf[8];
    for (;;) {
        asm volatile("" ::: "memory");
#pragma unroll
        for (int k = 0; k < 4; ++k) sm[k] = ldc16(rs, l * 16, (int)OFF_SXM + (s * 32 + 4 * w + k) * 1024);
        if (fresh(__builtin_elementwise_max(__builtin_elementwise_max(sm[0], sm[1]), __builtin_elementwise_max(sm[2], sm[3]))) || ++mspin >= SPIN_CAP) break;
    }
    TLS(s < 40 ? 9 : 11);
    FENCE();
    unsigned px[2][2][4]; unsigned pm[2][2];
#pragma unroll
    for (int qt = 0; qt < 2; ++qt) {
#pragma unroll
        for (int a = 0; a < 2; ++a) {
            const unsigned u00 = sm[2 * a][2 * qt], u01 = sm[2 * a + 1][2 * qt], u10 = sm[2 * a][2 * qt + 1], u11 = sm[2 * a + 1][2 * qt + 1];
            const float t0 = bitsf(u00) + bitsf(u01), t1 = bitsf(u10) + bitsf(u11);
            const float m = xg_max_raw(fmaxf(mxx[a][qt], fmaxf(t0, t1)));
            const float e0 = ex2(t0 - m), e1 = ex2(t1 - m), be = ex2(mxx[a][qt] - m);
            const float f = (qt == 0 ? gate0 : gate1) * __builtin_amdgcn_rcpf(xg_sum((e0 + e1) + be * lxx[a][qt]));
            const float fx = f * be;
            if (qt == 0) { px[a][0][0] = pack2(sxx[a][0][0] * fx, sxx[a][0][1] * fx); px[a][0][1] = pack2(sxx[a][0][2] * fx, sxx[a][0][3] * fx); }
            else {
                px[a][1][0] = pack2(sxx[a][1][0] * fx, sxx[a][1][1] * fx); px[a][1][1] = pack2(sxx[a][1][2] * fx, sxx[a][1][3] * fx);
                px[a][1][2] = pack2(sxx[a][2][0] * fx, sxx[a][2][1] * fx); px[a][1][3] = pack2(sxx[a][2][2] * fx, sxx[a][2][3] * fx);
            }
            pm[qt][a] = pack2(e0 * f, e1 * f);
        }
    }
    *(LAS v4u*)(L + w * 4096 + l * 16) = v4u{px[0][0][0], px[0][0][1], px[1][0][0], px[1][0][1]};
    *(LAS v4u*)(L + w * 4096 + 1024 + l * 16) = v4u{px[0][1][0], px[0][1][1], px[0][1][2], px[0][1][3]};
    *(LAS v4u*)(L + w * 4096 + 2048 + l * 16) = v4u{px[1][1][0], px[1][1][1], px[1][1][2], px[1][1][3]};
    *(LAS v4u*)(L + w * 4096 + 3072 + l * 16) = v4u{pm[0][0], pm[0][1], pm[1][0], pm[1][1]};
    BAR();
    if (w < 7) {
#pragma unroll
        for (int k = 0; k < 8; ++k) yf[k] = __builtin_amdgcn_raw_buffer_load_b64(rs, l * 8, (int)OFF_YM + (s * 224 + v * 8 + k) * 512, 16);
        v4u pmm[8];
#pragma unroll
        for (int pr = 0; pr < 8; ++pr) {
            const v4u u0 = uf[2 * pr], u1 = uf[2 * pr + 1];
            const v4u pa = *(const LAS v4u*)(L + pr * 4096 + l * 16), pb = *(const LAS v4u*)(L + pr * 4096 + 1024 + l * 16),
                      pc = *(const LAS v4u*)(L + pr * 4096 + 2048 + l * 16);
            pmm[pr] = *(const LAS v4u*)(L + pr * 4096 + 3072 + l * 16);
            o1 = mfma32(u0, pb, o1);
            o0 = mfma32(v4u{u0.x, u0.y, u1.x, u1.y}, pa, o0);
            o1 = mfma32(u1, pc, o1);
        }
        FENCE();
        for (;;) {
            unsigned mm = 0u;
#pragma unroll
            for (int k = 0; k < 8; ++k) mm = max(mm, max(yf[k].x, yf[k].y));
            if (__all(mm != 0xFFFFFFFFu) || ++mspin >= SPIN_CAP) break;
            asm volatile("" ::: "memory");
            yf[0] = __builtin_amdgcn_raw_buffer_load_b64(rs, l * 8, (int)OFF_YM + (s * 224 + v * 8 + 0) * 512, 16);
            yf[1] = __builtin_amdgcn_raw_buffer_load_b64(rs, l * 8, (int)OFF_YM + (s * 224 + v * 8 + 1) * 512, 16);
            yf[2] = __builtin_amdgcn_raw_buffer_load_b64(rs, l * 8, (int)OFF_YM + (s * 224 + v * 8 + 2) * 512, 16);
            yf[3] = __builtin_amdgcn_raw_buffer_load_b64(rs, l * 8, (int)OFF_YM + (s * 224 + v * 8 + 3) * 512, 16);
            yf[4] = __builtin_amdgcn_raw_buffer_load_b64(rs, l * 8, (int)OFF_YM + (s * 224 + v * 8 + 4) * 512, 16);
            yf[5] = __builtin_amdgcn_raw_buffer_load_b64(rs, l * 8, (int)OFF_YM + (s * 224 + v * 8 + 5) * 512, 16);
            yf[6] = __builtin_amdgcn_raw_buffer_load_b64(rs, l * 8, (int)OFF_YM + (s * 224 + v * 8 + 6) * 512, 16);
            yf[7] = __builtin_amdgcn_raw_buffer_load_b64(rs, l * 8, (int)OFF_YM + (s * 224 + v * 8 + 7) * 512, 16);
        }
#pragma unroll
        for (int pp = 0; pp < 4; ++pp) {
            const v4u yy = v4u{yf[2 * pp].x, yf[2 * pp].y, yf[2 * pp + 1].x, yf[2 * pp + 1].y};
            o0 = mfma32(yy, v4u{pmm[2 * pp].x, pmm[2 * pp].y, pmm[2 * pp + 1].x, pmm[2 * pp + 1].y}, o0);
            o1 = mfma32(yy, v4u{pmm[2 * pp].z, pmm[2 * pp].w, pmm[2 * pp + 1].z, pmm[2 * pp + 1].w}, o1);
        }
        stg8(p.out + (size_t)(32 * s + n) * 448 + 16 * v + 4 * g, v2u{pack2(o0[0], o0[1]), pack2(o0[2], o0[3])});
        stg8(p.out + (size_t)(32 * s + 16 + n) * 448 + 16 * v + 4 * g, v2u{pack2(o1[0], o1[1]), pack2(o1[2], o1[3])});
    }
}

__global__ void __launch_bounds__(512, 2) k_main(Params p) {
    extern __shared__ __attribute__((aligned(16))) unsigned char lds_[];
    p.ws = align_ws(p.ws);
    const int rot = __builtin_amdgcn_readfirstlane(((const int*)(p.ws + OFF_CTL))[8]);
    const int grp = (blockIdx.x + rot) & 7, u = blockIdx.x >> 3;
    LAS unsigned char* L = (LAS unsigned char*)lds_;
    const int w = __builtin_amdgcn_readfirstlane(threadIdx.x >> 6);
    TLS(0);
    if (grp == 0) {
        if (w == 0) score_wave<1, 0>(p, L, u, w); else if (w < 3) score_wave<0, 0>(p, L, u, w); else if (w == 3) score_wave<0, DLY_3>(p, L, u, w);
        else if (w < 7) score_wave<0, DLY_4>(p, L, u, w); else score_wave<2, 0>(p, L, u, w);
        TLS(7);
        return;
    }
    if (grp == 1) {
        if (u < 28) { if (w < 3) value_wave<false, false>(p, L, u, w); else if (w < 7) value_wave<false, true>(p, L, u, w); else value_wave<true, false>(p, L, u, w); }
        TLS(7);
        return;
    }
    const __amdgpu_buffer_rsrc_t rs = __builtin_amdgcn_make_buffer_rsrc(p.ws, 0, (int)WS_END, 0x00020000);
    const int k = (grp - 2) * 32 + u;
    if (k < 96) {
        if (k % 3 == 0) { serve(p, L, rs, k / 3, w); TLS(7); return; }
        sweep<true>(p, L, rs, k / 3, k % 3 - 1, w);
    } else if (k < 186) sweep<false>(p, L, rs, k / 3, k % 3, w);
    else {
        const unsigned parmask = load_parmask(p.ws);
        const int l = threadIdx.x & 63, wi = (k - 186) * 8 + w;
        const v4u ff = v4u{0xFFFFFFFFu, 0xFFFFFFFFu, 0xFFFFFFFFu, 0xFFFFFFFFu};
        __builtin_amdgcn_s_sleep(127); __builtin_amdgcn_s_sleep(127);
        for (int pg = 1; pg < NPAGE; ++pg) {
            const int base = hop_off(pg * SPP, parmask);
            for (int i = wi; i < SPP * NCHUNK * 8; i += 48) { stc16(ff, rs, l * 16, base + (i >> 3) * CH + (i & 7) * 1024); if (PACE_Z) __builtin_amdgcn_s_sleep(PACE_Z); }
            for (int i = wi; i < SPP * 32 && pg * SPP * 32 + i < NSEG * 32; i += 48) stc16(ff, rs, l * 16, (int)OFF_SXM + (pg * SPP * 32 + i) * 1024);
            asm volatile("s_waitcnt vmcnt(0)" ::: "memory");
            BAR();
            if (threadIdx.x == 0) __hip_atomic_fetch_add((GAS unsigned*)(p.ws + CT_PZ), 1u << (4 * (pg - 1)), __ATOMIC_RELAXED, __HIP_MEMORY_SCOPE_AGENT);
        }
        if (threadIdx.x == 0) __hip_atomic_fetch_add((GAS unsigned*)(p.ws + CT_DONE), 1u, __ATOMIC_RELAXED, __HIP_MEMORY_SCOPE_AGENT);
    }
    const int wk = k < 96 ? (k / 3) * 2 + (k % 3 - 1) : k - 32;
    unsigned mspin = 0;
    if (w == 0) {
        for (;;) {
            asm volatile("" ::: "memory");
            const unsigned c = __builtin_amdgcn_raw_buffer_load_b32(rs, CT_DONE, 0, 16);
            if (__builtin_amdgcn_readfirstlane(c) >= 160u || ++mspin >= SPIN_CAP) break;
            __builtin_amdgcn_s_sleep(16);
        }
    }
    BAR();
    TLS(4);
    for (int it = wk * 8 + w; it < 2048; it += 1280) attn_item(p, rs, it);
    TLS(5);
    for (int i = 0; i < (wk * STAG) >> 4; ++i) __builtin_amdgcn_s_sleep(64);
    for (int it = wk; it < 256; it += 160) { out_item(p, L, rs, it >> 2, it & 3, w, mspin); TLS(it < 160 ? 6 : 7); }
}

extern "C" void kernel_launch(void* const* d_in, const int* in_sizes, int n_in, void* d_out, int out_size, void* d_ws, size_t ws_size,
                              hipStream_t stream) {
    Params p;
    p.x = (const u16*)d_in[0]; p.wqkv = (const u16*)d_in[1]; p.wo = (const u16*)d_in[2]; p.mem0 = (const u16*)d_in[4];
    p.Wq = (const u16*)d_in[5]; p.Wk = (const u16*)d_in[6]; p.Wv = (const u16*)d_in[7]; p.Wo = (const u16*)d_in[8];
    p.gate_w = (const u16*)d_in[10];
    p.out = (u16*)d_out; p.ws = (unsigned char*)d_ws;
    (void)hipFuncSetAttribute((const void*)k_prep, hipFuncAttributeMaxDynamicSharedMemorySize, K1_LDS);
    (void)hipFuncSetAttribute((const void*)k_main, hipFuncAttributeMaxDynamicSharedMemorySize, L_REC_END);
    k_prep<<<256, 512, K1_LDS, stream>>>(p);
    k_main<<<256, 512, L_REC_END, stream>>>(p);
}
```

```cpp
#include <hip/hip_runtime.h>
#include <hip/hip_bf16.h>
#include <stdint.h>

#define GAS __attribute__((address_space(1)))
#define LAS __attribute__((address_space(3)))
typedef __bf16 bf16x2 __attribute__((ext_vector_type(2)));
typedef __bf16 bf16x8 __attribute__((ext_vector_type(8)));
typedef __bf16 bf16x16 __attribute__((ext_vector_type(16)));
typedef short s16x4 __attribute__((ext_vector_type(4)));
typedef float f32x2 __attribute__((ext_vector_type(2)));
typedef float f32x4 __attribute__((ext_vector_type(4)));
typedef unsigned v2u __attribute__((ext_vector_type(2)));
typedef unsigned v4u __attribute__((ext_vector_type(4)));
typedef unsigned v8u __attribute__((ext_vector_type(8)));
typedef unsigned short u16;

constexpr int NSEG = 64;
constexpr float LOG2E = 1.4426950408889634f;
constexpr float LK = 1024.0f;

constexpr size_t SZ_W = (size_t)16 * 28 * 14 * 1024;
constexpr size_t OFF_HOP = 0;
constexpr int PAGE = 2 << 20, CH = 16384, NCHUNK = 9, STEPB = NCHUNK * CH, NPAGE = 5, SPP = 14;
constexpr int C_M = 0, C_M2 = 4 * CH, C_M3 = 5 * CH, C_S = 6 * CH, C_P = 7 * CH;
constexpr size_t HOP_BYTES = (size_t)NPAGE * PAGE;
constexpr size_t OFF_CTL = OFF_HOP + HOP_BYTES;
constexpr size_t CTL_BYTES = 36864;
constexpr int CT_DONE = (int)OFF_CTL + 256, CT_PZ = (int)OFF_CTL + 384, CT_OFC = (int)OFF_CTL + 512, CT_PROG = CT_OFC + 64 * 128;
constexpr size_t OFF_TW = OFF_CTL + CTL_BYTES;
constexpr size_t OFF_YW = OFF_TW + SZ_W;
constexpr size_t OFF_QW = OFF_YW + SZ_W;
constexpr size_t OFF_VW = OFF_QW + (size_t)32 * 28 * 1024;
constexpr size_t OFF_WOF = OFF_VW + (size_t)16 * 28 * 1024;
constexpr size_t OFF_XP = OFF_WOF + (size_t)28 * 16 * 1024;
constexpr size_t OFF_GATE = OFF_XP + (size_t)128 * 14 * 1024;
constexpr size_t SEGB = (size_t)448 * 1024;
constexpr size_t OFF_GF = OFF_GATE + 8192;
constexpr size_t OFF_UF = OFF_GF + 64 * SEGB;
constexpr size_t OFF_QF = OFF_UF + 64 * SEGB;
constexpr size_t OFF_KF = OFF_QF + (size_t)2048 * 1024;
constexpr size_t OFF_VF = OFF_KF + (size_t)2048 * 1024;
constexpr size_t OFF_OF = OFF_VF + (size_t)2048 * 1024;
constexpr size_t OFF_SXM = OFF_OF + (size_t)2048 * 1024;
constexpr size_t OFF_YM = OFF_SXM + (size_t)64 * 32 * 1024;
constexpr size_t WS_END = OFF_YM + (size_t)64 * 224 * 512;

struct Params {
    const u16* x; const u16* wqkv; const u16* wo; const u16* mem0;
    const u16* Wq; const u16* Wk; const u16* Wv; const u16* Wo; const u16* gate_w;
    u16* out; unsigned char* ws;
};
__device__ __forceinline__ unsigned char* align_ws(unsigned char* w) {
    return w + ((0 - (size_t)w) & (size_t)(PAGE - 1));
}
__device__ __forceinline__ unsigned xcc_id() { return (unsigned)__builtin_amdgcn_s_getreg(20 | (3 << 11)) & 0xfu; }
typedef const __attribute__((address_space(4))) unsigned* cptr;
__device__ __forceinline__ unsigned load_parmask(const unsigned char* ws) {
    cptr c = (cptr)(ws + OFF_CTL);
    unsigned m = 0;
#pragma unroll
    for (int k = 0; k < NPAGE; ++k) m |= (c[k] & 1u) << k;
    return m;
}
__device__ __forceinline__ int hop_off(int s, unsigned parmask) {
    const int pg = s / SPP, sl = s - pg * SPP;
    return pg * PAGE + sl * STEPB + (int)((parmask >> pg) & 1u) * 8192;
}

__device__ __forceinline__ unsigned pack2(float a, float b) {
    return __builtin_bit_cast(unsigned, __builtin_convertvector(f32x2{a, b}, bf16x2));
}
__device__ __forceinline__ f32x4 mfma32(v4u a, v4u b, f32x4 c) {
    return __builtin_amdgcn_mfma_f32_16x16x32_bf16(__builtin_bit_cast(bf16x8, a), __builtin_bit_cast(bf16x8, b), c, 0, 0, 0);
}
__device__ __forceinline__ f32x4 mfma16(v2u a, v2u b, f32x4 c) {
    return __builtin_amdgcn_mfma_f32_16x16x16bf16_1k(__builtin_bit_cast(s16x4, a), __builtin_bit_cast(s16x4, b), c, 0, 0, 0);
}
__device__ __forceinline__ f32x4 smfmac(v4u a, v8u b, f32x4 c, int idx) {
    return __builtin_amdgcn_smfmac_f32_16x16x64_bf16(__builtin_bit_cast(bf16x8, a), __builtin_bit_cast(bf16x16, b), c, idx, 0, 0);
}
__device__ __forceinline__ v4u ldg16(const void* p) { return *(const GAS v4u*)p; }
__device__ __forceinline__ v2u ldg8(const void* p) { return *(const GAS v2u*)p; }
__device__ __forceinline__ void stg16(void* p, v4u v) { *(GAS v4u*)p = v; }
__device__ __forceinline__ void stg8(void* p, v2u v) { *(GAS v2u*)p = v; }
__device__ __forceinline__ void stc16(v4u v, const __amdgpu_buffer_rsrc_t rs, int voff, int soff) { __builtin_amdgcn_raw_buffer_store_b128(v, rs, voff + soff, 0, 16); }
__device__ __forceinline__ v2u tr_read(const LAS void* p) {
    return __builtin_bit_cast(v2u, __builtin_amdgcn_ds_read_tr16_b64_v4i16((LAS s16x4*)p));
}
__device__ __forceinline__ unsigned fbits(float v) { return __builtin_bit_cast(unsigned, v); }
__device__ __forceinline__ float bitsf(unsigned v) { return __builtin_bit_cast(float, v); }
__device__ __forceinline__ float xg_max(float v) {
    const v2u a = __builtin_amdgcn_permlane16_swap(fbits(v), fbits(v), false, false);
    v = fmaxf(bitsf(a.x), bitsf(a.y));
    const v2u b = __builtin_amdgcn_permlane32_swap(fbits(v), fbits(v), false, false);
    return fmaxf(bitsf(b.x), bitsf(b.y));
}
__device__ __forceinline__ float xg_sum(float v) {
    const v2u a = __builtin_amdgcn_permlane16_swap(fbits(v), fbits(v), false, false);
    v = bitsf(a.x) + bitsf(a.y);
    const v2u b = __builtin_amdgcn_permlane32_swap(fbits(v), fbits(v), false, false);
    return bitsf(b.x) + bitsf(b.y);
}
__device__ __forceinline__ float fold32(float a, float b) {
    const v2u r = __builtin_amdgcn_permlane32_swap(fbits(a), fbits(b), false, false);
    return bitsf(r.x) + bitsf(r.y);
}
__device__ __forceinline__ float ex2(float v) { return __builtin_amdgcn_exp2f(v); }
__device__ __forceinline__ unsigned sane(unsigned v) { return v < 0xFFFFFFFEu ? v : 0xFFFFFFFEu; }

__device__ __forceinline__ int porder(int f, int g, int e) {
    return 64 * (f >> 1) + 16 * (f & 1) + 4 * g + 32 * ((e >> 1) & 1) + 2 * (e >> 2) + (e & 1);
}
constexpr int NU_HOP = SPP * NCHUNK * 16;
template <bool MIDBAR>
__device__ __forceinline__ void misc_wave(const Params& p, const int mw, const int l) {
    const int n = l & 15, g = l >> 4;
    v2u xl[4], xh[4];
#pragma unroll
    for (int k = 0; k < 4; ++k) {
        const int i = mw * 4 + k, rt = i / 14, f = i % 14;
        const u16* row = p.x + (size_t)(rt * 16 + n) * 448 + 64 * (f >> 1) + 16 * (f & 1) + 4 * g;
        xl[k] = ldg8(row); xh[k] = ldg8(row + 32);
    }
    unsigned qv[3][8];
    bool qok[3];
#pragma unroll
    for (int k = 0; k < 3; ++k) {
        const int i = mw * 3 + k;
        const bool isv = i >= 896;
        const int fr = isv ? i - 896 : i;
        const int unit = fr / 28, t = (fr % 28) / 14, f = fr % 14;
        const int d = isv ? 16 * t + n : 8 * (n >> 2) + 4 * t + (n & 3);
        const int col = (isv ? 896 + unit * 28 : (unit >> 4) * 448 + (unit & 15) * 28) + (d < 28 ? d : 0);
        qok[k] = d < 28;
#pragma unroll
        for (int e = 0; e < 8; ++e) qv[k][e] = p.wqkv[(size_t)porder(f, g, e) * 1344 + col];
    }
    unsigned wv[8];
#pragma unroll
    for (int e = 0; e < 8; ++e) {
        const int d = (e < 4) ? 4 * g + e : 16 + 4 * g + e - 4;
        wv[e] = p.wo[(size_t)((mw & 15) * 28 + (d < 28 ? d : 0)) * 448 + 16 * (mw >> 4) + n];
        if (d >= 28) wv[e] = 0u;
    }
    v4u gxv[7], ggv[7];
    const int row = (mw & 255) * 8 + (l >> 3), c0 = (l & 7) * 8;
#pragma unroll
    for (int k = 0; k < 7; ++k) { gxv[k] = ldg16(p.x + (size_t)row * 448 + k * 64 + c0); ggv[k] = ldg16(p.gate_w + k * 64 + c0); }
    for (int i = mw; i < NU_HOP; i += 448) {
        const int st = i / (NCHUNK * 16), ck = (i >> 4) % NCHUNK, kb = i & 15;
        v4u val = v4u{0xFFFFFFFFu, 0xFFFFFFFFu, 0xFFFFFFFFu, 0xFFFFFFFFu};
        if (st == 0 && ck < 6 && (kb & 7) < 7) {
            const int im = (kb & 7) * 64 + l;
            const v4u m = ldg16(p.mem0 + (size_t)(im & 7) * 448 + (im >> 3) * 8);
            val = v4u{sane(m.x), sane(m.y), sane(m.z), sane(m.w)};
        }
        stg16(p.ws + OFF_HOP + (size_t)hop_off(st, 0u) + ck * CH + kb * 1024 + l * 16, val);
    }
    if (MIDBAR) { __builtin_amdgcn_sched_barrier(0); __builtin_amdgcn_s_barrier(); __builtin_amdgcn_sched_barrier(0); }
    static_assert(SPP * 32 <= 448, "one KiB of the first page's S_xm slots per wave");
    if (mw < SPP * 32) stg16(p.ws + OFF_SXM + (size_t)mw * 1024 + l * 16, v4u{0xFFFFFFFFu, 0xFFFFFFFFu, 0xFFFFFFFFu, 0xFFFFFFFFu});
    stg16(p.ws + OFF_YM + (size_t)60 * 224 * 512 + (size_t)mw * 1024 + l * 16, v4u{0xFFFFFFFFu, 0xFFFFFFFFu, 0xFFFFFFFFu, 0xFFFFFFFFu});
#pragma unroll
    for (int k = 0; k < 4; ++k) stg16(p.ws + OFF_XP + (size_t)(mw * 4 + k) * 1024 + l * 16, v4u{xl[k].x, xh[k].x, xl[k].y, xh[k].y});
#pragma unroll
    for (int k = 0; k < 3; ++k) {
        const v4u o = v4u{qv[k][0] | (qv[k][1] << 16), qv[k][2] | (qv[k][3] << 16), qv[k][4] | (qv[k][5] << 16), qv[k][6] | (qv[k][7] << 16)};
        stg16(p.ws + OFF_QW + (size_t)(mw * 3 + k) * 1024 + l * 16, qok[k] ? o : v4u{0u, 0u, 0u, 0u});
    }
    stg16(p.ws + OFF_WOF + (size_t)mw * 1024 + l * 16, v4u{wv[0] | (wv[1] << 16), wv[2] | (wv[3] << 16), wv[4] | (wv[5] << 16), wv[6] | (wv[7] << 16)});
    float s = 0.f;
#pragma unroll
    for (int k = 0; k < 7; ++k)
#pragma unroll
        for (int j = 0; j < 4; ++j) {
            s += bitsf(gxv[k][j] << 16) * bitsf(ggv[k][j] << 16);
            s += bitsf(gxv[k][j] & 0xFFFF0000u) * bitsf(ggv[k][j] & 0xFFFF0000u);
        }
    s += __shfl_xor(s, 1); s += __shfl_xor(s, 2); s += __shfl_xor(s, 4);
    if ((l & 7) == 0 && mw < 256) ((float*)(p.ws + OFF_GATE))[row] = 1.0f / (1.0f + __expf(-s));
}

constexpr int K1_LAUX = 0;
__device__ __forceinline__ v4u ldr16(const void* q) { return ldg16(q); }
constexpr int K1_RS = 528, K1_OS = 144, K1_IMG = 36864, K1_IS = 136, K1_LDS = K1_IMG + 7 * 64 * K1_IS;
template <int mat>
__device__ __forceinline__ void prep_heavy(const Params& p, LAS unsigned char* L, const int h, const int blk, const int w) {
    const int b = blockIdx.x, tid = threadIdx.x, l = tid & 63, n = l & 15, g = l >> 4;
    v4u t[4];
    if (mat == 0) {
#pragma unroll
        for (int k = 0; k < 4; ++k) { const int u = tid + 512 * k; t[k] = ldr16(p.Wq + ((size_t)(64 * blk + (u >> 5)) * 16 + h) * 256 + (u & 31) * 8); }
    } else {
#pragma unroll
        for (int k = 0; k < 4; ++k) { const int u = tid + 512 * k; t[k] = ldr16(p.Wo + ((size_t)h * 256 + (u >> 3)) * 448 + 64 * blk + (u & 7) * 8); }
    }
    __builtin_amdgcn_sched_barrier(0);
    const int rs_ = mat == 0 ? K1_RS : K1_OS, sh = mat == 0 ? 5 : 3;
    if (w == 7) {
#pragma unroll
        for (int k = 0; k < 4; ++k) { const int u = tid + 512 * k; *(LAS v4u*)(L + (u >> sh) * rs_ + (u & ((1 << sh) - 1)) * 16) = t[k]; }
        asm volatile("s_waitcnt lgkmcnt(0)" ::: "memory");
        misc_wave<true>(p, 224 + b, l);
        return;
    }
    v4u cr[4][8];
    LAS unsigned char* img = L + K1_IMG + w * 64 * K1_IS;
    {
        const __amdgpu_buffer_rsrc_t wr = __builtin_amdgcn_make_buffer_rsrc((void*)(mat == 0 ? p.Wk : p.Wv), 0, 448 * 4096 * 2, 0x00020000);
        const int vo = ((64 * w + (l >> 5)) * 16 + h) * 512 + (l & 31) * 16;
#define K1_LOAD(tt) do { _Pragma("unroll") for (int j = 0; j < 8; ++j) \
            cr[tt][j] = __builtin_amdgcn_raw_buffer_load_b128(wr, vo, (16 * ((tt) >> 1) + 2 * ((tt) & 1) + 4 * (j >> 1) + 32 * (j & 1)) * 8192, K1_LAUX); \
            __builtin_amdgcn_sched_barrier(0); } while (0)
#define K1_TURN(tt) do { \
        _Pragma("unroll") for (int j = 0; j < 8; ++j) *(LAS v4u*)(img + (2 * j + (l >> 5)) * K1_RS + (l & 31) * 16) = cr[tt][j]; \
        asm volatile("" ::: "memory"); \
        _Pragma("unroll") for (int es = 0; es < 8; ++es) cr[tt][es] = *(const LAS v4u*)(img + n * K1_RS + es * 64 + g * 16); \
        asm volatile("" ::: "memory"); __builtin_amdgcn_sched_barrier(0); } while (0)
        K1_LOAD(0); K1_LOAD(1); K1_LOAD(2);
#pragma unroll
        for (int k = 0; k < 4; ++k) { const int u = tid + 512 * k; *(LAS v4u*)(L + (u >> sh) * rs_ + (u & ((1 << sh) - 1)) * 16) = t[k]; }
        __builtin_amdgcn_sched_barrier(0);
        K1_TURN(0);
        K1_LOAD(3);
        K1_TURN(1); K1_TURN(2); K1_TURN(3);
    }
    asm volatile("s_waitcnt lgkmcnt(0)" ::: "memory");
    __builtin_amdgcn_s_barrier();
    asm volatile("" ::: "memory");
    constexpr float SC = mat == 0 ? LOG2E / 16.0f / LK : 1.0f;
    const __amdgpu_buffer_rsrc_t ors = __builtin_amdgcn_make_buffer_rsrc(p.ws, 0, (int)WS_END, 0x00020000);
#pragma unroll
    for (int y = 0; y < 4; ++y) {
        v4u bfr[8];
#pragma unroll
        for (int es = 0; es < 8; ++es) {
            if constexpr (mat == 0) {
                bfr[es] = *(const LAS v4u*)(L + (16 * (y >> 1) + 2 * (y & 1) + 4 * (n >> 2) + 32 * ((n >> 1) & 1) + (n & 1)) * K1_RS + es * 64 + g * 16);
            } else {
                const LAS unsigned char* rp = L + (32 * es + 8 * g + (n >> 2)) * K1_OS + y * 32 + (l & 3) * 8;
                const v2u lo = tr_read(rp), hi = tr_read(rp + 4 * K1_OS);
                bfr[es] = v4u{lo.x, lo.y, hi.x, hi.y};
            }
        }
        f32x4 acc[4];
#pragma unroll
        for (int x = 0; x < 4; ++x) acc[x] = f32x4{0.f, 0.f, 0.f, 0.f};
#pragma unroll
        for (int es = 0; es < 8; ++es)
#pragma unroll
            for (int x = 0; x < 4; ++x) acc[x] = mfma32(cr[x][es], bfr[es], acc[x]);
        const int d = mat == 0 ? (int)OFF_TW + ((h * 28 + 4 * blk + y) * 7 + w) * 2048 : (int)OFF_YW + (((4 * blk + y) * 16 + h) * 7 + w) * 2048;
#pragma unroll
        for (int a = 0; a < 2; ++a) {
            const f32x4 lo = acc[2 * a] * SC, hi = acc[2 * a + 1] * SC;
            stc16(v4u{pack2(lo[0], lo[1]), pack2(lo[2], lo[3]), pack2(hi[0], hi[1]), pack2(hi[2], hi[3])}, ors, l * 16, d + a * 1024);
        }
        if (y == 1) __builtin_amdgcn_sched_barrier(0);
    }
}

__global__ void __launch_bounds__(512, 2) k_prep(Params p) {
    extern __shared__ __attribute__((aligned(16))) unsigned char lds_[];
    LAS unsigned char* L = (LAS unsigned char*)lds_;
    p.ws = align_ws(p.ws);
    const int b = blockIdx.x, tid = threadIdx.x;
    const int l = tid & 63, w = __builtin_amdgcn_readfirstlane(tid >> 6);
    if (b >= 224) {
        const int k = b - 224;
        if ((k & 7) == 0 && w < 2) {
            const int pg = (k >> 3) + 4 * w;
            if (pg < NPAGE && l == 0) {
                GAS unsigned* a = (GAS unsigned*)(p.ws + OFF_HOP + (size_t)pg * PAGE + 126 * CH);
                unsigned best[4] = {~0u, ~0u, ~0u, ~0u};
                for (int it = 0; it < 3; ++it) {
#pragma unroll
                    for (int q = 0; q < 4; ++q) {
                        const unsigned long long t0 = __builtin_amdgcn_s_memtime();
                        __hip_atomic_store(a + q * 1024, 0xFFFFFFFFu, __ATOMIC_RELAXED, __HIP_MEMORY_SCOPE_AGENT);
                        asm volatile("s_waitcnt vmcnt(0)" ::: "memory");
                        const unsigned dt = (unsigned)(__builtin_amdgcn_s_memtime() - t0);
                        best[q] = min(best[q], dt);
                    }
                }
                ((unsigned*)(p.ws + OFF_CTL))[pg] = ((best[0] + best[1] > best[2] + best[3]) ? 1u : 0u) ^ ((xcc_id() >> 2) & 1u);
            }
            if (pg == 7) {
                if (l == 0) ((unsigned*)(p.ws + OFF_CTL))[8] = xcc_id() & 7u;
                for (int i = l; i < (int)(CTL_BYTES - 256) / 16; i += 64) stg16(p.ws + OFF_CTL + 256 + i * 16, v4u{0u, 0u, 0u, 0u});
            }
            return;
        }
        if ((k & 7) == 0) return;
        const int kk = k - (k >> 3) - 1;
        misc_wave<false>(p, kk * 8 + w, l);
        return;
    }
    const int r = b >> 3, h = (b & 7) + 8 * (r & 1), q = r >> 1;
    if (q < 7) prep_heavy<0>(p, L, h, q, w); else prep_heavy<1>(p, L, h, q - 7, w);
}

constexpr int L_MST = 0, L_TIM = L_MST + 2 * 7168, L_RED = L_TIM + 8192, L_REC_END = 96 * 1024;
constexpr int DLY_M = 1820;
constexpr int PF_GAP = 6;
constexpr int DLY_YS = 128;
constexpr int PF = 4;
constexpr int STAGA = 1;
constexpr int PDY = 64;
constexpr int PDL = 104;
constexpr int PDO = 104;
constexpr int PRX = 2;
constexpr int PRL = 1;
constexpr int W3Y = 3;
constexpr int LY = 0;
constexpr int GAP_M = 150;
constexpr int DLY_G = 300;
constexpr int DLY_3 = 130;
constexpr int DLY_4 = 60;
constexpr int DLY_V = 100;
constexpr int DLY_Q = 200;
constexpr int SW_DLY = 45;
constexpr int STAG = 1;
template <int C> __device__ __forceinline__ void delay_cycles() {
    if constexpr (C >= 64) asm volatile(".rept %0\n\ts_nop 15\n\t.endr" :: "i"(C / 64));
    if constexpr ((C & 63) >= 4) asm volatile("s_nop %0" :: "i"(((C & 63) >> 2) - 1));
}

__device__ __forceinline__ bool fresh(v4u x) {
    const unsigned m = max(max(x.x, x.y), max(x.z, x.w));
    return __all(m != 0xFFFFFFFFu);
}
#define BAR() do { asm volatile("s_waitcnt lgkmcnt(0)" ::: "memory"); __builtin_amdgcn_s_barrier(); asm volatile("" ::: "memory"); } while (0)
#define FENCE() do { asm volatile("" ::: "memory"); __builtin_amdgcn_sched_barrier(0); } while (0)

typedef _Float16 f16x2 __attribute__((ext_vector_type(2)));
__device__ __forceinline__ float hlo(unsigned v) { const f16x2 h = __builtin_bit_cast(f16x2, v); return (float)h.x; }
__device__ __forceinline__ float hhi(unsigned v) { const f16x2 h = __builtin_bit_cast(f16x2, v); return (float)h.y; }
constexpr unsigned SPIN_CAP = 1u << 20;

template <bool COLFOLD>
__device__ __forceinline__ unsigned fold_pack(f32x4 a, unsigned sel) {
    unsigned r;
    if constexpr (COLFOLD)
        asm("s_nop 7\n\tv_permlane32_swap_b32 v200, v202\n\tv_permlane32_swap_b32 v201, v203\n\tv_add_f32 v200, v200, v202\n\tv_add_f32 v201, v201, v203\n\t"
            "s_nop 0\n\tv_add_f32_dpp v200, v200, v200 row_ror:8 row_mask:0xf bank_mask:0xf\n\tv_add_f32_dpp v201, v201, v201 row_ror:8 row_mask:0xf bank_mask:0xf\n\t"
            "v_cvt_pkrtz_f16_f32 v200, v200, v201\n\ts_nop 1\n\tv_mov_b32_dpp v201, v200 quad_perm:[1,0,3,2] row_mask:0xf bank_mask:0xf\n\t"
            "v_perm_b32 %0, v201, v200, %2\n\tv_min_u32 %0, -2, %0" : "=v"(r), "+{v[200:203]}"(a) : "v"(sel));
    else
        asm("s_nop 7\n\tv_permlane32_swap_b32 v200, v202\n\tv_permlane32_swap_b32 v201, v203\n\tv_add_f32 v200, v200, v202\n\tv_add_f32 v201, v201, v203\n\t"
            "v_cvt_pkrtz_f16_f32 v200, v200, v201\n\ts_nop 1\n\tv_mov_b32_dpp v201, v200 quad_perm:[1,0,3,2] row_mask:0xf bank_mask:0xf\n\t"
            "v_perm_b32 %0, v201, v200, %2\n\tv_min_u32 %0, -2, %0" : "=v"(r), "+{v[200:203]}"(a) : "v"(sel));
    return r;
}
__device__ __forceinline__ unsigned pkmaxh(unsigned a, unsigned b) { unsigned r; asm("v_pk_max_f16 %0, %1, %2" : "=v"(r) : "v"(a), "v"(b)); return r; }
__device__ __forceinline__ float vmax(float a, float b) { float r; asm("v_max_f32 %0, %1, %2" : "=v"(r) : "v"(a), "v"(b)); return r; }
__device__ __forceinline__ float xg_max_raw(float v) {
    const v2u a = __builtin_amdgcn_permlane16_swap(fbits(v), fbits(v), false, false);
    v = vmax(bitsf(a.x), bitsf(a.y));
    const v2u b = __builtin_amdgcn_permlane32_swap(fbits(v), fbits(v), false, false);
    return vmax(bitsf(b.x), bitsf(b.y));
}
__device__ __forceinline__ v2u fold_y(f32x4 a, f32x4 b) {
    unsigned r0, r1;
    asm volatile("s_nop 7\n\tv_permlane32_swap_b32 v200, v202\n\tv_permlane32_swap_b32 v201, v203\n\tv_permlane32_swap_b32 v204, v206\n\tv_permlane32_swap_b32 v205, v207\n\t"
        "v_add_f32 v200, v200, v202\n\tv_add_f32 v201, v201, v203\n\tv_add_f32 v204, v204, v206\n\tv_add_f32 v205, v205, v207\n\t"
        "v_cvt_pk_bf16_f32 %0, v200, v201\n\tv_cvt_pk_bf16_f32 %1, v204, v205" : "=&v"(r0), "=&v"(r1), "+{v[200:203]}"(a), "+{v[204:207]}"(b));
    return v2u{r0, r1};
}
__device__ __forceinline__ float xmax_k(v4u pv) {
    unsigned h = pkmaxh(pkmaxh(pv.x, pv.y), pkmaxh(pv.z, pv.w));
    asm("v_pk_max_f16 %0, %0, %0 op_sel:[0,1] op_sel_hi:[1,0]" : "+v"(h));
    const v2u b = __builtin_amdgcn_permlane16_swap(h, h, false, false);
    h = pkmaxh(b.x, b.y);
    const v2u c = __builtin_amdgcn_permlane32_swap(h, h, false, false);
    h = pkmaxh(c.x, c.y);
    float r;
    asm("v_fma_mix_f32 %0, %1, %2, 0 op_sel_hi:[1,0,0]" : "=v"(r) : "v"(h), "s"(LK));
    return r;
}
__device__ __forceinline__ float addh_lo(unsigned a, unsigned b) { float r; asm("v_fma_mix_f32 %0, %1, 1.0, %2 op_sel_hi:[1,0,1]" : "=v"(r) : "v"(a), "v"(b)); return r; }
__device__ __forceinline__ float addh_hi(unsigned a, unsigned b) { float r; asm("v_fma_mix_f32 %0, %1, 1.0, %2 op_sel:[1,0,1] op_sel_hi:[1,0,1]" : "=v"(r) : "v"(a), "v"(b)); return r; }
__device__ __forceinline__ float subh_lo(unsigned a, float b) { float r; asm("v_fma_mix_f32 %0, %1, %3, -%2 op_sel_hi:[1,0,0]" : "=v"(r) : "v"(a), "v"(b), "s"(LK)); return r; }
__device__ __forceinline__ float subh_hi(unsigned a, float b) { float r; asm("v_fma_mix_f32 %0, %1, %3, -%2 op_sel:[1,0,0] op_sel_hi:[1,0,0]" : "=v"(r) : "v"(a), "v"(b), "s"(LK)); return r; }
#define UNLIKELY(x) __builtin_expect(!!(x), 0)

template <int NG, int STR, int SLP = 2>
__device__ __forceinline__ int wait_ready(const __amdgpu_buffer_rsrc_t rs, const int g0, const int need, unsigned& mspin) {
    const int l = threadIdx.x & 63;
    int ready;
    for (;;) {
        asm volatile("" ::: "memory");
        const int v = (int)__builtin_amdgcn_raw_buffer_load_b32(rs, CT_PROG + (g0 * 3 + (l < 3 * NG ? l : 0)) * 128, 0, 16);
        ready = 1 << 20;
#pragma unroll
        for (int k = 0; k < 3 * NG; ++k) if (k % 3 < STR) ready = min(ready, STR * __builtin_amdgcn_readlane(v, k) + (k % 3));
        if (ready > need || ++mspin >= SPIN_CAP) break;
        __builtin_amdgcn_s_sleep(SLP);
    }
    asm volatile("" ::: "memory");
    return ready;
}

template <int ROLE, int DLY1>
__device__ __forceinline__ void score_wave(const Params& p, LAS unsigned char* L, const int u, const int w) {
    constexpr bool SPECIAL = ROLE == 2;
    const int l = threadIdx.x & 63;
    const int n = l & 15, g = l >> 4, i8 = l & 7, hh = (l >> 3) & 1;
    const int half = u & 1;
    const __amdgpu_buffer_rsrc_t rs = __builtin_amdgcn_make_buffer_rsrc(p.ws, 0, (int)WS_END, 0x00020000);
    const int idx = hh ? 0xEEEE : 0x4444;
    const unsigned parmask = load_parmask(p.ws);
    v8u tw[SPECIAL ? 1 : 14];
    v4u gx[SPECIAL ? 14 : 1];
    const LAS unsigned char* mrd[SPECIAL ? 1 : 7];
    if constexpr (!SPECIAL) {
        const unsigned char* src = p.ws + OFF_TW + ((size_t)(u * 14 + 2 * w) * 7) * 2048 + l * 16;
#pragma unroll
        for (int k = 0; k < 7; ++k) {
            const int f = w + k < 7 ? w + k : w + k - 7;
            mrd[k] = L + L_MST + f * 1024 + l * 16;
#pragma unroll
            for (int t = 0; t < 2; ++t) {
                const v4u a = ldg16(src + (7 * t + f) * 2048), b = ldg16(src + (7 * t + f) * 2048 + 1024);
                tw[7 * t + k] = v8u{a.x, a.y, a.z, a.w, b.x, b.y, b.z, b.w};
            }
        }
    } else {
        *(LAS v4u*)(L + L_TIM + (half ? 0 : 14 * 512) + l * 16) = v4u{0u, 0u, 0u, 0u};
        __builtin_amdgcn_s_setprio(2);
    }
    const int moff = C_M + w * 1024 + hh * 512 + g * 128 + i8 * 16;
    const int sst_off = C_S + hh * 4096 + u * 128 + (4 * (g & 1) + 2 * (g >> 1) + (l & 1)) * 16 + (i8 >> 1) * 4;
    const unsigned ssel = (l & 1) ? 0x03020706u : 0x05040100u;
    const int pst_off = C_P + u * 256 + (4 * (g & 1) + 2 * (g >> 1) + (l & 1)) * 32 + (n >> 1) * 4;
    const int goff = ((u & ~1) + ((n >> 2) & 1)) * 14336 + (8 * half + 4 * (n >> 3) + (n & 3) + 16 * g) * 16;
    const int xoff = (int)OFF_XP + 7 * half * 1024 + l * 16;
    unsigned mspin = 0;
    int ready = 0;
    if constexpr (!SPECIAL) {
#pragma unroll
        for (int k = 0; k < 7; ++k) asm volatile("" : "+v"(mrd[k]));
    }
    v4u xq[14], x3 = v4u{0u, 0u, 0u, 0u};
    if constexpr (ROLE == 1) {
#pragma unroll
        for (int k = 0; k < 14; ++k) xq[k] = __builtin_amdgcn_raw_buffer_load_b128(rs, xoff + ((k / 7) * 14 + k % 7) * 1024, (NSEG - 1) * 28672, 0);
    }
    if constexpr (SPECIAL) {
        ready = wait_ready<2, 2>(rs, u & ~1, 0, mspin);
#pragma unroll
        for (int f = 0; f < 14; ++f) gx[f] = __builtin_amdgcn_raw_buffer_load_b128(rs, goff + f * 1024, (int)OFF_GF, 16);
    }
    for (int s = 0; s < NSEG; ++s) {
        const int ho = hop_off(s, parmask);
        FENCE();
        if constexpr (!SPECIAL) {
            if (s) delay_cycles<DLY_M>();
            FENCE();
            v4u xs = __builtin_amdgcn_raw_buffer_load_b128(rs, moff, ho, 16);
            FENCE(); delay_cycles<GAP_M>(); FENCE();
            const v4u x1 = __builtin_amdgcn_raw_buffer_load_b128(rs, moff, ho + CH, 16);
            FENCE(); delay_cycles<GAP_M>(); FENCE();
            const v4u x2 = __builtin_amdgcn_raw_buffer_load_b128(rs, moff, ho + 2 * CH, 16);
            FENCE();
#define MOV4(d_, s_) do { asm volatile("v_mov_b64 %0, %1" : "=v"(d_.lo) : "v"(s_.lo)); asm volatile("v_mov_b64 %0, %1" : "=v"(d_.hi) : "v"(s_.hi)); } while (0)
            f32x4 a0 = {0.f, 0.f, 0.f, 0.f}, a1 = a0;
            asm volatile("" : "+v"(a0), "+v"(a1));
            FENCE();
            *(LAS v4u*)mrd[0] = xs;
            FENCE();
            if (UNLIKELY(!fresh(xs))) {
                FENCE();
                x3 = __builtin_amdgcn_raw_buffer_load_b128(rs, moff, ho + 3 * CH, 16);
                FENCE();
                MOV4(xs, x1);
                if (!fresh(xs)) {
                    MOV4(xs, x2);
                    if (!fresh(xs)) {
                        MOV4(xs, x3);
                        if (!fresh(xs)) {
                            for (;;) {
                                asm volatile("" ::: "memory");
                                xs = __builtin_amdgcn_raw_buffer_load_b128(rs, moff, ho + 3 * CH, 16);
                                if (fresh(xs) || ++mspin >= SPIN_CAP) break;
                            }
                        }
                    }
                }
                *(LAS v4u*)mrd[0] = xs;
            }
            FENCE();
            a0 = smfmac(xs, tw[0], a0, idx); a1 = smfmac(xs, tw[7], a1, idx);
            asm volatile("" : "+v"(a0), "+v"(a1));
            FENCE();
            BAR();
            v4u mp[7];
            delay_cycles<DLY1>();
            FENCE();
#pragma unroll
            for (int k = 1; k < 7; ++k) mp[k] = *(const LAS v4u*)mrd[k];
            FENCE();
#pragma unroll
            for (int k = 1; k < 7; ++k) { a0 = smfmac(mp[k], tw[k], a0, idx); a1 = smfmac(mp[k], tw[7 + k], a1, idx); }
            *(LAS v2u*)(L + L_TIM + (2 * w + 2 * half) * 512 + n * 32 + (g ^ (n >> 2)) * 8) = v2u{pack2(a0[0], a0[1]), pack2(a0[2], a0[3])};
            *(LAS v2u*)(L + L_TIM + (2 * w + 2 * half + 1) * 512 + n * 32 + (g ^ (n >> 2)) * 8) = v2u{pack2(a1[0], a1[1]), pack2(a1[2], a1[3])};
            BAR();
            asm volatile("" : "+v"(x3) : "v"(x1), "v"(x2));
            if (ROLE == 1 && s == NSEG - 1) {
                v4u ta[7];
#pragma unroll
                for (int k = 0; k < 7; ++k) {
                    const v2u t0 = tr_read(L + L_TIM + (2 * k + 2 * half) * 512 + (l ^ g) * 8), t1 = tr_read(L + L_TIM + (2 * k + 2 * half + 1) * 512 + (l ^ g) * 8);
                    ta[k] = v4u{t0.x, t0.y, t1.x, t1.y};
                }
                FENCE();
                f32x4 d0 = {0.f, 0.f, 0.f, 0.f}, d1 = d0;
#pragma unroll
                for (int k = 0; k < 7; ++k) { d0 = mfma32(ta[k], xq[k], d0); d1 = mfma32(ta[k], xq[7 + k], d1); }
                stc16(v4u{sane(fbits(fold32(d0[0], d0[2]))), sane(fbits(fold32(d0[1], d0[3]))),
                                                           sane(fbits(fold32(d1[0], d1[2]))), sane(fbits(fold32(d1[1], d1[3])))},
                                                       rs, l * 16, (int)OFF_SXM + (s * 32 + u) * 1024);
                FENCE();
            }
        } else {
            f32x4 ae = {0.f, 0.f, 0.f, 0.f};
            asm volatile("" : "+v"(ae));
            BAR();
            v4u mp[7];
#pragma unroll
            for (int f = 0; f < 7; ++f) mp[f] = *(const LAS v4u*)(L + L_MST + f * 1024 + l * 16);
            FENCE();
            v4u m2[4];
#pragma unroll
            for (int f = 0; f < 7; ++f) {
                const v4u ga = gx[2 * f], gb = gx[2 * f + 1];
                ae = smfmac(mp[f], v8u{ga.x, ga.y, ga.z, ga.w, gb.x, gb.y, gb.z, gb.w}, ae, idx);
            }
            const unsigned pv = fold_pack<false>(ae, ssel);
            __builtin_amdgcn_raw_buffer_store_b32(pv, rs, pst_off, ho, 16);
            __builtin_amdgcn_raw_buffer_store_b32(pv, rs, pst_off, ho + CH, 16);
            FENCE();
#pragma unroll
            for (int b = 0; b < 4; ++b) m2[b] = *(const LAS v4u*)(L + L_MST + (3 * half + b) * 1024 + l * 16);
            FENCE();
            f32x4 sa = {0.f, 0.f, 0.f, 0.f};
            asm volatile("" : "+v"(sa));
            BAR();
            v8u tb[4];
#pragma unroll
            for (int b = 0; b < 4; ++b) {
                const v2u t0 = tr_read(L + L_TIM + (4 * b) * 512 + (l ^ g) * 8), t1 = tr_read(L + L_TIM + (4 * b + 1) * 512 + (l ^ g) * 8),
                          t2 = tr_read(L + L_TIM + (4 * b + 2) * 512 + (l ^ g) * 8), t3 = tr_read(L + L_TIM + (4 * b + 3) * 512 + (l ^ g) * 8);
                tb[b] = v8u{t0.x, t0.y, t1.x, t1.y, t2.x, t2.y, t3.x, t3.y};
            }
            FENCE();
#pragma unroll
            for (int b = 0; b < 4; ++b) sa = smfmac(m2[b], tb[b], sa, idx);
            __builtin_amdgcn_raw_buffer_store_b32(fold_pack<true>(sa, ssel), rs, sst_off, ho, 16);
            FENCE();
            delay_cycles<DLY_G>();
            if (UNLIKELY(ready <= s + 1 && s + 1 < NSEG)) ready = wait_ready<2, 2>(rs, u & ~1, s + 1, mspin);
            FENCE();
#pragma unroll
            for (int f = 0; f < 14; ++f) gx[f] = __builtin_amdgcn_raw_buffer_load_b128(rs, goff + f * 1024, (int)(OFF_GF + (s + 1 < NSEG ? s + 1 : s) * SEGB), 16);
            FENCE();
            if (UNLIKELY(ready <= s + 3 && ready < NSEG)) ready = wait_ready<2, 2>(rs, u & ~1, -1, mspin);
        }
    }
}

template <bool LAST, bool YOUNG>
__device__ __forceinline__ void value_wave(const Params& p, LAS unsigned char* L, const int v, const int w) {
    const int l = threadIdx.x & 63;
    const int g = l >> 4, i8 = l & 7, hh = (l >> 3) & 1;
    const __amdgpu_buffer_rsrc_t rs = __builtin_amdgcn_make_buffer_rsrc(p.ws, 0, (int)WS_END, 0x00020000);
    const int idx = hh ? 0xEEEE : 0x4444;
    const unsigned parmask = load_parmask(p.ws);
    v8u yw[14];
    const LAS unsigned char* mrd[7];
    {
        const unsigned char* src = p.ws + OFF_YW + (size_t)(v * 8 + w) * 14 * 2048 + l * 16;
#pragma unroll
        for (int k = 0; k < 7; ++k) {
            const int f = w + k < 7 ? w + k : w + k - 7;
            mrd[k] = L + L_MST + f * 1024 + l * 16;
#pragma unroll
            for (int t = 0; t < 2; ++t) {
                const v4u a = ldg16(src + (7 * t + f) * 2048), b = ldg16(src + (7 * t + f) * 2048 + 1024);
                yw[7 * t + k] = v8u{a.x, a.y, a.z, a.w, b.x, b.y, b.z, b.w};
            }
        }
    }
    if (LAST && PRL) __builtin_amdgcn_s_setprio(2);
    const int moff = C_M2 + w * 1024 + hh * 512 + g * 128 + i8 * 16;
    const int mst_off = C_M + v * 256 + ((l >> 2) & 1) * 128 + (l >> 3) * 16 + (l & 3) * 4;
    const int soff = C_S + ((2 * w + hh) * 2 + (g >> 1)) * 128 + i8 * 16 + (g & 1) * 8;
    const int poff = C_P + ((2 * w + hh) * 2 + (g >> 1)) * 256 + i8 * 32 + (g & 1) * 16;
    const int yst_off = (int)OFF_YM + (v * 8 + w) * 512 + l * 8;
    const int uoff = (v * 8 + w) * 2048 + l * 16;
    unsigned mlo = hh ? 0u : 0xFFFFFFFFu, mhi = ~mlo;
    v4u ones = v4u{0x3F803F80u, 0x3F803F80u, 0x3F803F80u, 0x3F803F80u};
    asm volatile("" : "+v"(mlo), "+v"(mhi), "+v"(ones));
#pragma unroll
    for (int k = 0; k < 7; ++k) asm volatile("" : "+v"(mrd[k]));
    unsigned mspin = 0;
    int ho = hop_off(0, parmask);
    int ready = wait_ready<1, 3>(rs, 32 + v, 0, mspin);
    v4u ua = __builtin_amdgcn_raw_buffer_load_b128(rs, uoff, (int)OFF_UF, 16);
    v4u ub = __builtin_amdgcn_raw_buffer_load_b128(rs, uoff + 1024, (int)OFF_UF, 16);
    v2u yprev = v2u{0u, 0u};
    int ypo = 0x7FFFFFF0;
#define V_Y_PHASE(MID) \
        f32x4 y0 = {0.f, 0.f, 0.f, 0.f}, y1 = y0; \
        if constexpr (!LAST) { \
            delay_cycles<DLY_V>(); \
            FENCE(); \
            v4u x = __builtin_amdgcn_raw_buffer_load_b128(rs, moff, ho, 16); \
            FENCE(); \
            delay_cycles<DLY_YS>();           \
            __builtin_amdgcn_raw_buffer_store_b64(yprev, rs, ypo, (s > 0 ? s - 1 : 0) * 224 * 512, 16); \
            asm volatile("" : "+v"(y0), "+v"(y1)); \
            FENCE(); \
            *(LAS v4u*)mrd[0] = x; \
            FENCE(); \
            if (UNLIKELY(!fresh(x))) { \
                for (;;) { asm volatile("" ::: "memory"); x = __builtin_amdgcn_raw_buffer_load_b128(rs, moff, ho, 16); if (fresh(x) || ++mspin >= SPIN_CAP) break; } \
                *(LAS v4u*)mrd[0] = x; \
            } \
            FENCE(); \
            y0 = smfmac(x, yw[0], y0, idx); y1 = smfmac(x, yw[7], y1, idx); \
            asm volatile("" : "+v"(y0), "+v"(y1)); \
            FENCE(); \
        } \
        BAR(); \
        v4u mp[7]; \
        if constexpr (LAST) asm volatile("s_waitcnt vmcnt(0)" ::: "memory");        \
        FENCE(); \
        _Pragma("unroll") for (int k = LAST ? 0 : 1; k < 7; ++k) mp[k] = *(const LAS v4u*)mrd[k]; \
        FENCE(); \
        _Pragma("unroll") for (int k = LAST ? 0 : 1; k < 7; ++k) { y0 = smfmac(mp[k], yw[k], y0, idx); y1 = smfmac(mp[k], yw[7 + k], y1, idx); } \
        MID; \
        v2u yop = fold_y(y0, y1); \
        FENCE()
    for (int s = 0; s < NSEG - 1; ++s) {
        const int hn = hop_off(s + 1, parmask);
        const int uo = (int)(OFF_UF + (s + 1) * SEGB);
        if constexpr (LAST) {
            if (UNLIKELY(ready <= s + 3 && ready < NSEG)) ready = wait_ready<1, 3>(rs, 32 + v, s + 1, mspin);
            if (UNLIKELY((s + 2) % SPP == 0)) {
                const int sh = 4 * ((s + 2) / SPP - 1);
                for (;;) {
                    asm volatile("" ::: "memory");
                    const unsigned c = __builtin_amdgcn_readfirstlane(__builtin_amdgcn_raw_buffer_load_b32(rs, CT_PZ, 0, 16));
                    if (((c >> sh) & 15u) >= 6u || ++mspin >= SPIN_CAP) break;
                }
            }
        }
        int ho1 = ho + CH, ho2 = ho + 4096, hn1 = hn + CH, hn2 = hn + 2 * CH, hn3 = hn + 3 * CH, hnn = hn;
        asm volatile("" : "+s"(ho1), "+s"(ho2), "+s"(hn1), "+s"(hn2), "+s"(hn3), "+s"(hnn));
        FENCE();
        v4u pv;
        constexpr int PD = YOUNG ? PDY : (LAST ? PDL : PDO);
        V_Y_PHASE(if (PD < 64) { FENCE(); delay_cycles<PD>(); pv = __builtin_amdgcn_raw_buffer_load_b128(rs, poff, ho, 16); FENCE(); });
        if (PD >= 64) {
            delay_cycles<(PD >= 64 ? PD - 64 : 0)>();
            FENCE();
            pv = __builtin_amdgcn_raw_buffer_load_b128(rs, poff, ho, 16);
            FENCE();
        }
        delay_cycles<DLY_Q>();
        FENCE();
        const v4u pq = __builtin_amdgcn_raw_buffer_load_b128(rs, poff, ho1, 16);
        FENCE();
#define X_MAX() do { mk = xmax_k(pv); } while (0)
        float mk;
        asm volatile("" : "+v"(pv) :: "memory");
        FENCE();
        v2u sv = __builtin_amdgcn_raw_buffer_load_b64(rs, soff, ho, 16);
        FENCE();
        if (YOUNG) __builtin_amdgcn_s_setprio(3);
        const bool pok = fresh(pv);
        X_MAX();
        asm volatile("" : "+v"(mk));
        FENCE();
        if (UNLIKELY(!pok)) {
            asm volatile("v_mov_b64 %0, %1" : "=v"(pv.lo) : "v"(pq.lo)); asm volatile("v_mov_b64 %0, %1" : "=v"(pv.hi) : "v"(pq.hi));
            if (!fresh(pv)) {
                for (;;) {
                    asm volatile("" ::: "memory");
                    pv = __builtin_amdgcn_raw_buffer_load_b128(rs, poff, ho1, 16);
                    if (fresh(pv) || ++mspin >= SPIN_CAP) break;
                }
            }
            X_MAX();
        }
        const unsigned e0 = pack2(ex2(subh_lo(pv.x, mk)), ex2(subh_hi(pv.x, mk))), e1 = pack2(ex2(subh_lo(pv.y, mk)), ex2(subh_hi(pv.y, mk)));
        FENCE();
        const v2u sw2 = __builtin_amdgcn_raw_buffer_load_b64(rs, soff, ho2, 16);
        FENCE();
        if (YOUNG && PRX != 3) __builtin_amdgcn_s_setprio(0);
        const unsigned e2 = pack2(ex2(subh_lo(pv.z, mk)), ex2(subh_hi(pv.z, mk))), e3 = pack2(ex2(subh_lo(pv.w, mk)), ex2(subh_hi(pv.w, mk)));
        const f32x4 z4 = {0.f, 0.f, 0.f, 0.f};
        const v4u eop = v4u{e0, e1, e2, e3};
        const f32x4 oa = mfma32(ua, eop, z4);
        const f32x4 ob = mfma32(ub, eop, z4);
        const f32x4 lx4 = mfma32(ones, eop, z4);
        FENCE();
        ua = __builtin_amdgcn_raw_buffer_load_b128(rs, uoff, uo, 16);
        FENCE();
        ub = __builtin_amdgcn_raw_buffer_load_b128(rs, uoff + 1024, uo, 16);
        FENCE();
#define S_UNPACK() do { const v2u sw = __builtin_amdgcn_permlane32_swap(sv.x, sv.y, false, false); t0k = addh_lo(sw.x, sw.y) * LK; t1k = addh_hi(sw.x, sw.y) * LK; \
                        asm("v_max3_f32 %0, %1, %2, %3" : "=v"(m) : "v"(t0k), "v"(t1k), "v"(mk)); m = xg_max_raw(m); } while (0)
        float t0k, t1k, m;
        if (YOUNG && PRX == 3) __builtin_amdgcn_s_setprio(0);
        if (YOUNG && PRX == 2) __builtin_amdgcn_s_setprio(3);
        const bool sok = __all(max(sv.x, sv.y) != 0xFFFFFFFFu);
        S_UNPACK();
        f32x4 ox = hh ? ob : oa;
        asm volatile("" : "+v"(m), "+v"(t0k), "+v"(t1k), "+v"(ox));
        FENCE();
        if (UNLIKELY(!sok)) {
            asm volatile("v_mov_b64 %0, %1" : "=v"(sv) : "v"(sw2));
            if (!__all(max(sv.x, sv.y) != 0xFFFFFFFFu)) {
                for (;;) {
                    asm volatile("" ::: "memory");
                    sv = __builtin_amdgcn_raw_buffer_load_b64(rs, soff, ho2, 16);
                    if (__all(max(sv.x, sv.y) != 0xFFFFFFFFu) || ++mspin >= SPIN_CAP) break;
                }
            }
            S_UNPACK();
        }
        if (YOUNG && PRX == 4) __builtin_amdgcn_s_setprio(3);
        const unsigned pk = pack2(ex2(t0k - m), ex2(t1k - m));
        const float beta = ex2(mk - m);
        const v2u bop = v2u{pk & mlo, pk & mhi};
        f32x4 lx = lx4;
        lx[0] *= beta;
        ox *= beta;
        FENCE();
        const f32x4 ls = mfma16(v2u{ones.x, ones.y}, bop, lx);
        f32x4 d = mfma16(yop, bop, ox);
        FENCE();
        const float r = __builtin_amdgcn_rcpf(ls[0]);
        d *= r;
        float r0, r1;
        asm("s_nop 1\n\tv_add_f32_dpp %0, %2, %2 row_ror:8 row_mask:0xf bank_mask:0x3\n\tv_add_f32_dpp %1, %3, %3 row_ror:8 row_mask:0xf bank_mask:0x3\n\t"
            "v_add_f32_dpp %0, %4, %4 row_ror:8 row_mask:0xf bank_mask:0xc\n\tv_add_f32_dpp %1, %5, %5 row_ror:8 row_mask:0xf bank_mask:0xc"
            : "=&v"(r0), "=&v"(r1) : "v"(d[0]), "v"(d[1]), "v"(d[2]), "v"(d[3]));
        *(LAS f32x2*)(L + L_RED + (w >> 1) * 1024 + i8 * 128 + ((g ^ (i8 & 3)) * 2 + hh) * 16 + (w & 1) * 8) = f32x2{r0, r1};
        if (YOUNG && (PRX == 2 || PRX == 4)) __builtin_amdgcn_s_setprio(0);
        FENCE();
        BAR();
        if constexpr (LAST) {
            f32x4 q[4];
#pragma unroll
            for (int k = 0; k < 4; ++k) q[k] = *(const LAS f32x4*)(L + L_RED + k * 1024 + (l ^ (2 * ((l >> 3) & 3))) * 16);
            FENCE();
            const f32x2 ta = (q[0].lo + q[0].hi) + (q[1].lo + q[1].hi);
            const f32x2 tot = (ta + (q[2].lo + q[2].hi)) + (q[3].lo + q[3].hi);
            const unsigned mv = sane(pack2(tot[0], tot[1]));
            __builtin_amdgcn_raw_buffer_store_b32(mv, rs, mst_off, hnn, 16);
            __builtin_amdgcn_raw_buffer_store_b32(mv, rs, mst_off + C_M2, hnn, 16);
            __builtin_amdgcn_raw_buffer_store_b32(mv, rs, mst_off, hn1, 16);
            __builtin_amdgcn_raw_buffer_store_b32(mv, rs, mst_off, hn2, 16);
            __builtin_amdgcn_raw_buffer_store_b32(mv, rs, mst_off, hn3, 16);
            __builtin_amdgcn_raw_buffer_store_b32(mv, rs, mst_off, hn + C_M3, 16);
            FENCE();
            asm volatile("" :: "v"(sw2), "v"(pq));
        } else {
            delay_cycles<256>();
            asm volatile("s_waitcnt vmcnt(0)" :: "v"(sw2), "v"(pq) : "memory");
        }
        if constexpr (LAST) __builtin_amdgcn_raw_buffer_store_b64(v2u{sane(yop.x), sane(yop.y)}, rs, yst_off, s * 224 * 512, 16);
        yprev = v2u{sane(yop.x), sane(yop.y)};
        ypo = yst_off;
        ho = hn;
    }
    {
        const int s = NSEG - 1;
        V_Y_PHASE();
        __builtin_amdgcn_raw_buffer_store_b64(v2u{sane(yop.x), sane(yop.y)}, rs, yst_off, s * 224 * 512, 16);
    }
}

__device__ __forceinline__ void prefetch_wave(const Params& p, const int j, const int w) {
    const int l = threadIdx.x & 63;
    const __amdgpu_buffer_rsrc_t rs = __builtin_amdgcn_make_buffer_rsrc(p.ws, 0, (int)WS_END, 0x00020000);
    unsigned mspin = 0;
    if (w == 7) return;
    const int v = 7 * j + w;
    int ready = 0;
    for (int s = 2; s < NSEG - 1; ++s) {
        if (s >= PF) {
            for (;;) {
                asm volatile("" ::: "memory");
                const unsigned x = __builtin_amdgcn_raw_buffer_load_b32(rs, 0, (int)OFF_SXM + ((s - PF) * 32 + v) * 1024 + 512, 16);
                if (__builtin_amdgcn_readfirstlane(x) != 0xFFFFFFFFu || ++mspin >= SPIN_CAP) break;
                __builtin_amdgcn_s_sleep(8);
            }
        }
        if (ready <= s) ready = wait_ready<1, 3>(rs, 32 + v, s, mspin);
        unsigned t[4];
#pragma unroll
        for (int k = 0; k < 4; ++k) {
            t[k] = __builtin_amdgcn_raw_buffer_load_b32(rs, v * 16384 + k * 4096 + l * 64, (int)(OFF_UF + s * SEGB), 16);
            FENCE();
            __builtin_amdgcn_s_sleep(PF_GAP);
        }
        asm volatile("" :: "v"(t[0]), "v"(t[1]), "v"(t[2]), "v"(t[3]));
    }
}

__device__ __forceinline__ void glds16(const void* gsrc, unsigned lds_dst) {
    unsigned keep;
    asm volatile("s_mov_b32 %0, m0\n\ts_mov_b32 m0, %2\n\ts_nop 0\n\tglobal_load_lds_dwordx4 %1, off\n\ts_mov_b32 m0, %0"
                 : "=&s"(keep) : "v"(gsrc), "s"(lds_dst) : "memory");
}
__device__ __forceinline__ v4u ldc16(const __amdgpu_buffer_rsrc_t rs, int voff, int soff) { return __builtin_amdgcn_raw_buffer_load_b128(rs, voff, soff, 16); }
__device__ __forceinline__ v4u pack8(f32x4 a, f32x4 b) { return v4u{pack2(a[0], a[1]), pack2(a[2], a[3]), pack2(b[0], b[1]), pack2(b[2], b[3])}; }
constexpr int L_XB = 0, XBUF = 28672;

template <bool GTYPE>
__device__ __forceinline__ void sweep(const Params& p, LAS unsigned char* L, const __amdgpu_buffer_rsrc_t rs, const int grp, const int r, const int w) {
    constexpr int STR = GTYPE ? 2 : 3;
    const int l = threadIdx.x & 63;
    const unsigned lbase = (unsigned)(size_t)L;
    const unsigned char* wsrc;
    int obase, ostride, osec;
    float oscale = 1.0f;
    if (GTYPE) {
        const int ks = 7 * (grp & 1) + w;
        if (w < 7) { wsrc = p.ws + OFF_TW + (size_t)((grp >> 1) * 14 + ks) * 28672; obase = (int)OFF_GF + ((grp >> 1) * 28 + ks) * 1024; ostride = (int)SEGB; osec = 14336; }
        else { wsrc = p.ws + OFF_QW + (size_t)grp * 28672; obase = (int)OFF_QF + (grp >> 4) * (2048 * 1024) + (grp & 15) * 1024; ostride = 32768; osec = 16384;
               if (grp < 16) oscale = LOG2E * 0.18898223650461362f; }
    } else {
        osec = 1024;
        if (grp < 60) { wsrc = p.ws + OFF_YW + (size_t)((grp - 32) * 8 + w) * 28672; obase = (int)OFF_UF + ((grp - 32) * 8 + w) * 2048; ostride = (int)SEGB; }
        else { wsrc = p.ws + OFF_QW + (size_t)(32 + (grp - 60) * 8 + w) * 28672; obase = (int)OFF_VF + ((grp - 60) * 8 + w) * 2048; ostride = 32768; }
    }
    const int npc = w < 4 ? 4 : 3, pc0 = w < 4 ? 4 * w : 16 + 3 * (w - 4);
    const unsigned char* xsrc = p.ws + OFF_XP + pc0 * 1024 + l * 16;
    if (r > 0) __builtin_amdgcn_s_sleep(SW_DLY);
    for (int k = 0; k < npc; ++k) glds16(xsrc + (size_t)r * XBUF + k * 1024, lbase + L_XB + (pc0 + k) * 1024);
    v4u wt[2][14];
#pragma unroll
    for (int f = 0; f < 14; ++f) { wt[0][f] = ldg16(wsrc + f * 1024 + l * 16); wt[1][f] = ldg16(wsrc + (14 + f) * 1024 + l * 16); }
    asm volatile("s_waitcnt vmcnt(28)" ::: "memory");
    BAR();
    const int nseg = (NSEG + STR - 1 - r) / STR;
    auto body = [&](const int i) __attribute__((always_inline)) {
        const int seg = r + STR * i;
        const LAS unsigned char* xb = L + L_XB + (i & 1) * XBUF + l * 16;
        f32x4 a[2][2];
#pragma unroll
        for (int t = 0; t < 4; ++t) a[t >> 1][t & 1] = f32x4{0.f, 0.f, 0.f, 0.f};
#pragma unroll
        for (int f = 0; f < 14; ++f) {
            const v4u x0 = *(const LAS v4u*)(xb + f * 1024), x1 = *(const LAS v4u*)(xb + (14 + f) * 1024);
            if (GTYPE) {
                a[0][0] = mfma32(wt[0][f], x0, a[0][0]); a[0][1] = mfma32(wt[1][f], x0, a[0][1]);
                a[1][0] = mfma32(wt[0][f], x1, a[1][0]); a[1][1] = mfma32(wt[1][f], x1, a[1][1]);
            } else {
                a[0][0] = mfma32(x0, wt[0][f], a[0][0]); a[0][1] = mfma32(x0, wt[1][f], a[0][1]);
                a[1][0] = mfma32(x1, wt[0][f], a[1][0]); a[1][1] = mfma32(x1, wt[1][f], a[1][1]);
            }
        }
        const v4u o0 = GTYPE ? pack8(a[0][0] * oscale, a[0][1] * oscale) : pack8(a[0][0], a[1][0]);
        const v4u o1 = GTYPE ? pack8(a[1][0] * oscale, a[1][1] * oscale) : pack8(a[0][1], a[1][1]);
        asm volatile("s_waitcnt vmcnt(0)" ::: "memory");
        stc16(o0, rs, l * 16, obase + seg * ostride);
        stc16(o1, rs, l * 16, obase + seg * ostride + osec);
    };
    auto stage = [&](const int i) __attribute__((always_inline)) {
        for (int k = 0; k < npc; ++k) glds16(xsrc + (size_t)(r + STR * i) * XBUF + k * 1024, lbase + L_XB + (i & 1) * XBUF + (pc0 + k) * 1024);
    };
    body(0);
    stage(1);
    asm volatile("s_waitcnt vmcnt(0)" ::: "memory");
    BAR();
    if (threadIdx.x == 0) __builtin_amdgcn_raw_buffer_store_b32(1u, rs, CT_PROG + (grp * 3 + r) * 128, 0, 16);
    for (int i = 1; i < nseg; ++i) {
        if (i + 1 < nseg) stage(i + 1);
        body(i);
        const bool drain = (i < 2 || i + 1 == nseg);
        if (drain) asm volatile("s_waitcnt vmcnt(0)" ::: "memory");
        BAR();
        if (threadIdx.x == 0) __builtin_amdgcn_raw_buffer_store_b32((unsigned)(drain ? i + 1 : i), rs, CT_PROG + (grp * 3 + r) * 128, 0, 16);
    }
    if (threadIdx.x == 0) __hip_atomic_fetch_add((GAS unsigned*)(p.ws + CT_DONE), 1u, __ATOMIC_RELAXED, __HIP_MEMORY_SCOPE_AGENT);
}

__device__ __forceinline__ void serve(const Params& p, LAS unsigned char* L, const __amdgpu_buffer_rsrc_t rs, const int grp, const int w) {
    const int l = threadIdx.x & 63, n = l & 15, g = l >> 4, hh = (l >> 3) & 1;
    const unsigned parmask = load_parmask(p.ws);
    unsigned mspin = 0;
    if (w == 7) {
        for (int s = 0; s < NSEG - 1; ++s) {
            BAR();
            BAR();
            f32x4 q[7];
#pragma unroll
            for (int k = 0; k < 7; ++k) q[k] = *(const LAS f32x4*)(L + 8192 + k * 1024 + l * 16);
            const f32x4 t = ((q[0] + q[1]) + (q[2] + q[3])) + ((q[4] + q[5]) + q[6]);
            stc16(v4u{sane(fbits(t[0])), sane(fbits(t[1])), sane(fbits(t[2])), sane(fbits(t[3]))}, rs, l * 16, (int)OFF_SXM + (s * 32 + grp) * 1024);
        }
        return;
    }
    const int idx = hh ? 0xEEEE : 0x4444;
    v8u tw[14];
    {
        const unsigned char* src = p.ws + OFF_TW + ((size_t)(grp * 14 + 2 * w) * 7) * 2048 + l * 16;
        __builtin_amdgcn_s_sleep(100);
#pragma unroll
        for (int f = 0; f < 14; ++f) {
            const v4u a = ldg16(src + f * 2048), b = ldg16(src + f * 2048 + 1024);
            tw[f] = v8u{a.x, a.y, a.z, a.w, b.x, b.y, b.z, b.w};
        }
#pragma unroll
        for (int f = 0; f < 14; ++f) asm volatile("" : "+v"(tw[f]));
    }
    const int moff = C_M3 + w * 1024 + hh * 512 + g * 128 + (l & 7) * 16;
    const unsigned char* xr = p.ws + OFF_XP + (7 * (grp & 1) + w) * 1024 + l * 16;
    LAS unsigned char* img = L + 16384 + w * 1024;
    for (int s = 0; s < NSEG - 1; ++s) {
        const v4u xa = ldg16(xr + (size_t)s * 28672), xb = ldg16(xr + (size_t)s * 28672 + 14336);
        const int ho = hop_off(s, parmask);
        v4u x;
        for (;;) {
            asm volatile("" ::: "memory");
            x = ldc16(rs, moff, ho);
            if (fresh(x) || ++mspin >= SPIN_CAP) break;
            __builtin_amdgcn_s_sleep(2);
        }
        *(LAS v4u*)(L + w * 1024 + l * 16) = x;
        BAR();
        v4u mp[7];
#pragma unroll
        for (int f = 0; f < 7; ++f) mp[f] = *(const LAS v4u*)(L + f * 1024 + l * 16);
        FENCE();
        f32x4 a0 = {0.f, 0.f, 0.f, 0.f}, a1 = a0;
#pragma unroll
        for (int f = 0; f < 7; ++f) { a0 = smfmac(mp[f], tw[f], a0, idx); a1 = smfmac(mp[f], tw[7 + f], a1, idx); }
        *(LAS v2u*)(img + n * 32 + (g ^ (n >> 2)) * 8) = v2u{pack2(a0[0], a0[1]), pack2(a0[2], a0[3])};
        *(LAS v2u*)(img + 512 + n * 32 + (g ^ (n >> 2)) * 8) = v2u{pack2(a1[0], a1[1]), pack2(a1[2], a1[3])};
        asm volatile("" ::: "memory");
        const v2u t0 = tr_read(img + (l ^ g) * 8), t1 = tr_read(img + 512 + (l ^ g) * 8);
        const v4u ta = v4u{t0.x, t0.y, t1.x, t1.y};
        const f32x4 z4 = {0.f, 0.f, 0.f, 0.f};
        const f32x4 d0 = mfma32(ta, xa, z4), d1 = mfma32(ta, xb, z4);
        *(LAS f32x4*)(L + 8192 + w * 1024 + l * 16) = f32x4{fold32(d0[0], d0[2]), fold32(d0[1], d0[3]), fold32(d1[0], d1[2]), fold32(d1[1], d1[3])};
        BAR();
    }
}

__device__ __forceinline__ void attn_item(const Params& p, const __amdgpu_buffer_rsrc_t rs, const int item) {
    const int l = threadIdx.x & 63, n = l & 15, g = l >> 4;
    const int rt = item >> 4, h = item & 15;
    const int s = rt >> 1, q = rt * 16 + n;
    const v4u qf = ldc16(rs, l * 16, (int)OFF_QF + (rt * 16 + h) * 1024);
    f32x4 sc[18];
    const f32x4 z4 = {0.f, 0.f, 0.f, 0.f};
    const float NEG = -3.0e38f;
    v4u kf[18];
#pragma unroll
    for (int k = 0; k < 18; ++k) { const int kt = max(2 * (s - 8) + k, 0); kf[k] = ldc16(rs, l * 16, (int)OFF_KF + (kt * 16 + h) * 1024); }
#pragma unroll
    for (int k = 0; k < 18; ++k) {
        const int kt = 2 * (s - 8) + k;
        f32x4 a = mfma32(kf[k], qf, z4);
        if (k < 2 || k >= 16) {
#pragma unroll
            for (int r = 0; r < 4; ++r) {
                const int dq = q - (16 * kt + 4 * g + r);
                a[r] = (dq >= 0 && dq < 256) ? a[r] : NEG;
            }
        }
        sc[k] = kt >= 0 ? a : f32x4{NEG, NEG, NEG, NEG};
    }
    FENCE();
    v4u vf[18];
#pragma unroll
    for (int k = 0; k < 18; ++k) { const int b = max(s - 8 + (k >> 1), 0); vf[k] = ldc16(rs, l * 16, (int)OFF_VF + ((b * 16 + h) * 2 + (k & 1)) * 1024); }
    float m = NEG;
#pragma unroll
    for (int k = 0; k < 18; ++k) m = fmaxf(m, fmaxf(fmaxf(sc[k][0], sc[k][1]), fmaxf(sc[k][2], sc[k][3])));
    m = xg_max(m);
    float sum = 0.f;
    f32x4 o0 = z4, o1 = z4;
#pragma unroll
    for (int blk = 0; blk < 9; ++blk) {
        f32x4 e0, e1;
#pragma unroll
        for (int r = 0; r < 4; ++r) { e0[r] = ex2(sc[2 * blk][r] - m); e1[r] = ex2(sc[2 * blk + 1][r] - m); }
        sum += ((e0[0] + e0[1]) + (e0[2] + e0[3])) + ((e1[0] + e1[1]) + (e1[2] + e1[3]));
        const v4u pb = pack8(e0, e1);
        o0 = mfma32(vf[2 * blk], pb, o0);
        o1 = mfma32(vf[2 * blk + 1], pb, o1);
    }
    sum = xg_sum(sum);
    const float f = ((const float*)(p.ws + OFF_GATE))[q] / sum;
    stc16(pack8(o0 * f, o1 * f), rs, l * 16, (int)OFF_OF + (rt * 16 + h) * 1024);
    asm volatile("s_waitcnt vmcnt(0)" ::: "memory");
    if (l == 0) __hip_atomic_fetch_add((GAS unsigned*)(p.ws + CT_OFC + s * 128), 1u, __ATOMIC_RELAXED, __HIP_MEMORY_SCOPE_AGENT);
}

__device__ __forceinline__ void out_item(const Params& p, LAS unsigned char* L, const __amdgpu_buffer_rsrc_t rs, const int s, const int q4, const int w, unsigned& mspin) {
    const int l = threadIdx.x & 63, n = l & 15, g = l >> 4;
    const f32x4 z4 = {0.f, 0.f, 0.f, 0.f};
    const float NEG = -3.0e38f;
    const int v = 7 * q4 + (w < 7 ? w : 0);
    f32x4 sxx[2][3];
#pragma unroll
    for (int a = 0; a < 2; ++a) { sxx[a][0] = z4; sxx[a][1] = z4; sxx[a][2] = z4; }
    {
        const int gp = (int)OFF_GF + s * (int)SEGB + (2 * w) * 28 * 1024;
        const unsigned char* xr = p.ws + OFF_XP + (size_t)s * 28672 + l * 16;
#pragma unroll 1
        for (int kh = 0; kh < 2; ++kh) {
            v4u x0[7], x1[7], kk[4][7];
#pragma unroll
            for (int j = 0; j < 7; ++j) {
                const int ks = 7 * kh + j;
                x0[j] = ldg16(xr + ks * 1024); x1[j] = ldg16(xr + (14 + ks) * 1024);
#pragma unroll
                for (int t = 0; t < 4; ++t) kk[t][j] = ldc16(rs, l * 16, gp + (t * 14 + ks) * 1024);
            }
            FENCE();
#pragma unroll
            for (int j = 0; j < 7; ++j) {
                const v4u xa = x0[j], xb = x1[j];
#pragma unroll
                for (int a = 0; a < 2; ++a) {
                    sxx[a][0] = mfma32(kk[2 * a][j], xa, sxx[a][0]);
                    sxx[a][1] = mfma32(kk[2 * a][j], xb, sxx[a][1]);
                    sxx[a][2] = mfma32(kk[2 * a + 1][j], xb, sxx[a][2]);
                }
            }
            FENCE();
        }
    }
    if (w == 0) {
        for (;;) {
            asm volatile("" ::: "memory");
            const unsigned c = __builtin_amdgcn_raw_buffer_load_b32(rs, CT_OFC + s * 128, 0, 16);
            if (__builtin_amdgcn_readfirstlane(c) >= 32u || ++mspin >= SPIN_CAP) break;
            __builtin_amdgcn_s_sleep(4);
        }
    }
    BAR();
    f32x4 o0 = z4, o1 = z4;
    {
        const unsigned char* wp = p.ws + OFF_WOF + (size_t)(v * 16) * 1024 + l * 16;
#pragma unroll 1
        for (int hb = 0; hb < 16; hb += 8) {
            v4u wf[8], oa[8], ob[8];
#pragma unroll
            for (int h = 0; h < 8; ++h) {
                wf[h] = ldg16(wp + (hb + h) * 1024);
                oa[h] = ldc16(rs, l * 16, (int)OFF_OF + (2 * s * 16 + hb + h) * 1024);
                ob[h] = ldc16(rs, l * 16, (int)OFF_OF + ((2 * s + 1) * 16 + hb + h) * 1024);
            }
            FENCE();
#pragma unroll
            for (int h = 0; h < 8; ++h) { o0 = mfma32(wf[h], oa[h], o0); o1 = mfma32(wf[h], ob[h], o1); }
            FENCE();
        }
    }
    v4u uf[16];
#pragma unroll
    for (int h = 0; h < 16; ++h) uf[h] = ldc16(rs, l * 16, (int)OFF_UF + s * (int)SEGB + (v * 16 + h) * 1024);
    float gate0 = 1.0f - ((const float*)(p.ws + OFF_GATE))[32 * s + n], gate1 = 1.0f - ((const float*)(p.ws + OFF_GATE))[32 * s + 16 + n];
    float mxx[2][2], lxx[2][2];
#pragma unroll
    for (int a = 0; a < 2; ++a) {
#pragma unroll
        for (int r = 0; r < 4; ++r) { sxx[a][0][r] = (4 * g + r <= n) ? sxx[a][0][r] : NEG; sxx[a][2][r] = (4 * g + r <= n) ? sxx[a][2][r] : NEG; }
        mxx[a][0] = xg_max(fmaxf(fmaxf(sxx[a][0][0], sxx[a][0][1]), fmaxf(sxx[a][0][2], sxx[a][0][3])));
        mxx[a][1] = xg_max(fmaxf(fmaxf(fmaxf(sxx[a][1][0], sxx[a][1][1]), fmaxf(sxx[a][1][2], sxx[a][1][3])), fmaxf(fmaxf(sxx[a][2][0], sxx[a][2][1]), fmaxf(sxx[a][2][2], sxx[a][2][3]))));
#pragma unroll
        for (int r = 0; r < 4; ++r) {
            sxx[a][0][r] = ex2((sxx[a][0][r] - mxx[a][0]) * LK); sxx[a][1][r] = ex2((sxx[a][1][r] - mxx[a][1]) * LK); sxx[a][2][r] = ex2((sxx[a][2][r] - mxx[a][1]) * LK);
        }
        lxx[a][0] = (sxx[a][0][0] + sxx[a][0][1]) + (sxx[a][0][2] + sxx[a][0][3]);
        lxx[a][1] = ((sxx[a][1][0] + sxx[a][1][1]) + (sxx[a][1][2] + sxx[a][1][3])) + ((sxx[a][2][0] + sxx[a][2][1]) + (sxx[a][2][2] + sxx[a][2][3]));
    }
#pragma unroll
    for (int h = 0; h < 16; ++h) asm volatile("" : "+v"(uf[h]));
#pragma unroll
    for (int a = 0; a < 2; ++a)
        asm volatile("" : "+v"(sxx[a][0]), "+v"(sxx[a][1]), "+v"(sxx[a][2]), "+v"(mxx[a][0]), "+v"(mxx[a][1]), "+v"(lxx[a][0]), "+v"(lxx[a][1]));
    asm volatile("" : "+v"(gate0), "+v"(gate1), "+v"(o0), "+v"(o1));
    FENCE();
    if (s + 4 < NSEG) {
        for (;;) {
            asm volatile("" ::: "memory");
            const unsigned x = __builtin_amdgcn_raw_buffer_load_b32(rs, 0, (int)OFF_SXM + ((s + 3) * 32 + 4 * w) * 1024, 16);
            if (__builtin_amdgcn_readfirstlane(x) != 0xFFFFFFFFu || ++mspin >= SPIN_CAP) break;
            __builtin_amdgcn_s_sleep(6);
        }
    } else {
        for (;;) {
            asm volatile("" ::: "memory");
            const unsigned x = __builtin_amdgcn_raw_buffer_load_b32(rs, 0, (int)OFF_SXM + ((s - 2) * 32 + 4 * w) * 1024, 16);
            if (__builtin_amdgcn_readfirstlane(x) != 0xFFFFFFFFu || ++mspin >= SPIN_CAP) break;
            __builtin_amdgcn_s_sleep(6);
        }
    }
    asm volatile("" ::: "memory");
    v4u sm[4];
    v2u yf[8];
    for (;;) {
        asm volatile("" ::: "memory");
#pragma unroll
        for (int k = 0; k < 4; ++k) sm[k] = ldc16(rs, l * 16, (int)OFF_SXM + (s * 32 + 4 * w + k) * 1024);
        if (fresh(__builtin_elementwise_max(__builtin_elementwise_max(sm[0], sm[1]), __builtin_elementwise_max(sm[2], sm[3]))) || ++mspin >= SPIN_CAP) break;
    }
    FENCE();
    unsigned px[2][2][4]; unsigned pm[2][2];
#pragma unroll
    for (int qt = 0; qt < 2; ++qt) {
#pragma unroll
        for (int a = 0; a < 2; ++a) {
            const unsigned u00 = sm[2 * a][2 * qt], u01 = sm[2 * a + 1][2 * qt], u10 = sm[2 * a][2 * qt + 1], u11 = sm[2 * a + 1][2 * qt + 1];
            const float t0 = bitsf(u00) + bitsf(u01), t1 = bitsf(u10) + bitsf(u11);
            const float m = xg_max_raw(fmaxf(mxx[a][qt], fmaxf(t0, t1)));
            const float e0 = ex2((t0 - m) * LK), e1 = ex2((t1 - m) * LK), be = ex2((mxx[a][qt] - m) * LK);
            const float f = (qt == 0 ? gate0 : gate1) * __builtin_amdgcn_rcpf(xg_sum((e0 + e1) + be * lxx[a][qt]));
            const float fx = f * be;
            if (qt == 0) { px[a][0][0] = pack2(sxx[a][0][0] * fx, sxx[a][0][1] * fx); px[a][0][1] = pack2(sxx[a][0][2] * fx, sxx[a][0][3] * fx); }
            else {
                px[a][1][0] = pack2(sxx[a][1][0] * fx, sxx[a][1][1] * fx); px[a][1][1] = pack2(sxx[a][1][2] * fx, sxx[a][1][3] * fx);
                px[a][1][2] = pack2(sxx[a][2][0] * fx, sxx[a][2][1] * fx); px[a][1][3] = pack2(sxx[a][2][2] * fx, sxx[a][2][3] * fx);
            }
            pm[qt][a] = pack2(e0 * f, e1 * f);
        }
    }
    *(LAS v4u*)(L + w * 4096 + l * 16) = v4u{px[0][0][0], px[0][0][1], px[1][0][0], px[1][0][1]};
    *(LAS v4u*)(L + w * 4096 + 1024 + l * 16) = v4u{px[0][1][0], px[0][1][1], px[0][1][2], px[0][1][3]};
    *(LAS v4u*)(L + w * 4096 + 2048 + l * 16) = v4u{px[1][1][0], px[1][1][1], px[1][1][2], px[1][1][3]};
    *(LAS v4u*)(L + w * 4096 + 3072 + l * 16) = v4u{pm[0][0], pm[0][1], pm[1][0], pm[1][1]};
    BAR();
    if (w < 7) {
#pragma unroll
        for (int k = 0; k < 8; ++k) yf[k] = __builtin_amdgcn_raw_buffer_load_b64(rs, l * 8, (int)OFF_YM + (s * 224 + v * 8 + k) * 512, 16);
        v4u pmm[8];
#pragma unroll
        for (int pr = 0; pr < 8; ++pr) {
            const v4u u0 = uf[2 * pr], u1 = uf[2 * pr + 1];
            const v4u pa = *(const LAS v4u*)(L + pr * 4096 + l * 16), pb = *(const LAS v4u*)(L + pr * 4096 + 1024 + l * 16),
                      pc = *(const LAS v4u*)(L + pr * 4096 + 2048 + l * 16);
            pmm[pr] = *(const LAS v4u*)(L + pr * 4096 + 3072 + l * 16);
            o1 = mfma32(u0, pb, o1);
            o0 = mfma32(v4u{u0.x, u0.y, u1.x, u1.y}, pa, o0);
            o1 = mfma32(u1, pc, o1);
        }
        FENCE();
        for (;;) {
            unsigned mm = 0u;
#pragma unroll
            for (int k = 0; k < 8; ++k) mm = max(mm, max(yf[k].x, yf[k].y));
            if (__all(mm != 0xFFFFFFFFu) || ++mspin >= SPIN_CAP) break;
            asm volatile("" ::: "memory");
            yf[0] = __builtin_amdgcn_raw_buffer_load_b64(rs, l * 8, (int)OFF_YM + (s * 224 + v * 8 + 0) * 512, 16);
            yf[1] = __builtin_amdgcn_raw_buffer_load_b64(rs, l * 8, (int)OFF_YM + (s * 224 + v * 8 + 1) * 512, 16);
            yf[2] = __builtin_amdgcn_raw_buffer_load_b64(rs, l * 8, (int)OFF_YM + (s * 224 + v * 8 + 2) * 512, 16);
            yf[3] = __builtin_amdgcn_raw_buffer_load_b64(rs, l * 8, (int)OFF_YM + (s * 224 + v * 8 + 3) * 512, 16);
            yf[4] = __builtin_amdgcn_raw_buffer_load_b64(rs, l * 8, (int)OFF_YM + (s * 224 + v * 8 + 4) * 512, 16);
            yf[5] = __builtin_amdgcn_raw_buffer_load_b64(rs, l * 8, (int)OFF_YM + (s * 224 + v * 8 + 5) * 512, 16);
            yf[6] = __builtin_amdgcn_raw_buffer_load_b64(rs, l * 8, (int)OFF_YM + (s * 224 + v * 8 + 6) * 512, 16);
            yf[7] = __builtin_amdgcn_raw_buffer_load_b64(rs, l * 8, (int)OFF_YM + (s * 224 + v * 8 + 7) * 512, 16);
        }
#pragma unroll
        for (int pp = 0; pp < 4; ++pp) {
            const v4u yy = v4u{yf[2 * pp].x, yf[2 * pp].y, yf[2 * pp + 1].x, yf[2 * pp + 1].y};
            o0 = mfma32(yy, v4u{pmm[2 * pp].x, pmm[2 * pp].y, pmm[2 * pp + 1].x, pmm[2 * pp + 1].y}, o0);
            o1 = mfma32(yy, v4u{pmm[2 * pp].z, pmm[2 * pp].w, pmm[2 * pp + 1].z, pmm[2 * pp + 1].w}, o1);
        }
        stg8(p.out + (size_t)(32 * s + n) * 448 + 16 * v + 4 * g, v2u{pack2(o0[0], o0[1]), pack2(o0[2], o0[3])});
        stg8(p.out + (size_t)(32 * s + 16 + n) * 448 + 16 * v + 4 * g, v2u{pack2(o1[0], o1[1]), pack2(o1[2], o1[3])});
    }
}

__global__ void __launch_bounds__(512, 2) k_main(Params p) {
    extern __shared__ __attribute__((aligned(16))) unsigned char lds_[];
    p.ws = align_ws(p.ws);
    const int rot = (int)((cptr)(p.ws + OFF_CTL))[8];
    const int grp = (blockIdx.x + rot) & 7, u = blockIdx.x >> 3;
    LAS unsigned char* L = (LAS unsigned char*)lds_;
    const int w = __builtin_amdgcn_readfirstlane(threadIdx.x >> 6);
    if (grp == 0) {
        if (w == 0) score_wave<1, 0>(p, L, u, w); else if (w < 3) score_wave<0, 0>(p, L, u, w); else if (w == 3) score_wave<0, DLY_3>(p, L, u, w);
        else if (w < 7) score_wave<0, DLY_4>(p, L, u, w); else score_wave<2, 0>(p, L, u, w);
        return;
    }
    if (grp == 1) {
        if (u < 28) { if (w < W3Y) value_wave<false, false>(p, L, u, w); else if (w < 7) value_wave<false, true>(p, L, u, w); else value_wave<true, LY>(p, L, u, w); }
        else prefetch_wave(p, u - 28, w);
        return;
    }
    const __amdgpu_buffer_rsrc_t rs = __builtin_amdgcn_make_buffer_rsrc(p.ws, 0, (int)WS_END, 0x00020000);
    const int k = (grp - 2) * 32 + u;
    if (k < 96) {
        if (k % 3 == 0) { serve(p, L, rs, k / 3, w); return; }
        sweep<true>(p, L, rs, k / 3, k % 3 - 1, w);
    } else if (k < 186) sweep<false>(p, L, rs, k / 3, k % 3, w);
    else {
        const unsigned parmask = load_parmask(p.ws);
        const int l = threadIdx.x & 63, wi = (k - 186) * 8 + w;
        const v4u ff = v4u{0xFFFFFFFFu, 0xFFFFFFFFu, 0xFFFFFFFFu, 0xFFFFFFFFu};
        __builtin_amdgcn_s_sleep(127); __builtin_amdgcn_s_sleep(127);
        for (int pg = 1; pg < NPAGE; ++pg) {
            const int base = hop_off(pg * SPP, parmask);
            for (int i = wi; i < SPP * NCHUNK * 8; i += 48) { stc16(ff, rs, l * 16, base + (i >> 3) * CH + (i & 7) * 1024); }
            for (int i = wi; i < SPP * 32 && pg * SPP * 32 + i < NSEG * 32; i += 48) stc16(ff, rs, l * 16, (int)OFF_SXM + (pg * SPP * 32 + i) * 1024);
            asm volatile("s_waitcnt vmcnt(0)" ::: "memory");
            BAR();
            if (threadIdx.x == 0) __hip_atomic_fetch_add((GAS unsigned*)(p.ws + CT_PZ), 1u << (4 * (pg - 1)), __ATOMIC_RELAXED, __HIP_MEMORY_SCOPE_AGENT);
        }
        if (threadIdx.x == 0) __hip_atomic_fetch_add((GAS unsigned*)(p.ws + CT_DONE), 1u, __ATOMIC_RELAXED, __HIP_MEMORY_SCOPE_AGENT);
    }
    const int wk = k < 96 ? (k / 3) * 2 + (k % 3 - 1) : k - 32;
    unsigned mspin = 0;
    if (wk >= 64) {
        int rq = 0, rk = 0, rv = 0;
        for (int i = 0; i < ((wk - 64) * STAGA) >> 4; ++i) __builtin_amdgcn_s_sleep(64);
        for (int it = (wk - 64) * 8 + w; it < 2048; it += 768) {
            const int s = it >> 5, h = it & 15;
            if (rq <= s) rq = wait_ready<1, 2, 32>(rs, h, s, mspin);
            if (rk <= s) rk = wait_ready<1, 2, 32>(rs, 16 + h, s, mspin);
            if (rv <= s) rv = wait_ready<1, 3, 32>(rs, 60 + (h >> 3), s, mspin);
            attn_item(p, rs, it);
        }
    }
    if (w == 0) {
        for (;;) {
            asm volatile("" ::: "memory");
            const unsigned c = __builtin_amdgcn_raw_buffer_load_b32(rs, CT_DONE, 0, 16);
            if (__builtin_amdgcn_readfirstlane(c) >= 160u || ++mspin >= SPIN_CAP) break;
            __builtin_amdgcn_s_sleep(16);
        }
    }
    BAR();
    for (int i = 0; i < (wk * STAG) >> 4; ++i) __builtin_amdgcn_s_sleep(64);
    for (int it = wk; it < 256; it += 160) { out_item(p, L, rs, it >> 2, it & 3, w, mspin); }
}

extern "C" void kernel_launch(void* const* d_in, const int* in_sizes, int n_in, void* d_out, int out_size, void* d_ws, size_t ws_size,
                              hipStream_t stream) {
    Params p;
    p.x = (const u16*)d_in[0]; p.wqkv = (const u16*)d_in[1]; p.wo = (const u16*)d_in[2]; p.mem0 = (const u16*)d_in[4];
    p.Wq = (const u16*)d_in[5]; p.Wk = (const u16*)d_in[6]; p.Wv = (const u16*)d_in[7]; p.Wo = (const u16*)d_in[8];
    p.gate_w = (const u16*)d_in[10];
    p.out = (u16*)d_out; p.ws = (unsigned char*)d_ws;
    (void)hipFuncSetAttribute((const void*)k_prep, hipFuncAttributeMaxDynamicSharedMemorySize, K1_LDS);
    (void)hipFuncSetAttribute((const void*)k_main, hipFuncAttributeMaxDynamicSharedMemorySize, L_REC_END);
    k_prep<<<256, 512, K1_LDS, stream>>>(p);
    k_main<<<256, 512, L_REC_END, stream>>>(p);
}
```
